```python
import torch

def _softmax(x, axis=-1):
    return torch.softmax(x, dim=axis)
def _gelu(x):
    return torch.nn.functional.gelu(x, approximate="tanh")
def _silu(x):
    return torch.nn.functional.silu(x)
def _sigmoid(x):
    return torch.sigmoid(x)
def _layernorm(x, axis=-1, eps=1e-5):
    m = x.mean(dim=axis, keepdim=True)
    v = x.var(dim=axis, keepdim=True, unbiased=False)
    return (x - m) / torch.sqrt(v + eps)
def _scatter_add(tgt, idx, src, axis=0):
    return tgt.clone().index_add_(axis, idx, src)
def _segment_sum(src, seg, n):
    out = torch.zeros((n,) + src.shape[1:], dtype=src.dtype)
    return out.index_add_(0, seg, src)
def _pad(x, pw):
    flat = tuple(v for pair in reversed(pw) for v in pair)
    return torch.nn.functional.pad(x, flat)

kernel_name = 'concat_heads__mha_block__compressed_kv_cla__prompt'

def setup_inputs():

    _w = (lambda a: (lambda t: t.to(torch.bfloat16) if t.is_floating_point() else t)(torch.as_tensor(a)))
    def _A_setup():
        B, S, D, H = 1, 2048, 640, 32
        return {
            "x": _w((lambda *s: torch.randn(*s, dtype=torch.bfloat16) * 0.2)(B, S, D)),
            "wqkv": _w((lambda *s: torch.randn(*s, dtype=torch.bfloat16) * 0.2)(D, 3 * D)),
            "wo": _w((lambda *s: torch.randn(*s, dtype=torch.bfloat16) * 0.05)(D, D)),
        }
    _A_in = _A_setup()

    def _B_setup():
        B, H, Sq, Sk, D, M = 1, 32, 2048, 2048, 128, 640
        Dc = 4 * D
        return {
            "x": _w((lambda *s: torch.randn(*s, dtype=torch.bfloat16) * 0.4)(B, Sq, M)),
            "c_kv": _w((lambda *s: torch.randn(*s, dtype=torch.bfloat16) * 0.4)(B, Sk, Dc)),
            "w_q": _w((lambda *s: torch.randn(*s, dtype=torch.bfloat16) * 3.2)(M, H, D) * (M ** -0.5)),
            "w_uk": _w((lambda *s: torch.randn(*s, dtype=torch.bfloat16) * 3.2)(Dc, H, D) * (Dc ** -0.5)),
            "w_uv": _w((lambda *s: torch.randn(*s, dtype=torch.bfloat16) * 0.4)(Dc, H, D) * (Dc ** -0.5)),
            "w_o": _w((lambda *s: torch.randn(*s, dtype=torch.bfloat16) * 0.4)(H, D, M) * ((H * D) ** -0.5)),
        }
    _B_in = _B_setup()

    return {
        "A_x": _A_in["x"],
        "A_wqkv": _A_in["wqkv"],
        "A_wo": _A_in["wo"],
        "B_x": _B_in["x"],
        "B_c_kv": _B_in["c_kv"],
        "B_w_q": _B_in["w_q"],
        "B_w_uk": _B_in["w_uk"],
        "B_w_uv": _B_in["w_uv"],
        "B_w_o": _B_in["w_o"],
    }

def reference(A_x, A_wqkv, A_wo, B_x, B_c_kv, B_w_q, B_w_uk, B_w_uv, B_w_o):
    def _A_ref(x, wqkv, wo):
        B, S, D = x.shape
        H = 32
        Dh = D // H
        qkv = (lambda x, *d: x.permute(*d))((x @ wqkv).reshape(B, S, 3, H, Dh), 2, 0, 3, 1, 4)
        q, k, v = qkv[0], qkv[1], qkv[2]
        scores = torch.einsum("bhqd,bhkd->bhqk", q, k) * (Dh ** -0.5)
        p = _softmax(scores, axis=-1)
        o = torch.einsum("bhqk,bhkd->bhqd", p, v)
        return (lambda x, *d: x.permute(*d))(o, 0, 2, 1, 3).reshape(B, S, D) @ wo

    def _B_ref(x, c_kv, w_q, w_uk, w_uv, w_o):
        B, Sq, M = x.shape[0], x.shape[1], x.shape[2]
        Sk = c_kv.shape[1]
        H, D = w_q.shape[1], w_q.shape[2]
        q = torch.einsum("bqm,mhd->bhqd", x, w_q)
        k = torch.einsum("bkc,chd->bhkd", c_kv, w_uk)
        v = torch.einsum("bkc,chd->bhkd", c_kv, w_uv)
        scores = torch.einsum("bhqd,bhkd->bhqk", q, k) * (D ** -0.5)
        off = Sk - Sq
        qi = torch.arange(Sq)[:, None]
        kj = torch.arange(Sk)[None, :]
        cmask = kj <= (qi + off)
        scores = torch.where(cmask[None, None], scores, -1e9)
        p = _softmax(scores, axis=-1)
        ctx = torch.einsum("bhqk,bhkd->bhqd", p, v)
        return torch.einsum("bhqd,hdm->bqm", ctx, w_o)

    a = _A_ref(A_x, A_wqkv, A_wo)
    b = _B_ref(B_x, B_c_kv, B_w_q, B_w_uk, B_w_uv, B_w_o)
    return torch.concatenate([a, b], axis=-1)
```

```cpp
#include <hip/hip_runtime.h>
#include <hip/hip_bf16.h>
#include <stdint.h>

typedef short s16x4 __attribute__((ext_vector_type(4)));
typedef short bf16x8 __attribute__((ext_vector_type(8)));
typedef float f32x16 __attribute__((ext_vector_type(16)));
typedef float f32x4 __attribute__((ext_vector_type(4)));
typedef float f32x2 __attribute__((ext_vector_type(2)));
typedef unsigned u32x4 __attribute__((ext_vector_type(4)));
typedef unsigned u32x2 __attribute__((ext_vector_type(2)));
typedef __bf16 bf16x2_t __attribute__((ext_vector_type(2)));
#define LAS __attribute__((address_space(3)))
#define DI __device__ __forceinline__
#define MFMA32(a, b, c) __builtin_amdgcn_mfma_f32_32x32x16_bf16((a), (b), (c), 0, 0, 0)
#define CFENCE() asm volatile("" ::: "memory")
#define BARRIER() do { CFENCE(); __builtin_amdgcn_s_barrier(); CFENCE(); } while (0)
#define WAIT_VM(n) asm volatile("s_waitcnt vmcnt(" #n ")" ::: "memory")

DI unsigned pack2(float a, float b) {
    f32x2 v = {a, b};
    bf16x2_t r = __builtin_convertvector(v, bf16x2_t);
    return __builtin_bit_cast(unsigned, r);
}
DI u32x4 pack8(const f32x16& x, int s) {
    u32x4 w;
    w[0] = pack2(x[8 * s + 0], x[8 * s + 1]);
    w[1] = pack2(x[8 * s + 2], x[8 * s + 3]);
    w[2] = pack2(x[8 * s + 4], x[8 * s + 5]);
    w[3] = pack2(x[8 * s + 6], x[8 * s + 7]);
    return w;
}
DI u32x4 pack8s(const f32x16& x, int s, float c) {
    u32x4 w;
    w[0] = pack2(x[8 * s + 0] * c, x[8 * s + 1] * c);
    w[1] = pack2(x[8 * s + 2] * c, x[8 * s + 3] * c);
    w[2] = pack2(x[8 * s + 4] * c, x[8 * s + 5] * c);
    w[3] = pack2(x[8 * s + 6] * c, x[8 * s + 7] * c);
    return w;
}
DI void glds16(const void* g, LAS char* l) {
    __builtin_amdgcn_global_load_lds((const unsigned*)g, (LAS unsigned*)l, 16, 0, 0);
}
DI bf16x8 tr_frag(LAS char* p0, LAS char* p1) {
    s16x4 lo = __builtin_amdgcn_ds_read_tr16_b64_v4i16((LAS s16x4*)p0);
    s16x4 hi = __builtin_amdgcn_ds_read_tr16_b64_v4i16((LAS s16x4*)p1);
    return __builtin_shufflevector(lo, hi, 0, 1, 2, 3, 4, 5, 6, 7);
}

constexpr size_t WS_KVB = 0;
constexpr size_t WS_QB = WS_KVB + (size_t)32 * 1048576;
constexpr size_t WS_KVA = WS_QB + (size_t)16 * 1048576;
constexpr size_t WS_QA = WS_KVA + (size_t)8 * 1048576;
constexpr size_t WS_CTXB = WS_QA + (size_t)4 * 1048576;
constexpr size_t WS_CTXA = WS_CTXB + (size_t)16 * 1048576;
constexpr size_t WS_PART = WS_CTXA + (size_t)4 * 1048576;
constexpr int NSPLIT = 5;
constexpr size_t SLAB = (size_t)64 * 20 * 4096;

constexpr float CB = 0.08838834764831845f * 1.4426950408889634f;
constexpr float CA = 0.22360679774997896f * 1.4426950408889634f;

constexpr int G1_LDS = 2 * 65536;
enum { EP_BK = 0, EP_BV = 1, EP_BQ = 2, EP_AQK = 3, EP_AV = 4 };

template <int EP>
DI void g1_body(const __hip_bfloat16* __restrict__ X, const __hip_bfloat16* __restrict__ W, int K, int ldw,
                int m0, int n0, int nlim, int nsec, char* __restrict__ ws, LAS char* lds) {
    constexpr bool TR = (EP == EP_BK || EP == EP_BQ || EP == EP_AQK);
    const int tid = threadIdx.x, lane = tid & 63, wid = __builtin_amdgcn_readfirstlane(tid >> 6);
    const int wm = wid >> 2, wn = wid & 3, r = lane & 31, h = lane >> 5;
    const int nkt = K >> 6;

    unsigned xoff[4], woff[4];
#pragma unroll
    for (int i = 0; i < 4; ++i) {
        const int row = (wid * 4 + i) * 8 + (lane >> 3);
        const int c = (lane & 7) ^ ((row >> 1) & 7);
        xoff[i] = (unsigned)(((m0 + row) * K + c * 8) * 2);
        const int k = (wid * 4 + i) * 2 + (lane >> 5);
        const int p = lane & 31, b = (p >> 2) ^ (k & 3);
        int col = n0 + (b * 4 + (p & 3)) * 8;
        col = col < nlim ? col : nlim - 8;
        woff[i] = (unsigned)((k * ldw + col) * 2);
    }
    const char* Xb = (const char*)X;
    const char* Wb = (const char*)W;
    const unsigned wstep = (unsigned)(64 * ldw * 2);
    auto stage = [&](int kt, int buf) {
        LAS char* xb = lds + buf * 65536 + wid * 4096;
        LAS char* wb = lds + buf * 65536 + 32768 + wid * 4096;
#pragma unroll
        for (int i = 0; i < 4; ++i) glds16(Xb + xoff[i] + (unsigned)kt * 128u, xb + i * 1024);
#pragma unroll
        for (int i = 0; i < 4; ++i) glds16(Wb + woff[i] + (unsigned)kt * wstep, wb + i * 1024);
    };

    const int xsw = (r >> 1) & 7;
    const int xbase = (wm * 128 + r) * 128;
    const int q = (lane >> 2) & 3, p4 = lane & 3, g16 = (lane >> 4) & 1;
    int wbase[2];
#pragma unroll
    for (int ni = 0; ni < 2; ++ni) {
        const int b = wn * 2 + ni;
        wbase[ni] = 32768 + (8 * h + q) * 512 + ((b ^ q) * 64) + g16 * 32 + p4 * 8;
    }

    f32x16 acc[4][2];
#pragma unroll
    for (int mi = 0; mi < 4; ++mi)
#pragma unroll
        for (int ni = 0; ni < 2; ++ni) acc[mi][ni] = f32x16{};

    stage(0, 0);
    for (int kt = 0; kt < nkt; ++kt) {
        const int buf = kt & 1;
        WAIT_VM(0);
        BARRIER();
        if (kt + 1 < nkt) stage(kt + 1, buf ^ 1);
        LAS char* bb = lds + buf * 65536;
#pragma unroll
        for (int ks = 0; ks < 4; ++ks) {
            bf16x8 xf[4], wf[2];
            const int xc = ((2 * ks + h) ^ xsw) * 16;
#pragma unroll
            for (int mi = 0; mi < 4; ++mi) xf[mi] = *(LAS bf16x8*)(bb + xbase + xc + mi * 4096);
#pragma unroll
            for (int ni = 0; ni < 2; ++ni) wf[ni] = tr_frag(bb + wbase[ni] + ks * 8192, bb + wbase[ni] + ks * 8192 + 2048);
#pragma unroll
            for (int mi = 0; mi < 4; ++mi)
#pragma unroll
                for (int ni = 0; ni < 2; ++ni)
                    acc[mi][ni] = TR ? MFMA32(wf[ni], xf[mi], acc[mi][ni]) : MFMA32(xf[mi], wf[ni], acc[mi][ni]);
        }
    }

#pragma unroll
    for (int mi = 0; mi < 4; ++mi) {
        const int tb = (m0 + wm * 128 + mi * 32) >> 5;
        const int tile = tb >> 1, kb = tb & 1;
#pragma unroll
        for (int ni = 0; ni < 2; ++ni) {
            const int nb = n0 + wn * 64 + ni * 32;
            if (nb >= nlim) continue;
            if constexpr (EP == EP_BK) {
                const int head = nb >> 7, db = (nb >> 5) & 3;
                char* dst = ws + WS_KVB + (size_t)head * 1048576 + tile * 32768 + (kb * 8 + db * 2) * 1024 + lane * 16;
                *(u32x4*)(dst) = pack8(acc[mi][ni], 0);
                *(u32x4*)(dst + 1024) = pack8(acc[mi][ni], 1);
            } else if constexpr (EP == EP_BV) {
                const int head = nb >> 7, db = (nb >> 5) & 3;
                char* dst = ws + WS_KVB + (size_t)head * 1048576 + tile * 32768 + 16384 + (kb * 8 + db) * 1024 + lane * 16;
                *(u32x4*)(dst) = pack8(acc[mi][ni], 0);
                *(u32x4*)(dst + 4096) = pack8(acc[mi][ni], 1);
            } else if constexpr (EP == EP_BQ) {
                const int head = nb >> 7, db = (nb >> 5) & 3;
                char* dst = ws + WS_QB + (size_t)((head * 64 + tb) * 8 + db * 2) * 1024 + lane * 16;
                *(u32x4*)(dst) = pack8s(acc[mi][ni], 0, CB);
                *(u32x4*)(dst + 1024) = pack8s(acc[mi][ni], 1, CB);
            } else if constexpr (EP == EP_AQK) {
                const bool isq = nb < 640;
                const float sc = isq ? CA : 1.0f;
#pragma unroll
                for (int g = 0; g < 4; ++g) {
                    const int n = nb - (isq ? 0 : 640) + 8 * g + 4 * h;
                    const int head = n / 20, d0 = n - head * 20;
                    char* base = isq ? ws + WS_QA + (size_t)((head * 64 + tb) * 2) * 1024
                                     : ws + WS_KVA + (size_t)head * 262144 + tile * 8192 + kb * 2048;
                    char* dst = base + (d0 >> 4) * 1024 + (r + 32 * ((d0 >> 3) & 1)) * 16 + (d0 & 4) * 2;
                    u32x2 w;
                    w[0] = pack2(acc[mi][ni][4 * g + 0] * sc, acc[mi][ni][4 * g + 1] * sc);
                    w[1] = pack2(acc[mi][ni][4 * g + 2] * sc, acc[mi][ni][4 * g + 3] * sc);
                    *(u32x2*)dst = w;
                    if (d0 == 16) {
                        *(u32x2*)(dst + 8) = (u32x2){0u, 0u};
                        *(u32x4*)(base + 1024 + (r + 32) * 16) = (u32x4){0u, 0u, 0u, 0u};
                    }
                }
            } else {
                const int n = nb - nsec + r;
                const int head = n / 20, d = n - head * 20;
                char* dst = ws + WS_KVA + (size_t)head * 262144 + tile * 8192 + 4096 + kb * 2048 + (d + 32 * h) * 16;
                *(u32x4*)(dst) = pack8(acc[mi][ni], 0);
                *(u32x4*)(dst + 1024) = pack8(acc[mi][ni], 1);
                if (d == 19) {
                    const u32x4 one = {0x3F803F80u, 0x3F803F80u, 0x3F803F80u, 0x3F803F80u};
                    *(u32x4*)(dst + 16) = one;
                    *(u32x4*)(dst + 1024 + 16) = one;
                }
            }
        }
    }
}

__global__ __launch_bounds__(512, 2) void k1_proj(const __hip_bfloat16* __restrict__ Ax, const __hip_bfloat16* __restrict__ Awqkv,
                                                  const __hip_bfloat16* __restrict__ Bx, const __hip_bfloat16* __restrict__ Bckv,
                                                  const __hip_bfloat16* __restrict__ Bwq, const __hip_bfloat16* __restrict__ Bwuk,
                                                  const __hip_bfloat16* __restrict__ Bwuv, char* __restrict__ ws) {
    extern __shared__ __attribute__((aligned(16))) char smem[];
    LAS char* lds = (LAS char*)smem;
    const int bid = blockIdx.x;
    const int mt = (bid >> 3) & 7;
    const int ct = (bid & 7) + 8 * (bid >> 6);
    const int m0 = mt * 256;
    if (ct < 16) g1_body<EP_BK>(Bckv, Bwuk, 512, 4096, m0, ct * 256, 4096, 0, ws, lds);
    else if (ct < 32) g1_body<EP_BV>(Bckv, Bwuv, 512, 4096, m0, (ct - 16) * 256, 4096, 0, ws, lds);
    else if (ct < 48) g1_body<EP_BQ>(Bx, Bwq, 640, 4096, m0, (ct - 32) * 256, 4096, 0, ws, lds);
    else if (ct < 53) g1_body<EP_AQK>(Ax, Awqkv, 640, 1920, m0, (ct - 48) * 256, 1280, 0, ws, lds);
    else g1_body<EP_AV>(Ax, Awqkv, 640, 1920, m0, 1280 + (ct - 53) * 256, 1920, 1280, ws, lds);
}

constexpr int SLOT = 40960;
constexpr int K2_LDS = 3 * SLOT;
constexpr int NIT = 34;

DI void k2_stage(const char* kvb, const char* kva, int tb, int ta, LAS char* slot, int wid, int lane) {
    const char* sb = kvb + (size_t)tb * 32768 + wid * 4096 + lane * 16;
#pragma unroll
    for (int i = 0; i < 4; ++i) glds16(sb + i * 1024, slot + wid * 4096 + i * 1024);
    glds16(kva + (size_t)ta * 8192 + wid * 1024 + lane * 16, slot + 32768 + wid * 1024);
}

__global__ __launch_bounds__(512, 2) void k2_attn(char* __restrict__ ws) {
    extern __shared__ __attribute__((aligned(16))) char smem[];
    LAS char* lds = (LAS char*)smem;
    const int tid = threadIdx.x, lane = tid & 63, wid = __builtin_amdgcn_readfirstlane(tid >> 6);
    const int r = lane & 31, h = lane >> 5;
    const int g = blockIdx.x, xcd = g & 7, idx = g >> 3;
    const int head = xcd * 4 + (idx >> 3), x = idx & 7;
    const char* kvb = ws + WS_KVB + (size_t)head * 1048576;
    const char* kva = ws + WS_KVA + (size_t)head * 262144;
    const int n1 = 32 - 2 * x;

    k2_stage(kvb, kva, 0, 0, lds, wid, lane);
    k2_stage(kvb, kva, 1, 1, lds + SLOT, wid, lane);

#define K2_SYNC(it)                                                                         \
    do {                                                                                    \
        if ((it) + 1 < NIT) { WAIT_VM(5); } else { WAIT_VM(0); }                            \
        BARRIER();                                                                          \
        if ((it) + 2 < NIT) {                                                               \
            const int t2 = (it) + 2;                                                        \
            const int tb2 = t2 < n1 ? t2 : t2 - n1;                                         \
            const int ta2 = t2 < 32 ? t2 : 31;                                              \
            k2_stage(kvb, kva, tb2, ta2, lds + (t2 % 3) * SLOT, wid, lane);                 \
        }                                                                                   \
    } while (0)

    if (wid < 4) {
        int it = 0;
        for (int blk = 0; blk < 2; ++blk) {
            const int qb = blk == 0 ? 15 - x : x;
            const int nt = 2 * qb + 2;
            const int q0 = qb * 128 + wid * 32;
            const int tb32 = q0 >> 5;
            bf16x8 qf[8];
            const char* qp = ws + WS_QB + (size_t)((head * 64 + tb32) * 8) * 1024 + lane * 16;
#pragma unroll
            for (int ks = 0; ks < 8; ++ks) qf[ks] = *(const bf16x8*)(qp + ks * 1024);
            f32x16 o[4];
#pragma unroll
            for (int db = 0; db < 4; ++db) o[db] = f32x16{};
            f32x16 negm = f32x16{};
            float l = 0.f;
            for (int t = 0; t < nt; ++t, ++it) {
                K2_SYNC(it);
                const int rel = (q0 - 64 * t) >> 5;
                if (rel < 0) continue;
                LAS char* sl = lds + (it % 3) * SLOT + lane * 16;
                f32x16 s0, s1;
#define B_QK()                                                                                          \
    do {                                                                                                \
        s0 = MFMA32(*(LAS bf16x8*)(sl), qf[0], negm);                                                   \
        s1 = MFMA32(*(LAS bf16x8*)(sl + 8192), qf[0], negm);                                            \
        _Pragma("unroll") for (int ks = 1; ks < 8; ++ks) {                                              \
            s0 = MFMA32(*(LAS bf16x8*)(sl + ks * 1024), qf[ks], s0);                                    \
            s1 = MFMA32(*(LAS bf16x8*)(sl + 8192 + ks * 1024), qf[ks], s1);                             \
        }                                                                                               \
        if (rel <= 1) {                                                                                 \
            const int lim = 32 * rel + r - 4 * h;                                                       \
            _Pragma("unroll") for (int i = 0; i < 16; ++i) {                                            \
                const int kr = (i & 3) + 8 * (i >> 2);                                                  \
                if (kr > lim) s0[i] = -__builtin_inff();                                                \
                if (kr + 32 > lim) s1[i] = -__builtin_inff();                                           \
            }                                                                                           \
        }                                                                                               \
    } while (0)
                B_QK();
                float ps = 0.f;
                if (t > 0) {
#pragma unroll
                    for (int i = 0; i < 16; ++i) { s0[i] = __builtin_amdgcn_exp2f(s0[i]); ps += s0[i]; }
#pragma unroll
                    for (int i = 0; i < 16; ++i) { s1[i] = __builtin_amdgcn_exp2f(s1[i]); ps += s1[i]; }
                }
                if (t == 0 || __any(!(ps <= 65536.f))) {
                    if (t > 0) B_QK();
                    float mx = s0[0];
#pragma unroll
                    for (int i = 1; i < 16; ++i) mx = fmaxf(mx, s0[i]);
#pragma unroll
                    for (int i = 0; i < 16; ++i) mx = fmaxf(mx, s1[i]);
                    auto rr = __builtin_amdgcn_permlane32_swap(__float_as_uint(mx), __float_as_uint(mx), false, false);
                    mx = fmaxf(__uint_as_float(rr[0]), __uint_as_float(rr[1]));
                    float delta = mx;
                    if (t > 0) {
                        delta = fmaxf(mx, 0.f);
                        const float alpha = __builtin_amdgcn_exp2f(-delta);
                        l *= alpha;
#pragma unroll
                        for (int db = 0; db < 4; ++db)
#pragma unroll
                            for (int i = 0; i < 16; ++i) o[db][i] *= alpha;
                    }
                    const float nm = negm[0] - delta;
#pragma unroll
                    for (int i = 0; i < 16; ++i) negm[i] = nm;
                    ps = 0.f;
#pragma unroll
                    for (int i = 0; i < 16; ++i) { s0[i] = __builtin_amdgcn_exp2f(s0[i] - delta); ps += s0[i]; }
#pragma unroll
                    for (int i = 0; i < 16; ++i) { s1[i] = __builtin_amdgcn_exp2f(s1[i] - delta); ps += s1[i]; }
                }
                l += ps;
                bf16x8 pf[4];
                pf[0] = __builtin_bit_cast(bf16x8, pack8(s0, 0));
                pf[1] = __builtin_bit_cast(bf16x8, pack8(s0, 1));
                pf[2] = __builtin_bit_cast(bf16x8, pack8(s1, 0));
                pf[3] = __builtin_bit_cast(bf16x8, pack8(s1, 1));
#pragma unroll
                for (int s4 = 0; s4 < 4; ++s4)
#pragma unroll
                    for (int db = 0; db < 4; ++db)
                        o[db] = MFMA32(*(LAS bf16x8*)(sl + 16384 + (s4 * 4 + db) * 1024), pf[s4], o[db]);
            }
            auto rr = __builtin_amdgcn_permlane32_swap(__float_as_uint(l), __float_as_uint(l), false, false);
            const float inv = 1.0f / (__uint_as_float(rr[0]) + __uint_as_float(rr[1]));
            char* cp = ws + WS_CTXB + (size_t)(tb32 * 256 + head * 8) * 1024 + lane * 16;
#pragma unroll
            for (int db = 0; db < 4; ++db) {
                *(u32x4*)(cp + (2 * db) * 1024) = pack8s(o[db], 0, inv);
                *(u32x4*)(cp + (2 * db + 1) * 1024) = pack8s(o[db], 1, inv);
            }
        }
#undef B_QK
    } else {
        const int wa = wid - 4;
        const int tb32 = x * 8 + wa * 2;
        bf16x8 qf[2][2];
        const char* qp = ws + WS_QA + (size_t)((head * 64 + tb32) * 2) * 1024 + lane * 16;
#pragma unroll
        for (int qt = 0; qt < 2; ++qt)
#pragma unroll
            for (int ks = 0; ks < 2; ++ks) qf[qt][ks] = *(const bf16x8*)(qp + (qt * 2 + ks) * 1024);
        f32x16 o[2], negm[2];
        o[0] = f32x16{}; o[1] = f32x16{}; negm[0] = f32x16{}; negm[1] = f32x16{};
        for (int it = 0; it < NIT; ++it) {
            K2_SYNC(it);
            if (it >= 32) continue;
            LAS char* sl = lds + (it % 3) * SLOT + 32768 + lane * 16;
#pragma unroll
            for (int kb = 0; kb < 2; ++kb) {
                const bf16x8 k0 = *(LAS bf16x8*)(sl + kb * 2048);
                const bf16x8 k1 = *(LAS bf16x8*)(sl + kb * 2048 + 1024);
                f32x16 s[2];
                u32x4 pf[2][2];
#pragma unroll
                for (int qt = 0; qt < 2; ++qt) {
                    s[qt] = MFMA32(k0, qf[qt][0], negm[qt]);
                    s[qt] = MFMA32(k1, qf[qt][1], s[qt]);
                }
                const bool first = (it == 0 && kb == 0);
                unsigned chk = 0;
                if (!first) {
#pragma unroll
                    for (int qt = 0; qt < 2; ++qt) {
                        f32x16 e;
#pragma unroll
                        for (int i = 0; i < 16; ++i) e[i] = __builtin_amdgcn_exp2f(s[qt][i]);
                        pf[qt][0] = pack8(e, 0);
                        pf[qt][1] = pack8(e, 1);
                        chk |= pf[qt][0][0] | pf[qt][0][1] | pf[qt][0][2] | pf[qt][0][3];
                        chk |= pf[qt][1][0] | pf[qt][1][1] | pf[qt][1][2] | pf[qt][1][3];
                    }
                }
                if (first || __any((chk & 0x40004000u) != 0u)) {
#pragma unroll
                    for (int qt = 0; qt < 2; ++qt) {
                        float mx = s[qt][0];
#pragma unroll
                        for (int i = 1; i < 16; ++i) mx = fmaxf(mx, s[qt][i]);
                        auto rr = __builtin_amdgcn_permlane32_swap(__float_as_uint(mx), __float_as_uint(mx), false, false);
                        mx = fmaxf(__uint_as_float(rr[0]), __uint_as_float(rr[1]));
                        float delta = mx + 10.f;
                        if (!first) {
                            delta = fmaxf(delta, 0.f);
                            const float alpha = __builtin_amdgcn_exp2f(-delta);
#pragma unroll
                            for (int i = 0; i < 16; ++i) o[qt][i] *= alpha;
                        }
                        const float nm = negm[qt][0] - delta;
#pragma unroll
                        for (int i = 0; i < 16; ++i) negm[qt][i] = nm;
                        f32x16 e;
#pragma unroll
                        for (int i = 0; i < 16; ++i) e[i] = __builtin_amdgcn_exp2f(s[qt][i] - delta);
                        pf[qt][0] = pack8(e, 0);
                        pf[qt][1] = pack8(e, 1);
                    }
                }
#pragma unroll
                for (int s2 = 0; s2 < 2; ++s2) {
                    const bf16x8 vf = *(LAS bf16x8*)(sl + 4096 + (kb * 2 + s2) * 1024);
#pragma unroll
                    for (int qt = 0; qt < 2; ++qt) o[qt] = MFMA32(vf, __builtin_bit_cast(bf16x8, pf[qt][s2]), o[qt]);
                }
            }
        }
#pragma unroll
        for (int qt = 0; qt < 2; ++qt) {
            const unsigned lv = __float_as_uint(o[qt][8]);
            auto rr = __builtin_amdgcn_permlane32_swap(lv, lv, false, false);
            const float lsum = __uint_as_float(h ? lv : rr[1]);
            const float inv = 1.0f / lsum;
            char* cp = ws + WS_CTXA + (size_t)((tb32 + qt) * 40) * 1024;
#pragma unroll
            for (int gq = 0; gq < 3; ++gq) {
                const int d0 = 8 * gq + 4 * h;
                if (d0 < 20) {
                    const int k = head * 20 + d0;
                    u32x2 w;
                    w[0] = pack2(o[qt][4 * gq + 0] * inv, o[qt][4 * gq + 1] * inv);
                    w[1] = pack2(o[qt][4 * gq + 2] * inv, o[qt][4 * gq + 3] * inv);
                    *(u32x2*)(cp + (k >> 4) * 1024 + (r + 32 * ((k >> 3) & 1)) * 16 + (k & 4) * 2) = w;
                }
            }
        }
    }
#undef K2_SYNC
}

constexpr int G3_STAGE = 49152;
constexpr int G3_LDS = 2 * G3_STAGE;

__global__ __launch_bounds__(512, 2) void k3_oproj(const __hip_bfloat16* __restrict__ Awo, const __hip_bfloat16* __restrict__ Bwo,
                                                   char* __restrict__ ws) {
    extern __shared__ __attribute__((aligned(16))) char smem[];
    LAS char* lds = (LAS char*)smem;
    const int tid = threadIdx.x, lane = tid & 63, wid = __builtin_amdgcn_readfirstlane(tid >> 6);
    const int wm = wid >> 1, wn = wid & 1, h = lane >> 5;
    const int bid = blockIdx.x;
    const bool isB = bid < 200;
    int mt, nt, sp, kt0, nkt, kstot;
    const char* Xc; const char* Wb;
    if (isB) {
        mt = bid & 7; const int rest = bid >> 3; nt = rest % 5; sp = rest / 5;
        kt0 = sp * 13; nkt = sp == 4 ? 12 : 13; kstot = 256;
        Xc = ws + WS_CTXB; Wb = (const char*)Bwo;
    } else {
        const int b2 = bid - 200; mt = b2 & 7; nt = b2 >> 3; sp = 5;
        kt0 = 0; nkt = 10; kstot = 40;
        Xc = ws + WS_CTXA; Wb = (const char*)Awo;
    }
    const char* xs = Xc + (size_t)((mt * 8 + wid) * kstot + kt0 * 4) * 1024 + lane * 16;
    unsigned woff[2];
#pragma unroll
    for (int i = 0; i < 2; ++i) {
        const int k = (wid * 2 + i) * 4 + (lane >> 4);
        const int p = lane & 15, b = (p >> 2) ^ (k & 3);
        woff[i] = (unsigned)(((kt0 * 64 + k) * 640 + nt * 128 + (b * 4 + (p & 3)) * 8) * 2);
    }
    auto stage = [&](int kt, int buf) {
        LAS char* xb = lds + buf * G3_STAGE + wid * 4096;
        LAS char* wb = lds + buf * G3_STAGE + 32768 + wid * 2048;
#pragma unroll
        for (int i = 0; i < 4; ++i) glds16(xs + (size_t)kt * 4096 + i * 1024, xb + i * 1024);
#pragma unroll
        for (int i = 0; i < 2; ++i) glds16(Wb + woff[i] + (unsigned)kt * (64u * 640u * 2u), wb + i * 1024);
    };
    const int q = (lane >> 2) & 3, p4 = lane & 3, g16 = (lane >> 4) & 1;
    const int r0 = isB ? 4 * h : 8 * h, r1 = isB ? 8 + 4 * h : 8 * h + 4;
    int wb0[2], wb1[2];
#pragma unroll
    for (int ni = 0; ni < 2; ++ni) {
        const int b = wn * 2 + ni;
        const int c = ((b ^ q) * 64) + g16 * 32 + p4 * 8;
        wb0[ni] = 32768 + (r0 + q) * 256 + c;
        wb1[ni] = 32768 + (r1 + q) * 256 + c;
    }
    f32x16 acc[2][2];
#pragma unroll
    for (int mi = 0; mi < 2; ++mi)
#pragma unroll
        for (int ni = 0; ni < 2; ++ni) acc[mi][ni] = f32x16{};

    stage(0, 0);
    for (int kt = 0; kt < nkt; ++kt) {
        const int buf = kt & 1;
        WAIT_VM(0);
        BARRIER();
        if (kt + 1 < nkt) stage(kt + 1, buf ^ 1);
        LAS char* bb = lds + buf * G3_STAGE;
#pragma unroll
        for (int ks = 0; ks < 4; ++ks) {
            bf16x8 xf[2], wf[2];
#pragma unroll
            for (int mi = 0; mi < 2; ++mi) xf[mi] = *(LAS bf16x8*)(bb + (wm * 2 + mi) * 4096 + ks * 1024 + lane * 16);
#pragma unroll
            for (int ni = 0; ni < 2; ++ni) wf[ni] = tr_frag(bb + wb0[ni] + ks * 4096, bb + wb1[ni] + ks * 4096);
#pragma unroll
            for (int mi = 0; mi < 2; ++mi)
#pragma unroll
                for (int ni = 0; ni < 2; ++ni) acc[mi][ni] = MFMA32(wf[ni], xf[mi], acc[mi][ni]);
        }
    }
#pragma unroll
    for (int mi = 0; mi < 2; ++mi)
#pragma unroll
        for (int ni = 0; ni < 2; ++ni) {
            const int mt32 = mt * 8 + wm * 2 + mi, nt32 = nt * 4 + wn * 2 + ni;
            char* dst = ws + WS_PART + (size_t)sp * SLAB + (size_t)(mt32 * 20 + nt32) * 4096 + lane * 16;
#pragma unroll
            for (int gq = 0; gq < 4; ++gq) {
                f32x4 v = {acc[mi][ni][4 * gq], acc[mi][ni][4 * gq + 1], acc[mi][ni][4 * gq + 2], acc[mi][ni][4 * gq + 3]};
                *(f32x4*)(dst + gq * 1024) = v;
            }
        }
}

__global__ __launch_bounds__(256) void k4_reduce(const char* __restrict__ ws, __hip_bfloat16* __restrict__ out) {
    const int lane = threadIdx.x & 63;
    const int w = blockIdx.x * 4 + (threadIdx.x >> 6);
    const int sub = w & 3, t = w >> 2;
    const int mt32 = t / 40, ct = t - mt32 * 40;
    const bool isB = ct >= 20;
    const int nt32 = isB ? ct - 20 : ct;
    const int ql = sub * 8 + (lane & 7), c8 = lane >> 3;
    const char* src = ws + WS_PART + (size_t)(mt32 * 20 + nt32) * 4096 + (c8 >> 1) * 1024 + (ql + 32 * (c8 & 1)) * 16;
    f32x4 v;
    if (isB) {
        v = *(const f32x4*)(src);
#pragma unroll
        for (int s = 1; s < NSPLIT; ++s) v += *(const f32x4*)(src + (size_t)s * SLAB);
    } else {
        v = *(const f32x4*)(src + (size_t)5 * SLAB);
    }
    u32x2 o;
    o[0] = pack2(v[0], v[1]);
    o[1] = pack2(v[2], v[3]);
    *(u32x2*)((char*)out + (size_t)(mt32 * 32 + ql) * 2560 + (ct * 32 + c8 * 4) * 2) = o;
}

extern "C" void kernel_launch(void* const* d_in, const int* in_sizes, int n_in,
                              void* d_out, int out_size, void* d_ws, size_t ws_size,
                              hipStream_t stream) {
    (void)in_sizes; (void)n_in; (void)out_size; (void)ws_size;
    const __hip_bfloat16* Ax = (const __hip_bfloat16*)d_in[0];
    const __hip_bfloat16* Awqkv = (const __hip_bfloat16*)d_in[1];
    const __hip_bfloat16* Awo = (const __hip_bfloat16*)d_in[2];
    const __hip_bfloat16* Bx = (const __hip_bfloat16*)d_in[3];
    const __hip_bfloat16* Bckv = (const __hip_bfloat16*)d_in[4];
    const __hip_bfloat16* Bwq = (const __hip_bfloat16*)d_in[5];
    const __hip_bfloat16* Bwuk = (const __hip_bfloat16*)d_in[6];
    const __hip_bfloat16* Bwuv = (const __hip_bfloat16*)d_in[7];
    const __hip_bfloat16* Bwo = (const __hip_bfloat16*)d_in[8];
    char* ws = (char*)d_ws;
    (void)hipFuncSetAttribute((const void*)k1_proj, hipFuncAttributeMaxDynamicSharedMemorySize, G1_LDS);
    (void)hipFuncSetAttribute((const void*)k2_attn, hipFuncAttributeMaxDynamicSharedMemorySize, K2_LDS);
    (void)hipFuncSetAttribute((const void*)k3_oproj, hipFuncAttributeMaxDynamicSharedMemorySize, G3_LDS);
    k1_proj<<<448, 512, G1_LDS, stream>>>(Ax, Awqkv, Bx, Bckv, Bwq, Bwuk, Bwuv, ws);
    k2_attn<<<256, 512, K2_LDS, stream>>>(ws);
    k3_oproj<<<240, 512, G3_LDS, stream>>>(Awo, Bwo, ws);
    k4_reduce<<<64 * 40, 256, 0, stream>>>(ws, (__hip_bfloat16*)d_out);
}
```

```cpp
#include <hip/hip_runtime.h>
#include <hip/hip_bf16.h>
#include <stdint.h>
#include <type_traits>

typedef short s16x4 __attribute__((ext_vector_type(4)));
typedef short bf16x8 __attribute__((ext_vector_type(8)));
typedef float f32x16 __attribute__((ext_vector_type(16)));
typedef float f32x4 __attribute__((ext_vector_type(4)));
typedef float f32x2 __attribute__((ext_vector_type(2)));
typedef unsigned u32x4 __attribute__((ext_vector_type(4)));
typedef unsigned u32x2 __attribute__((ext_vector_type(2)));
typedef __bf16 bf16x2_t __attribute__((ext_vector_type(2)));
#define LAS __attribute__((address_space(3)))
#define DI __device__ __forceinline__
#define MFMA32(a, b, c) __builtin_amdgcn_mfma_f32_32x32x16_bf16((a), (b), (c), 0, 0, 0)
#define CFENCE() asm volatile("" ::: "memory")
#define BARRIER() do { CFENCE(); __builtin_amdgcn_s_barrier(); CFENCE(); } while (0)
#define WAIT_VM(n) asm volatile("s_waitcnt vmcnt(" #n ") lgkmcnt(0)" ::: "memory")
#define SBAR() __builtin_amdgcn_sched_barrier(0)

DI unsigned pack2(float a, float b) {
    f32x2 v = {a, b};
    bf16x2_t r = __builtin_convertvector(v, bf16x2_t);
    return __builtin_bit_cast(unsigned, r);
}
DI u32x4 pack8(const f32x16& x, int s) {
    u32x4 w;
    w[0] = pack2(x[8 * s + 0], x[8 * s + 1]);
    w[1] = pack2(x[8 * s + 2], x[8 * s + 3]);
    w[2] = pack2(x[8 * s + 4], x[8 * s + 5]);
    w[3] = pack2(x[8 * s + 6], x[8 * s + 7]);
    return w;
}
DI u32x4 pack8s(const f32x16& x, int s, float c) {
    u32x4 w;
    w[0] = pack2(x[8 * s + 0] * c, x[8 * s + 1] * c);
    w[1] = pack2(x[8 * s + 2] * c, x[8 * s + 3] * c);
    w[2] = pack2(x[8 * s + 4] * c, x[8 * s + 5] * c);
    w[3] = pack2(x[8 * s + 6] * c, x[8 * s + 7] * c);
    return w;
}
DI void glds16(const void* g, LAS char* l) {
    __builtin_amdgcn_global_load_lds((const unsigned*)g, (LAS unsigned*)l, 16, 0, 0);
}
DI bf16x8 tr_frag(LAS char* p0, LAS char* p1) {
    s16x4 lo = __builtin_amdgcn_ds_read_tr16_b64_v4i16((LAS s16x4*)p0);
    s16x4 hi = __builtin_amdgcn_ds_read_tr16_b64_v4i16((LAS s16x4*)p1);
    return __builtin_shufflevector(lo, hi, 0, 1, 2, 3, 4, 5, 6, 7);
}

template <int I, int N, class F> DI void sfor(F&& f) {
    if constexpr (I < N) { f(std::integral_constant<int, I>{}); sfor<I + 1, N>(f); }
}
template <int OFF> DI void lds_rd(bf16x8& d, unsigned addr) {
    asm volatile("ds_read_b128 %0, %1 offset:%2" : "=v"(d) : "v"(addr), "i"(OFF));
}
template <int N> DI void lds_wait(bf16x8& d) { asm volatile("s_waitcnt lgkmcnt(%1)" : "+v"(d) : "i"(N)); }
template <int N> DI void lds_wait2(bf16x8& d, bf16x8& e) { asm volatile("s_waitcnt lgkmcnt(%2)" : "+v"(d), "+v"(e) : "i"(N)); }
#define LGKM0() __builtin_amdgcn_s_waitcnt(0xC07F)
DI void set_m0(unsigned v) { asm volatile("s_mov_b32 m0, %0\n\ts_nop 0" :: "s"(v) : "memory"); }
template <int OFF> DI void dma_s(unsigned voff, const char* sbase) {
    asm volatile("global_load_lds_dwordx4 %0, %1 offset:%2" :: "v"(voff), "s"(sbase), "i"(OFF) : "memory");
}

#define ABL_NOPIECE 0
#define ABL_NOBAR 0
#define ABL_T0 0
#define ABL_K2 0
#define ABL_NODMA 0
#define ABL_NOMFMA 0
#define SEG(i) do { } while (0)
#define STAMP(kid, idx) do { } while (0)
#define STAMPW(kid, idx, w) do { } while (0)
constexpr size_t WS_KVB = 0;
constexpr size_t WS_QB = WS_KVB + (size_t)32 * 1048576;
constexpr size_t WS_KVA = WS_QB + (size_t)16 * 1048576;
constexpr size_t WS_QA = WS_KVA + (size_t)8 * 1048576;
constexpr size_t WS_CTXB = WS_QA + (size_t)4 * 1048576;
constexpr size_t WS_CTXA = WS_CTXB + (size_t)16 * 1048576;
constexpr size_t WS_PART = WS_CTXA + (size_t)4 * 1048576;
constexpr int NSPLIT = 5;
constexpr size_t SLAB = (size_t)64 * 20 * 4096;

constexpr float CB = 0.08838834764831845f * 1.4426950408889634f;
constexpr float CA = 0.22360679774997896f * 1.4426950408889634f;

constexpr int G1_LDS = 2 * 65536;
enum { EP_BK = 0, EP_BV = 1, EP_BQ = 2, EP_AQK = 3, EP_AV = 4 };

template <int EP>
DI void g1_body(const __hip_bfloat16* __restrict__ X, const __hip_bfloat16* __restrict__ W, int K, int ldw,
                int m0, int n0, int nlim, int nsec, char* __restrict__ ws, LAS char* lds) {
    constexpr bool TR = (EP == EP_BK || EP == EP_BQ || EP == EP_AQK);
    const int tid = threadIdx.x, lane = tid & 63, wid = __builtin_amdgcn_readfirstlane(tid >> 6);
    const int wm = wid >> 2, wn = wid & 3, r = lane & 31, h = lane >> 5;
    const int nkt = K >> 6;

    unsigned xoff[4], woff[4];
#pragma unroll
    for (int i = 0; i < 4; ++i) {
        const int row = (wid * 4 + i) * 8 + (lane >> 3);
        const int c = (lane & 7) ^ ((row >> 1) & 7);
        xoff[i] = (unsigned)(((m0 + row) * K + c * 8) * 2);
        const int k = (wid * 4 + i) * 2 + (lane >> 5);
        const int p = lane & 31, b = (p >> 2) ^ (k & 3);
        int col = n0 + (b * 4 + (p & 3)) * 8;
        col = col < nlim ? col : nlim - 8;
        woff[i] = (unsigned)((k * ldw + col) * 2);
    }
    const char* Xb = (const char*)X;
    const char* Wb = (const char*)W;
    const unsigned wstep = (unsigned)(64 * ldw * 2);
    const unsigned lds0 = (unsigned)(uintptr_t)lds;
#pragma unroll
    for (int i = 0; i < 4; ++i) { xoff[i] -= i * 1024; woff[i] -= i * 1024; }
    auto piece = [&](int kt, int buf, auto ic) {
        constexpr int i = decltype(ic)::value;
        if constexpr (i == 0) set_m0(lds0 + buf * 65536 + wid * 4096);
        if constexpr (i == 4) set_m0(lds0 + buf * 65536 + 32768 + wid * 4096);
        if constexpr (i < 4) dma_s<i * 1024>(xoff[i], Xb + (size_t)kt * 128);
        else dma_s<(i - 4) * 1024>(woff[i - 4], Wb + (size_t)kt * wstep);
    };
    auto stage = [&](int kt, int buf) { sfor<0, 8>([&](auto ic) { piece(kt, buf, ic); }); };

    const int xsw = (r >> 1) & 7;
    const int xbase = (wm * 128 + r) * 128;
    const int q = (lane >> 2) & 3, p4 = lane & 3, g16 = (lane >> 4) & 1;
    int wbase[2];
#pragma unroll
    for (int ni = 0; ni < 2; ++ni) {
        const int b = wn * 2 + ni;
        wbase[ni] = 32768 + (8 * h + q) * 512 + ((b ^ q) * 64) + g16 * 32 + p4 * 8;
    }

    f32x16 acc[4][2];
#pragma unroll
    for (int mi = 0; mi < 4; ++mi)
#pragma unroll
        for (int ni = 0; ni < 2; ++ni) acc[mi][ni] = f32x16{};

    bf16x8 xf[2][4], wf[2][2];
#define G1_RD(set, bb, ks)                                                                                       \
    do {                                                                                                         \
        const int xc_ = ((2 * (ks) + h) ^ xsw) * 16;                                                             \
        _Pragma("unroll") for (int ni = 0; ni < 2; ++ni)                                                         \
            wf[set][ni] = tr_frag((bb) + wbase[ni] + (ks) * 8192, (bb) + wbase[ni] + (ks) * 8192 + 2048);        \
        _Pragma("unroll") for (int mi = 0; mi < 4; ++mi) xf[set][mi] = *(LAS bf16x8*)((bb) + xbase + xc_ + mi * 4096); \
    } while (0)
#define G1_MM(set, kn, bn_, p0, pe)                                                                              \
    sfor<0, 4>([&](auto mc) {                                                                                    \
        constexpr int mi = decltype(mc)::value;                                                                  \
        _Pragma("unroll") for (int ni = 0; ni < 2; ++ni)                                                         \
            acc[mi][ni] = TR ? MFMA32(wf[set][ni], xf[set][mi], acc[mi][ni]) : MFMA32(xf[set][mi], wf[set][ni], acc[mi][ni]); \
        if constexpr ((p0) + mi < (pe)) { SBAR(); if ((kn) < nkt) piece((kn), (bn_), std::integral_constant<int, (p0) + mi>{}); SBAR(); } \
    })
    STAMP(0, 0);
    stage(0, 0);
    stage(1, 1);
    WAIT_VM(8);
    BARRIER();
    STAMP(0, 1);
    G1_RD(0, lds, 0);
#pragma unroll 1
    for (int kt = 0; kt < nkt; ++kt) {
        LAS char* bb = lds + (kt & 1) * 65536;
        LAS char* bn = lds + ((kt + 1) & 1) * 65536;
        const int k1 = kt >= 1 ? kt + 1 : nkt;
        LGKM0(); G1_RD(1, bb, 1); SBAR(); G1_MM(0, k1, (kt + 1) & 1, 4, 8); SBAR();
        LGKM0(); G1_RD(0, bb, 2); SBAR(); G1_MM(1, nkt, 0, 0, 0); SBAR();
        LGKM0(); G1_RD(1, bb, 3); SBAR(); G1_MM(0, nkt, 0, 0, 0); SBAR();
        LGKM0(); WAIT_VM(0);
        BARRIER();
        if (kt + 1 < nkt) G1_RD(0, bn, 0);
        SBAR(); G1_MM(1, kt + 2, kt & 1, 0, 4); SBAR();
    }
#undef G1_RD
#undef G1_MM

    STAMP(0, 2);
#pragma unroll
    for (int mi = 0; mi < 4; ++mi) {
        const int tb = (m0 + wm * 128 + mi * 32) >> 5;
        const int tile = tb >> 1, kb = tb & 1;
#pragma unroll
        for (int ni = 0; ni < 2; ++ni) {
            const int nb = n0 + wn * 64 + ni * 32;
            if (nb >= nlim) continue;
            if constexpr (EP == EP_BK) {
                const int head = nb >> 7, db = (nb >> 5) & 3;
                char* dst = ws + WS_KVB + (size_t)head * 1048576 + tile * 32768 + (kb * 8 + db * 2) * 1024 + lane * 16;
                *(u32x4*)(dst) = pack8(acc[mi][ni], 0);
                *(u32x4*)(dst + 1024) = pack8(acc[mi][ni], 1);
            } else if constexpr (EP == EP_BV) {
                const int head = nb >> 7, db = (nb >> 5) & 3;
                char* dst = ws + WS_KVB + (size_t)head * 1048576 + tile * 32768 + 16384 + (kb * 8 + db) * 1024 + lane * 16;
                *(u32x4*)(dst) = pack8(acc[mi][ni], 0);
                *(u32x4*)(dst + 4096) = pack8(acc[mi][ni], 1);
            } else if constexpr (EP == EP_BQ) {
                const int head = nb >> 7, db = (nb >> 5) & 3;
                char* dst = ws + WS_QB + (size_t)((head * 64 + tb) * 8 + db * 2) * 1024 + lane * 16;
                *(u32x4*)(dst) = pack8s(acc[mi][ni], 0, CB);
                *(u32x4*)(dst + 1024) = pack8s(acc[mi][ni], 1, CB);
            } else if constexpr (EP == EP_AQK) {
                const bool isq = nb < 640;
                const float sc = isq ? CA : 1.0f;
#pragma unroll
                for (int g = 0; g < 4; ++g) {
                    const int n = nb - (isq ? 0 : 640) + 8 * g + 4 * h;
                    const int head = n / 20, d0 = n - head * 20;
                    char* base = isq ? ws + WS_QA + (size_t)((head * 64 + tb) * 2) * 1024
                                     : ws + WS_KVA + (size_t)head * 262144 + tile * 8192 + kb * 2048;
                    char* dst = base + (d0 >> 4) * 1024 + (r + 32 * ((d0 >> 3) & 1)) * 16 + (d0 & 4) * 2;
                    u32x2 w;
                    w[0] = pack2(acc[mi][ni][4 * g + 0] * sc, acc[mi][ni][4 * g + 1] * sc);
                    w[1] = pack2(acc[mi][ni][4 * g + 2] * sc, acc[mi][ni][4 * g + 3] * sc);
                    *(u32x2*)dst = w;
                    if (d0 == 16) {
                        *(u32x2*)(dst + 8) = (u32x2){isq ? 0u : 0x3F803F80u, 0u};
                        *(u32x4*)(base + 1024 + (r + 32) * 16) = (u32x4){0u, 0u, 0u, 0u};
                    }
                }
            } else {
                const int n = nb - nsec + r;
                const int head = n / 20, d = n - head * 20;
                char* dst = ws + WS_KVA + (size_t)head * 262144 + tile * 8192 + 4096 + kb * 2048 + (d + 32 * h) * 16;
                *(u32x4*)(dst) = pack8(acc[mi][ni], 0);
                *(u32x4*)(dst + 1024) = pack8(acc[mi][ni], 1);
                if (d == 19) {
                    const u32x4 one = {0x3F803F80u, 0x3F803F80u, 0x3F803F80u, 0x3F803F80u};
                    *(u32x4*)(dst + 16) = one;
                    *(u32x4*)(dst + 1024 + 16) = one;
                }
            }
        }
    }
    STAMP(0, 3);
}

__global__ __launch_bounds__(512, 2) void k1_proj(const __hip_bfloat16* __restrict__ Ax, const __hip_bfloat16* __restrict__ Awqkv,
                                                  const __hip_bfloat16* __restrict__ Bx, const __hip_bfloat16* __restrict__ Bckv,
                                                  const __hip_bfloat16* __restrict__ Bwq, const __hip_bfloat16* __restrict__ Bwuk,
                                                  const __hip_bfloat16* __restrict__ Bwuv, char* __restrict__ ws) {
    extern __shared__ __attribute__((aligned(16))) char smem[];
    LAS char* lds = (LAS char*)smem;
    const int bid = blockIdx.x;
    const int mt = (bid >> 3) & 7;
    const int ct = (bid & 7) + 8 * (bid >> 6);
    const int m0 = mt * 256;
    if (ct < 16) g1_body<EP_BK>(Bckv, Bwuk, 512, 4096, m0, ct * 256, 4096, 0, ws, lds);
    else if (ct < 32) g1_body<EP_BV>(Bckv, Bwuv, 512, 4096, m0, (ct - 16) * 256, 4096, 0, ws, lds);
    else if (ct < 48) g1_body<EP_BQ>(Bx, Bwq, 640, 4096, m0, (ct - 32) * 256, 4096, 0, ws, lds);
    else if (ct < 53) g1_body<EP_AQK>(Ax, Awqkv, 640, 1920, m0, (ct - 48) * 256, 1280, 0, ws, lds);
    else g1_body<EP_AV>(Ax, Awqkv, 640, 1920, m0, 1280 + (ct - 53) * 256, 1920, 1280, ws, lds);
}

constexpr int SLOT = 40960;
constexpr int NSLOT = 4;
constexpr int K2_LDS = NSLOT * SLOT;
constexpr int NIT = 34;

__global__ __launch_bounds__(512, 2) void k2_attn(char* __restrict__ ws) {
    extern __shared__ __attribute__((aligned(16))) char smem[];
    LAS char* lds = (LAS char*)smem;
    const int tid = threadIdx.x, lane = tid & 63, wid = __builtin_amdgcn_readfirstlane(tid >> 6);
    const int r = lane & 31, h = lane >> 5;
    const int g = blockIdx.x, xcd = g & 7, idx = g >> 3;
    const int head = xcd * 4 + (idx >> 3), x = idx & 7;
    const char* kvb = ws + WS_KVB + (size_t)head * 1048576;
    const char* kva = ws + WS_KVA + (size_t)head * 262144;
    const int n1 = 32 - 2 * x;
    const unsigned lbase = (unsigned)(uintptr_t)lds + lane * 16;

    STAMP(1, 0);
    const unsigned lds0 = (unsigned)(uintptr_t)lds;
    const unsigned voff = lane * 16;
    const int wa = wid & 3;
#define K2_GRP(tt, gg)                                                                      \
    const int t3_ = (tt) < NIT ? (tt) : NIT - 1;                                            \
    const int tb3_ = t3_ < n1 ? t3_ : t3_ - n1;                                             \
    const int ta3_ = t3_ < 32 ? t3_ : 31;                                                   \
    const unsigned sl_ = lds0 + ((tt) < NIT ? ((tt) & 3) : 2 + ((tt) & 1)) * SLOT;          \
    const char* sb_ = (gg) < 2 ? kvb + (size_t)tb3_ * 32768 + wa * 8192 + (gg) * 4096 : kva + (size_t)ta3_ * 8192 + wa * 2048; \
    set_m0((gg) < 2 ? sl_ + wa * 8192 + (gg) * 4096 : sl_ + 32768 + wa * 2048)
#define K2_STAGE_ALL(tt)                                                                    \
    do {                                                                                    \
        { K2_GRP(tt, 0); dma_s<0>(voff, sb_); dma_s<1024>(voff, sb_); dma_s<2048>(voff, sb_); dma_s<3072>(voff, sb_); } \
        { K2_GRP(tt, 1); dma_s<0>(voff, sb_); dma_s<1024>(voff, sb_); dma_s<2048>(voff, sb_); dma_s<3072>(voff, sb_); } \
        { K2_GRP(tt, 2); dma_s<0>(voff, sb_); dma_s<1024>(voff, sb_); }                     \
    } while (0)
    if (wid >= 4) { K2_STAGE_ALL(0); K2_STAGE_ALL(1); }

#define K2_SYNC_A() do { WAIT_VM(10); BARRIER(); } while (0)
#define K2_SYNC_B() do { asm volatile("s_waitcnt lgkmcnt(0)" ::: "memory"); BARRIER(); } while (0)

    if (wid < 4) {
        bf16x8 fr[8];
        K2_SYNC_B();
        STAMP(1, 1);
        sfor<0, 8>([&](auto ic) { constexpr int j = decltype(ic)::value; lds_rd<((j & 1) * 8 + (j >> 1)) * 1024>(fr[j], lbase); });
        int it = 0;
        for (int blk = 0; blk < 2; ++blk) {
            const int qb = blk == 0 ? 15 - x : x;
            const int nt = 2 * qb + 2;
            const int q0 = qb * 128 + wid * 32;
            const int tb32 = q0 >> 5;
            bf16x8 qf[8];
            const char* qp = ws + WS_QB + (size_t)((head * 64 + tb32) * 8) * 1024 + lane * 16;
#pragma unroll
            for (int ks = 0; ks < 8; ++ks) qf[ks] = *(const bf16x8*)(qp + ks * 1024);
            f32x16 o[4];
#pragma unroll
            for (int db = 0; db < 4; ++db) o[db] = f32x16{};
            f32x16 negm = f32x16{};
            float l = 0.f;
            for (int t = 0; t < nt; ++t, ++it) {
                const int rel = (q0 - 64 * t) >> 5;
                const unsigned ac = lbase + (it & 3) * SLOT;
                const unsigned an = lbase + ((it + 1) & 3) * SLOT;
                f32x16 s0, s1;
                SEG(0);
                sfor<0, 16>([&](auto ic) {
                    constexpr int j = decltype(ic)::value, ks = j >> 1;
                    lds_wait<7>(fr[j & 7]);
                    if constexpr (j == 0) s0 = MFMA32(fr[0], qf[0], negm);
                    else if constexpr (j == 1) s1 = MFMA32(fr[1], qf[0], negm);
                    else if constexpr ((j & 1) == 0) s0 = MFMA32(fr[j & 7], qf[ks], s0);
                    else s1 = MFMA32(fr[j & 7], qf[ks], s1);
                    constexpr int jn = j + 8;
                    SBAR();
                    if constexpr (jn < 16) lds_rd<((jn & 1) * 8 + (jn >> 1)) * 1024>(fr[j & 7], ac);
                    else lds_rd<16384 + (jn - 16) * 1024>(fr[j & 7], ac);
                    SBAR();
                });
#define B_MASK()                                                                                        \
    do {                                                                                                \
        if (rel <= 1) {                                                                                 \
            const int lim = 32 * rel + r - 4 * h;                                                       \
            _Pragma("unroll") for (int i = 0; i < 16; ++i) {                                            \
                const int kr = (i & 3) + 8 * (i >> 2);                                                  \
                if (kr > lim) s0[i] = -__builtin_inff();                                                \
                if (kr + 32 > lim) s1[i] = -__builtin_inff();                                           \
            }                                                                                           \
        }                                                                                               \
    } while (0)
                SEG(1);
                B_MASK();
                float ps = 0.f;
                if (t > 0) {
#pragma unroll
                    for (int i = 0; i < 16; ++i) { s0[i] = __builtin_amdgcn_exp2f(s0[i]); ps += s0[i]; }
#pragma unroll
                    for (int i = 0; i < 16; ++i) { s1[i] = __builtin_amdgcn_exp2f(s1[i]); ps += s1[i]; }
                }
                if (t == 0 || __any(!(ps <= 65536.f))) {
                    if (t > 0) {
                        LAS char* sl = lds + (it & 3) * SLOT + lane * 16;
                        s0 = negm; s1 = negm;
#pragma unroll 1
                        for (int ks = 0; ks < 8; ++ks) {
                            bf16x8 qq = qf[0];
#pragma unroll
                            for (int u = 1; u < 8; ++u) qq = (ks == u) ? qf[u] : qq;
                            s0 = MFMA32(*(LAS bf16x8*)(sl + ks * 1024), qq, s0);
                            s1 = MFMA32(*(LAS bf16x8*)(sl + 8192 + ks * 1024), qq, s1);
                        }
                        B_MASK();
                    }
                    float mx = s0[0];
#pragma unroll
                    for (int i = 1; i < 16; ++i) mx = fmaxf(mx, s0[i]);
#pragma unroll
                    for (int i = 0; i < 16; ++i) mx = fmaxf(mx, s1[i]);
                    auto rr = __builtin_amdgcn_permlane32_swap(__float_as_uint(mx), __float_as_uint(mx), false, false);
                    mx = fmaxf(__uint_as_float(rr[0]), __uint_as_float(rr[1]));
                    float delta = mx;
                    if (t > 0) {
                        delta = fmaxf(mx, 0.f);
                        const float alpha = __builtin_amdgcn_exp2f(-delta);
                        l *= alpha;
#pragma unroll
                        for (int db = 0; db < 4; ++db)
#pragma unroll
                            for (int i = 0; i < 16; ++i) o[db][i] *= alpha;
                    }
                    const float nm = negm[0] - delta;
#pragma unroll
                    for (int i = 0; i < 16; ++i) negm[i] = nm;
                    ps = 0.f;
#pragma unroll
                    for (int i = 0; i < 16; ++i) { s0[i] = __builtin_amdgcn_exp2f(s0[i] - delta); ps += s0[i]; }
#pragma unroll
                    for (int i = 0; i < 16; ++i) { s1[i] = __builtin_amdgcn_exp2f(s1[i] - delta); ps += s1[i]; }
                }
                l += ps;
                bf16x8 pf[4];
                pf[0] = __builtin_bit_cast(bf16x8, pack8(s0, 0));
                pf[1] = __builtin_bit_cast(bf16x8, pack8(s0, 1));
                pf[2] = __builtin_bit_cast(bf16x8, pack8(s1, 0));
                pf[3] = __builtin_bit_cast(bf16x8, pack8(s1, 1));
                SEG(2);
                if (it + 1 < NIT) K2_SYNC_B();
                SEG(3);
                sfor<0, 16>([&](auto ic) {
                    constexpr int j = decltype(ic)::value, s4 = j >> 2, db = j & 3;
                    lds_wait<7>(fr[j & 7]);
                    o[db] = MFMA32(fr[j & 7], pf[s4], o[db]);
                    SBAR();
                    if constexpr (j < 8) lds_rd<16384 + (j + 8) * 1024>(fr[j & 7], ac);
                    else { constexpr int jn = j - 8; lds_rd<((jn & 1) * 8 + (jn >> 1)) * 1024>(fr[j & 7], an); }
                    SBAR();
                });
                SEG(4);
            }
            auto rr = __builtin_amdgcn_permlane32_swap(__float_as_uint(l), __float_as_uint(l), false, false);
            const float inv = 1.0f / (__uint_as_float(rr[0]) + __uint_as_float(rr[1]));
            char* cp = ws + WS_CTXB + (size_t)(tb32 * 256 + head * 8) * 1024 + lane * 16;
#pragma unroll
            for (int db = 0; db < 4; ++db) {
                *(u32x4*)(cp + (2 * db) * 1024) = pack8s(o[db], 0, inv);
                *(u32x4*)(cp + (2 * db + 1) * 1024) = pack8s(o[db], 1, inv);
            }
        }
        WAIT_VM(0);
        STAMP(1, 2);
#undef B_MASK
    } else {
        const int tb32 = x * 8 + wa * 2;
        bf16x8 qf[2][2];
        const char* qp = ws + WS_QA + (size_t)((head * 64 + tb32) * 2) * 1024 + lane * 16;
#pragma unroll
        for (int qt = 0; qt < 2; ++qt)
#pragma unroll
            for (int ks = 0; ks < 2; ++ks) qf[qt][ks] = *(const bf16x8*)(qp + (qt * 2 + ks) * 1024);
        f32x16 o[2];
        o[0] = f32x16{}; o[1] = f32x16{};
        float negm[2] = {0.f, 0.f};
        bf16x8 kf[4], vf[4];
        f32x16 s[2][2];
        K2_SYNC_A();
        {
            const unsigned a0 = lbase + 32768;
            lds_rd<0>(kf[0], a0); lds_rd<1024>(kf[1], a0); lds_rd<2048>(kf[2], a0); lds_rd<3072>(kf[3], a0);
        }
        for (int it = 0; it <= 32; ++it) {
            if (it > 0) {
                u32x4 pf[2][2][2];
                unsigned chk = 0;
                sfor<0, 4>([&](auto bc) {
                    constexpr int blk = decltype(bc)::value, kb = blk >> 1, qt = blk & 1;
                    f32x16 e;
                    if constexpr (blk < 3) {
                        K2_GRP(it + 2, blk);
                        sfor<0, 4>([&](auto ic) {
                            constexpr int c = decltype(ic)::value;
#pragma unroll
                            for (int i = 4 * c; i < 4 * c + 4; ++i) e[i] = __builtin_amdgcn_exp2f(s[kb][qt][i]);
                            SBAR();
                            if constexpr (blk < 2 || c < 2) dma_s<c * 1024>(voff, sb_);
                            SBAR();
                        });
                    } else {
#pragma unroll
                        for (int i = 0; i < 16; ++i) e[i] = __builtin_amdgcn_exp2f(s[kb][qt][i]);
                    }
                    pf[kb][qt][0] = pack8(e, 0);
                    pf[kb][qt][1] = pack8(e, 1);
                    chk |= pf[kb][qt][0][0] | pf[kb][qt][0][1] | pf[kb][qt][0][2] | pf[kb][qt][0][3];
                    chk |= pf[kb][qt][1][0] | pf[kb][qt][1][1] | pf[kb][qt][1][2] | pf[kb][qt][1][3];
                });
                const bool first = (it == 1);
                if (first || __any((chk & 0x40004000u) != 0u)) {
#pragma unroll
                    for (int qt = 0; qt < 2; ++qt) {
                        float mx = s[0][qt][0];
#pragma unroll
                        for (int i = 1; i < 16; ++i) mx = fmaxf(mx, s[0][qt][i]);
#pragma unroll
                        for (int i = 0; i < 16; ++i) mx = fmaxf(mx, s[1][qt][i]);
                        auto rr = __builtin_amdgcn_permlane32_swap(__float_as_uint(mx), __float_as_uint(mx), false, false);
                        mx = fmaxf(__uint_as_float(rr[0]), __uint_as_float(rr[1]));
                        float delta = mx + 10.f;
                        if (!first) delta = fmaxf(delta, 0.f);
                        const float tgt = negm[qt] - delta;
                        const unsigned hi = pack2(tgt, 0.f) & 0xFFFFu;
                        const float hif = __uint_as_float(hi << 16);
                        const unsigned lo = pack2(tgt - hif, 0.f) & 0xFFFFu;
                        const float neff = hif + __uint_as_float(lo << 16);
                        delta = negm[qt] - neff;
                        negm[qt] = neff;
                        {
                            u32x4 qq = __builtin_bit_cast(u32x4, qf[qt][1]);
                            qq[2] = h ? 0u : (hi | (lo << 16));
                            qf[qt][1] = __builtin_bit_cast(bf16x8, qq);
                        }
                        if (!first) {
                            const float alpha = __builtin_amdgcn_exp2f(-delta);
#pragma unroll
                            for (int i = 0; i < 16; ++i) o[qt][i] *= alpha;
                        }
#pragma unroll
                        for (int kb = 0; kb < 2; ++kb) {
                            f32x16 e;
#pragma unroll
                            for (int i = 0; i < 16; ++i) e[i] = __builtin_amdgcn_exp2f(s[kb][qt][i] - delta);
                            pf[kb][qt][0] = pack8(e, 0);
                            pf[kb][qt][1] = pack8(e, 1);
                        }
                    }
                }
                lds_wait2<4>(vf[0], vf[1]);
                lds_wait2<4>(vf[2], vf[3]);
#pragma unroll
                for (int kb = 0; kb < 2; ++kb)
#pragma unroll
                    for (int s2 = 0; s2 < 2; ++s2)
#pragma unroll
                        for (int qt = 0; qt < 2; ++qt)
                            o[qt] = MFMA32(vf[kb * 2 + s2], __builtin_bit_cast(bf16x8, pf[kb][qt][s2]), o[qt]);
            } else {
                K2_STAGE_ALL(2);
            }
            lds_wait2<0>(kf[0], kf[1]);
            lds_wait2<0>(kf[2], kf[3]);
#pragma unroll
            for (int kb = 0; kb < 2; ++kb)
#pragma unroll
                for (int qt = 0; qt < 2; ++qt) {
                    s[kb][qt] = MFMA32(kf[kb * 2], qf[qt][0], f32x16{});
                    s[kb][qt] = MFMA32(kf[kb * 2 + 1], qf[qt][1], s[kb][qt]);
                }
            SBAR();
            if (it == 32) STAMPW(1, 3, 4);
            K2_SYNC_A();
            const unsigned ac = lbase + (it & 3) * SLOT + 32768;
            const unsigned an = lbase + ((it + 1) & 3) * SLOT + 32768;
            lds_rd<4096>(vf[0], ac); lds_rd<5120>(vf[1], ac); lds_rd<6144>(vf[2], ac); lds_rd<7168>(vf[3], ac);
            lds_rd<0>(kf[0], an); lds_rd<1024>(kf[1], an); lds_rd<2048>(kf[2], an); lds_rd<3072>(kf[3], an);
        }
        WAIT_VM(0);
#pragma unroll
        for (int qt = 0; qt < 2; ++qt) {
            const unsigned lv = __float_as_uint(o[qt][8]);
            auto rr = __builtin_amdgcn_permlane32_swap(lv, lv, false, false);
            const float lsum = __uint_as_float(rr[1]);
            const float inv = 1.0f / lsum;
            char* cp = ws + WS_CTXA + (size_t)((tb32 + qt) * 40) * 1024;
#pragma unroll
            for (int gq = 0; gq < 3; ++gq) {
                const int d0 = 8 * gq + 4 * h;
                if (d0 < 20) {
                    const int k = head * 20 + d0;
                    u32x2 w;
                    w[0] = pack2(o[qt][4 * gq + 0] * inv, o[qt][4 * gq + 1] * inv);
                    w[1] = pack2(o[qt][4 * gq + 2] * inv, o[qt][4 * gq + 3] * inv);
                    *(u32x2*)(cp + (k >> 4) * 1024 + (r + 32 * ((k >> 3) & 1)) * 16 + (k & 4) * 2) = w;
                }
            }
        }
    }
}

constexpr int G3_STAGE = 49152;
constexpr int G3_LDS = 2 * G3_STAGE;

__global__ __launch_bounds__(512, 2) void k3_oproj(const __hip_bfloat16* __restrict__ Awo, const __hip_bfloat16* __restrict__ Bwo,
                                                   char* __restrict__ ws) {
    extern __shared__ __attribute__((aligned(16))) char smem[];
    LAS char* lds = (LAS char*)smem;
    const int tid = threadIdx.x, lane = tid & 63, wid = __builtin_amdgcn_readfirstlane(tid >> 6);
    const int wm = wid >> 1, wn = wid & 1, h = lane >> 5;
    const int bid = blockIdx.x;
    const bool isB = bid < 200;
    int mt, nt, sp, kt0, nkt, kstot;
    const char* Xc; const char* Wb;
    if (isB) {
        mt = bid & 7; const int rest = bid >> 3; nt = rest % 5; sp = rest / 5;
        kt0 = sp * 13; nkt = sp == 4 ? 12 : 13; kstot = 256;
        Xc = ws + WS_CTXB; Wb = (const char*)Bwo;
    } else {
        const int b2 = bid - 200; mt = b2 & 7; nt = b2 >> 3; sp = 5;
        kt0 = 0; nkt = 10; kstot = 40;
        Xc = ws + WS_CTXA; Wb = (const char*)Awo;
    }
    unsigned woff[2];
#pragma unroll
    for (int i = 0; i < 2; ++i) {
        const int k = (wid * 2 + i) * 4 + (lane >> 4);
        const int p = lane & 15, b = (p >> 2) ^ (k & 3);
        woff[i] = (unsigned)(((kt0 * 64 + k) * 640 + nt * 128 + (b * 4 + (p & 3)) * 8) * 2);
    }
    const unsigned lds0 = (unsigned)(uintptr_t)lds;
    const char* xsb = Xc + (size_t)((mt * 8 + wid) * kstot + kt0 * 4) * 1024;
    const unsigned voff = lane * 16;
    woff[1] -= 1024;
    auto piece = [&](int kt, int buf, auto ic) {
        constexpr int i = decltype(ic)::value;
        if constexpr (i == 0) set_m0(lds0 + buf * G3_STAGE + wid * 4096);
        if constexpr (i == 4) set_m0(lds0 + buf * G3_STAGE + 32768 + wid * 2048);
        if constexpr (i < 4) dma_s<i * 1024>(voff, xsb + (size_t)kt * 4096);
        else dma_s<(i - 4) * 1024>(woff[i - 4], Wb + (size_t)kt * (64u * 640u * 2u));
    };
    auto stage = [&](int kt, int buf) { sfor<0, 6>([&](auto ic) { piece(kt, buf, ic); }); };
    const int q = (lane >> 2) & 3, p4 = lane & 3, g16 = (lane >> 4) & 1;
    const int r0 = isB ? 4 * h : 8 * h, r1 = isB ? 8 + 4 * h : 8 * h + 4;
    int wb0[2], wb1[2];
#pragma unroll
    for (int ni = 0; ni < 2; ++ni) {
        const int b = wn * 2 + ni;
        const int c = ((b ^ q) * 64) + g16 * 32 + p4 * 8;
        wb0[ni] = 32768 + (r0 + q) * 256 + c;
        wb1[ni] = 32768 + (r1 + q) * 256 + c;
    }
    f32x16 acc[2][2];
#pragma unroll
    for (int mi = 0; mi < 2; ++mi)
#pragma unroll
        for (int ni = 0; ni < 2; ++ni) acc[mi][ni] = f32x16{};

    bf16x8 xf[2][2], wf[2][2];
#define G3_RD(set, bb, ks)                                                                                       \
    do {                                                                                                         \
        _Pragma("unroll") for (int ni = 0; ni < 2; ++ni)                                                         \
            wf[set][ni] = tr_frag((bb) + wb0[ni] + (ks) * 4096, (bb) + wb1[ni] + (ks) * 4096);                   \
        _Pragma("unroll") for (int mi = 0; mi < 2; ++mi)                                                         \
            xf[set][mi] = *(LAS bf16x8*)((bb) + (wm * 2 + mi) * 4096 + (ks) * 1024 + lane * 16);                 \
    } while (0)
#define G3_MM(set, kn, bn_, p0)                                                                                  \
    sfor<0, 2>([&](auto mc) {                                                                                    \
        constexpr int mi = decltype(mc)::value;                                                                  \
        _Pragma("unroll") for (int ni = 0; ni < 2; ++ni) acc[mi][ni] = MFMA32(wf[set][ni], xf[set][mi], acc[mi][ni]); \
        SBAR(); if ((kn) < nkt) piece((kn), (bn_), std::integral_constant<int, (p0) + mi>{}); SBAR();            \
    })
    STAMP(2, 0);
    stage(0, 0);
    stage(1, 1);
    WAIT_VM(6);
    BARRIER();
    STAMP(2, 1);
    G3_RD(0, lds, 0);
#pragma unroll 1
    for (int kt = 0; kt < nkt; ++kt) {
        LAS char* bb = lds + (kt & 1) * G3_STAGE;
        LAS char* bn = lds + ((kt + 1) & 1) * G3_STAGE;
        const int k1 = kt >= 1 ? kt + 1 : nkt;
        LGKM0(); G3_RD(1, bb, 1); SBAR(); G3_MM(0, k1, (kt + 1) & 1, 2); SBAR();
        LGKM0(); G3_RD(0, bb, 2); SBAR(); G3_MM(1, k1, (kt + 1) & 1, 4); SBAR();
        LGKM0(); G3_RD(1, bb, 3); SBAR(); G3_MM(0, nkt, 0, 0); SBAR();
        LGKM0(); WAIT_VM(0);
        BARRIER();
        if (kt + 1 < nkt) G3_RD(0, bn, 0);
        SBAR(); G3_MM(1, kt + 2, kt & 1, 0); SBAR();
    }
#undef G3_RD
#undef G3_MM
    STAMP(2, 2);
#pragma unroll
    for (int mi = 0; mi < 2; ++mi)
#pragma unroll
        for (int ni = 0; ni < 2; ++ni) {
            const int mt32 = mt * 8 + wm * 2 + mi, nt32 = nt * 4 + wn * 2 + ni;
            char* dst = ws + WS_PART + (size_t)sp * SLAB + (size_t)(mt32 * 20 + nt32) * 4096 + lane * 16;
#pragma unroll
            for (int gq = 0; gq < 4; ++gq) {
                f32x4 v = {acc[mi][ni][4 * gq], acc[mi][ni][4 * gq + 1], acc[mi][ni][4 * gq + 2], acc[mi][ni][4 * gq + 3]};
                *(f32x4*)(dst + gq * 1024) = v;
            }
        }
    STAMP(2, 3);
}

__global__ __launch_bounds__(256) void k4_reduce(const char* __restrict__ ws, __hip_bfloat16* __restrict__ out) {
    const int lane = threadIdx.x & 63;
    const int w = blockIdx.x * 4 + (threadIdx.x >> 6);
    const int sub = w & 3, t = w >> 2;
    const int mt32 = t / 40, ct = t - mt32 * 40;
    const bool isB = ct >= 20;
    const int nt32 = isB ? ct - 20 : ct;
    const int ql = sub * 8 + (lane & 7), c8 = lane >> 3;
    const char* src = ws + WS_PART + (size_t)(mt32 * 20 + nt32) * 4096 + (c8 >> 1) * 1024 + (ql + 32 * (c8 & 1)) * 16;
    f32x4 v;
    if (isB) {
        v = *(const f32x4*)(src);
#pragma unroll
        for (int s = 1; s < NSPLIT; ++s) v += *(const f32x4*)(src + (size_t)s * SLAB);
    } else {
        v = *(const f32x4*)(src + (size_t)5 * SLAB);
    }
    u32x2 o;
    o[0] = pack2(v[0], v[1]);
    o[1] = pack2(v[2], v[3]);
    *(u32x2*)((char*)out + (size_t)(mt32 * 32 + ql) * 2560 + (ct * 32 + c8 * 4) * 2) = o;
}

extern "C" void kernel_launch(void* const* d_in, const int* in_sizes, int n_in,
                              void* d_out, int out_size, void* d_ws, size_t ws_size,
                              hipStream_t stream) {
    (void)in_sizes; (void)n_in; (void)out_size; (void)ws_size;
    const __hip_bfloat16* Ax = (const __hip_bfloat16*)d_in[0];
    const __hip_bfloat16* Awqkv = (const __hip_bfloat16*)d_in[1];
    const __hip_bfloat16* Awo = (const __hip_bfloat16*)d_in[2];
    const __hip_bfloat16* Bx = (const __hip_bfloat16*)d_in[3];
    const __hip_bfloat16* Bckv = (const __hip_bfloat16*)d_in[4];
    const __hip_bfloat16* Bwq = (const __hip_bfloat16*)d_in[5];
    const __hip_bfloat16* Bwuk = (const __hip_bfloat16*)d_in[6];
    const __hip_bfloat16* Bwuv = (const __hip_bfloat16*)d_in[7];
    const __hip_bfloat16* Bwo = (const __hip_bfloat16*)d_in[8];
    char* ws = (char*)d_ws;
    (void)hipFuncSetAttribute((const void*)k1_proj, hipFuncAttributeMaxDynamicSharedMemorySize, G1_LDS);
    (void)hipFuncSetAttribute((const void*)k2_attn, hipFuncAttributeMaxDynamicSharedMemorySize, K2_LDS);
    (void)hipFuncSetAttribute((const void*)k3_oproj, hipFuncAttributeMaxDynamicSharedMemorySize, G3_LDS);
    k1_proj<<<448, 512, G1_LDS, stream>>>(Ax, Awqkv, Bx, Bckv, Bwq, Bwuk, Bwuv, ws);
    k2_attn<<<256, 512, K2_LDS, stream>>>(ws);
    k3_oproj<<<240, 512, G3_LDS, stream>>>(Awo, Bwo, ws);
    k4_reduce<<<64 * 40, 256, 0, stream>>>(ws, (__hip_bfloat16*)d_out);
}
```

```cpp
#include <hip/hip_runtime.h>
#include <hip/hip_bf16.h>
#include <stdint.h>
#include <type_traits>

typedef short s16x4 __attribute__((ext_vector_type(4)));
typedef short bf16x8 __attribute__((ext_vector_type(8)));
typedef float f32x16 __attribute__((ext_vector_type(16)));
typedef float f32x4 __attribute__((ext_vector_type(4)));
typedef float f32x2 __attribute__((ext_vector_type(2)));
typedef unsigned u32x4 __attribute__((ext_vector_type(4)));
typedef unsigned u32x2 __attribute__((ext_vector_type(2)));
typedef __bf16 bf16x2_t __attribute__((ext_vector_type(2)));
#define LAS __attribute__((address_space(3)))
#define DI __device__ __forceinline__
#define MFMA32(a, b, c) __builtin_amdgcn_mfma_f32_32x32x16_bf16((a), (b), (c), 0, 0, 0)
#define CFENCE() asm volatile("" ::: "memory")
#define BARRIER() do { CFENCE(); __builtin_amdgcn_s_barrier(); CFENCE(); } while (0)
#define WAIT_VM(n) asm volatile("s_waitcnt vmcnt(" #n ") lgkmcnt(0)" ::: "memory")
#define SBAR() __builtin_amdgcn_sched_barrier(0)

DI unsigned pack2(float a, float b) {
    f32x2 v = {a, b};
    bf16x2_t r = __builtin_convertvector(v, bf16x2_t);
    return __builtin_bit_cast(unsigned, r);
}
DI u32x4 pack8(const f32x16& x, int s) {
    u32x4 w;
    w[0] = pack2(x[8 * s + 0], x[8 * s + 1]);
    w[1] = pack2(x[8 * s + 2], x[8 * s + 3]);
    w[2] = pack2(x[8 * s + 4], x[8 * s + 5]);
    w[3] = pack2(x[8 * s + 6], x[8 * s + 7]);
    return w;
}
DI u32x4 pack8s(const f32x16& x, int s, float c) {
    u32x4 w;
    w[0] = pack2(x[8 * s + 0] * c, x[8 * s + 1] * c);
    w[1] = pack2(x[8 * s + 2] * c, x[8 * s + 3] * c);
    w[2] = pack2(x[8 * s + 4] * c, x[8 * s + 5] * c);
    w[3] = pack2(x[8 * s + 6] * c, x[8 * s + 7] * c);
    return w;
}
DI void glds16(const void* g, LAS char* l) {
    __builtin_amdgcn_global_load_lds((const unsigned*)g, (LAS unsigned*)l, 16, 0, 0);
}
DI bf16x8 tr_frag(LAS char* p0, LAS char* p1) {
    s16x4 lo = __builtin_amdgcn_ds_read_tr16_b64_v4i16((LAS s16x4*)p0);
    s16x4 hi = __builtin_amdgcn_ds_read_tr16_b64_v4i16((LAS s16x4*)p1);
    return __builtin_shufflevector(lo, hi, 0, 1, 2, 3, 4, 5, 6, 7);
}

#define STMODE 2
DI void st16(void* p, u32x4 v) {
    asm volatile("global_store_dwordx4 %0, %1, off sc1\n\ts_nop 1" :: "v"(p), "v"(v) : "memory");
}
template <int I, int N, class F> DI void sfor(F&& f) {
    if constexpr (I < N) { f(std::integral_constant<int, I>{}); sfor<I + 1, N>(f); }
}
template <int OFF> DI void lds_rd(bf16x8& d, unsigned addr) {
    asm volatile("ds_read_b128 %0, %1 offset:%2" : "=v"(d) : "v"(addr), "i"(OFF));
}
template <int N> DI void lds_wait(bf16x8& d) { asm volatile("s_waitcnt lgkmcnt(%1)" : "+v"(d) : "i"(N)); }
template <int N> DI void lds_wait2(bf16x8& d, bf16x8& e) { asm volatile("s_waitcnt lgkmcnt(%2)" : "+v"(d), "+v"(e) : "i"(N)); }
#define LGKM0() __builtin_amdgcn_s_waitcnt(0xC07F)
DI void set_m0(unsigned v) { asm volatile("s_mov_b32 m0, %0\n\ts_nop 0" :: "s"(v) : "memory"); }
template <int OFF> DI void dma_s(unsigned voff, const char* sbase) {
    asm volatile("global_load_lds_dwordx4 %0, %1 offset:%2" :: "v"(voff), "s"(sbase), "i"(OFF) : "memory");
}

#define ABL_NOPIECE 0
#define ABL_NOBAR 0
#define ABL_T0 0
#define ABL_K2 0
#define ABL_NODMA 0
#define ABL_NOMFMA 0
#define SEG(i) do { } while (0)
#define STAMP(kid, idx) do { } while (0)
#define STAMPW(kid, idx, w) do { } while (0)
constexpr size_t WS_KVB = 0;
constexpr size_t WS_QB = WS_KVB + (size_t)32 * 1048576;
constexpr size_t WS_KVA = WS_QB + (size_t)16 * 1048576;
constexpr size_t WS_QA = WS_KVA + (size_t)8 * 1048576;
constexpr size_t WS_CTXB = WS_QA + (size_t)4 * 1048576;
constexpr size_t WS_CTXA = WS_CTXB + (size_t)16 * 1048576;
constexpr size_t WS_PART = WS_CTXA + (size_t)4 * 1048576;
constexpr int NSPLIT = 5;
constexpr size_t SLAB = (size_t)64 * 20 * 4096;

constexpr float CB = 0.08838834764831845f * 1.4426950408889634f;
constexpr float CA = 0.22360679774997896f * 1.4426950408889634f;

constexpr int G1_LDS = 2 * 65536;
enum { EP_BK = 0, EP_BV = 1, EP_BQ = 2, EP_AQK = 3, EP_AV = 4 };

struct G1Src { unsigned xoff[4], woff[4]; const char* Xb; const char* Wb; unsigned wstep; int nkt; };
DI void g1_src(G1Src& s, const __hip_bfloat16* X, const __hip_bfloat16* W, int K, int ldw, int m0, int n0, int nlim, int wid, int lane) {
#pragma unroll
    for (int i = 0; i < 4; ++i) {
        const int row = (wid * 4 + i) * 8 + (lane >> 3);
        const int c = (lane & 7) ^ ((row >> 1) & 7);
        s.xoff[i] = (unsigned)(((m0 + row) * K + c * 8) * 2) + 4096 - i * 1024;
        const int k = (wid * 4 + i) * 2 + (lane >> 5);
        const int p = lane & 31, b = (p >> 2) ^ (k & 3);
        int col = n0 + (b * 4 + (p & 3)) * 8;
        col = col < nlim ? col : nlim - 8;
        s.woff[i] = (unsigned)((k * ldw + col) * 2) - i * 1024;
    }
    s.Xb = (const char*)X; s.Wb = (const char*)W; s.wstep = (unsigned)(64 * ldw * 2); s.nkt = K >> 6;
}
template <int I> DI void g1_piece(const G1Src& s, int kt) {
    if constexpr (I < 4) dma_s<I * 1024 - 4096>(s.xoff[I], s.Xb + (size_t)kt * 128);
    else dma_s<(I - 4) * 1024>(s.woff[I - 4], s.Wb + (size_t)kt * s.wstep);
}

template <bool TR>
DI void g1_loop(f32x16 (&acc)[4][2], bf16x8 (&xf)[2][4], bf16x8 (&wf)[2][2], const G1Src& cur, const G1Src& nxt, bool has_next, bool first,
                LAS char* lds, int wid, int lane) {
    const int wm = wid >> 2, wn = wid & 3, r = lane & 31, h = lane >> 5;
    const unsigned lds0 = (unsigned)(uintptr_t)lds;
    const int nkt = cur.nkt;
    const int xsw = (r >> 1) & 7;
    const int xbase = wm * 4 * 8192 + r * 128;
    const int q = (lane >> 2) & 3, p4 = lane & 3, g16 = (lane >> 4) & 1;
    int wbase[2];
#pragma unroll
    for (int ni = 0; ni < 2; ++ni) {
        const int b = wn * 2 + ni;
        wbase[ni] = h * 8192 + 4096 + q * 512 + ((b ^ q) * 64) + g16 * 32 + p4 * 8;
    }
#pragma unroll
    for (int mi = 0; mi < 4; ++mi)
#pragma unroll
        for (int ni = 0; ni < 2; ++ni) acc[mi][ni] = f32x16{};
#define G1_RD(set, bb, ks)                                                                                       \
    do {                                                                                                         \
        const int xc_ = ((2 * (ks) + h) ^ xsw) * 16;                                                             \
        _Pragma("unroll") for (int ni = 0; ni < 2; ++ni)                                                         \
            wf[set][ni] = tr_frag((bb) + wbase[ni] + (ks) * 16384, (bb) + wbase[ni] + (ks) * 16384 + 2048);      \
        _Pragma("unroll") for (int mi = 0; mi < 4; ++mi) xf[set][mi] = *(LAS bf16x8*)((bb) + xbase + xc_ + mi * 8192); \
    } while (0)
#define G1_MM(set, on, kn, p0, pe)                                                                               \
    sfor<0, 4>([&](auto mc) {                                                                                    \
        constexpr int mi = decltype(mc)::value;                                                                  \
        _Pragma("unroll") for (int ni = 0; ni < 2; ++ni)                                                         \
            acc[mi][ni] = TR ? MFMA32(wf[set][ni], xf[set][mi], acc[mi][ni]) : MFMA32(xf[set][mi], wf[set][ni], acc[mi][ni]); \
        if constexpr ((p0) + mi < (pe)) {                                                                        \
            SBAR();                                                                                              \
            if (on) { if ((kn) < nkt) g1_piece<(p0) + mi>(cur, (kn)); else g1_piece<(p0) + mi>(nxt, (kn) - nkt); } \
            SBAR();                                                                                              \
        }                                                                                                        \
    })
    if (first) G1_RD(0, lds, 0);
#pragma unroll 1
    for (int kt = 0; kt < nkt; ++kt) {
        LAS char* bb = lds + (kt & 1) * 65536;
        LAS char* bn = lds + ((kt + 1) & 1) * 65536;
        const bool on1 = !(first && kt == 0) && (kt + 1 < nkt || has_next);
        const bool on2 = kt + 2 < nkt || has_next;
        LGKM0(); G1_RD(1, bb, 1); SBAR(); G1_MM(0, on1, kt + 1, 4, 8); SBAR();
        LGKM0(); G1_RD(0, bb, 2); SBAR(); G1_MM(1, false, 0, 0, 0); SBAR();
        LGKM0(); G1_RD(1, bb, 3); SBAR(); G1_MM(0, false, 0, 0, 0); SBAR();
        LGKM0(); WAIT_VM(0);
        BARRIER();
        set_m0(lds0 + (kt & 1) * 65536 + wid * 8192 + 4096);
        if (kt + 1 < nkt || has_next) G1_RD(0, bn, 0);
        SBAR(); G1_MM(1, on2, kt + 2, 0, 4); SBAR();
    }
#undef G1_RD
#undef G1_MM
}

template <int EP>
DI void g1_epi(const f32x16 (&acc)[4][2], int m0, int n0, int nlim, int nsec, char* __restrict__ ws, int wid, int lane) {
    const int wm = wid >> 2, wn = wid & 3, r = lane & 31, h = lane >> 5;
#pragma unroll
    for (int mi = 0; mi < 4; ++mi) {
        const int tb = (m0 + wm * 128 + mi * 32) >> 5;
        const int tile = tb >> 1, kb = tb & 1;
#pragma unroll
        for (int ni = 0; ni < 2; ++ni) {
            const int nb = n0 + wn * 64 + ni * 32;
            if (nb >= nlim) continue;
            if constexpr (EP == EP_BK) {
                const int head = nb >> 7, db = (nb >> 5) & 3;
                char* dst = ws + WS_KVB + (size_t)head * 1048576 + tile * 32768 + (kb * 8 + db * 2) * 1024 + lane * 16;
                st16(dst, pack8(acc[mi][ni], 0));
                st16(dst + 1024, pack8(acc[mi][ni], 1));
            } else if constexpr (EP == EP_BV) {
                const int head = nb >> 7, db = (nb >> 5) & 3;
                char* dst = ws + WS_KVB + (size_t)head * 1048576 + tile * 32768 + 16384 + (kb * 8 + db) * 1024 + lane * 16;
                st16(dst, pack8(acc[mi][ni], 0));
                st16(dst + 4096, pack8(acc[mi][ni], 1));
            } else if constexpr (EP == EP_BQ) {
                const int head = nb >> 7, db = (nb >> 5) & 3;
                char* dst = ws + WS_QB + (size_t)((head * 64 + tb) * 8 + db * 2) * 1024 + lane * 16;
                st16(dst, pack8s(acc[mi][ni], 0, CB));
                st16(dst + 1024, pack8s(acc[mi][ni], 1, CB));
            } else if constexpr (EP == EP_AQK) {
                const bool isq = nb < 640;
                const float sc = isq ? CA : 1.0f;
#pragma unroll
                for (int g = 0; g < 4; ++g) {
                    const int n = nb - (isq ? 0 : 640) + 8 * g + 4 * h;
                    const int head = n / 20, d0 = n - head * 20;
                    char* base = isq ? ws + WS_QA + (size_t)((head * 64 + tb) * 2) * 1024
                                     : ws + WS_KVA + (size_t)head * 262144 + tile * 8192 + kb * 2048;
                    char* dst = base + (d0 >> 4) * 1024 + (r + 32 * ((d0 >> 3) & 1)) * 16 + (d0 & 4) * 2;
                    u32x2 w;
                    w[0] = pack2(acc[mi][ni][4 * g + 0] * sc, acc[mi][ni][4 * g + 1] * sc);
                    w[1] = pack2(acc[mi][ni][4 * g + 2] * sc, acc[mi][ni][4 * g + 3] * sc);
                    *(u32x2*)dst = w;
                    if (d0 == 16) {
                        *(u32x2*)(dst + 8) = (u32x2){isq ? 0u : 0x3F803F80u, 0u};
                        *(u32x4*)(base + 1024 + (r + 32) * 16) = (u32x4){0u, 0u, 0u, 0u};
                    }
                }
            } else {
                const int n = nb - nsec + r;
                const int head = n / 20, d = n - head * 20;
                char* dst = ws + WS_KVA + (size_t)head * 262144 + tile * 8192 + 4096 + kb * 2048 + (d + 32 * h) * 16;
                st16(dst, pack8(acc[mi][ni], 0));
                st16(dst + 1024, pack8(acc[mi][ni], 1));
                if (d == 19) {
                    const u32x4 one = {0x3F803F80u, 0x3F803F80u, 0x3F803F80u, 0x3F803F80u};
                    *(u32x4*)(dst + 16) = one;
                    *(u32x4*)(dst + 1024 + 16) = one;
                }
            }
        }
    }
}

__global__ __launch_bounds__(512, 2) void k1_proj(const __hip_bfloat16* __restrict__ Ax, const __hip_bfloat16* __restrict__ Awqkv,
                                                  const __hip_bfloat16* __restrict__ Bx, const __hip_bfloat16* __restrict__ Bckv,
                                                  const __hip_bfloat16* __restrict__ Bwq, const __hip_bfloat16* __restrict__ Bwuk,
                                                  const __hip_bfloat16* __restrict__ Bwuv, char* __restrict__ ws) {
    extern __shared__ __attribute__((aligned(16))) char smem[];
    LAS char* lds = (LAS char*)smem;
    const int tid = threadIdx.x, lane = tid & 63, wid = __builtin_amdgcn_readfirstlane(tid >> 6);
    const int bid = blockIdx.x;
    const int m0 = ((bid >> 3) & 7) * 256;
    const int c1 = (bid & 7) + 8 * (bid >> 6);
    const int c2 = c1;
    const bool has2 = c2 < 24;
    G1Src s1, s2;
    if (c1 < 16) g1_src(s1, Bckv, Bwuk, 512, 4096, m0, c1 * 256, 4096, wid, lane);
    else g1_src(s1, Bckv, Bwuv, 512, 4096, m0, (c1 - 16) * 256, 4096, wid, lane);
    const int n2 = c2 < 16 ? c2 * 256 : (c2 - 16) * 256;
    if (c2 < 16) g1_src(s2, Bx, Bwq, 640, 4096, m0, n2, 4096, wid, lane);
    else g1_src(s2, Ax, Awqkv, 640, 1920, m0, has2 ? n2 : 0, c2 < 21 ? 1280 : 1920, wid, lane);

    f32x16 acc[4][2];
    bf16x8 xf[2][4], wf[2][2];
    const unsigned lds0 = (unsigned)(uintptr_t)lds;
    STAMP(0, 0);
    set_m0(lds0 + wid * 8192 + 4096);
    sfor<0, 8>([&](auto ic) { g1_piece<decltype(ic)::value>(s1, 0); });
    set_m0(lds0 + 65536 + wid * 8192 + 4096);
    sfor<0, 8>([&](auto ic) { g1_piece<decltype(ic)::value>(s1, 1); });
    WAIT_VM(8);
    BARRIER();
    STAMP(0, 1);
    if (c1 < 16) { g1_loop<true>(acc, xf, wf, s1, s2, has2, true, lds, wid, lane); g1_epi<EP_BK>(acc, m0, c1 * 256, 4096, 0, ws, wid, lane); }
    else { g1_loop<false>(acc, xf, wf, s1, s2, has2, true, lds, wid, lane); g1_epi<EP_BV>(acc, m0, (c1 - 16) * 256, 4096, 0, ws, wid, lane); }
    STAMP(0, 2);
    if (c2 < 21) {
        g1_loop<true>(acc, xf, wf, s2, s2, false, false, lds, wid, lane);
        if (c2 < 16) g1_epi<EP_BQ>(acc, m0, n2, 4096, 0, ws, wid, lane);
        else g1_epi<EP_AQK>(acc, m0, n2, 1280, 0, ws, wid, lane);
    } else if (has2) {
        g1_loop<false>(acc, xf, wf, s2, s2, false, false, lds, wid, lane);
        g1_epi<EP_AV>(acc, m0, n2, 1920, 1280, ws, wid, lane);
    }
    STAMP(0, 3);
}

constexpr int SLOT = 40960;
constexpr int NSLOT = 4;
constexpr int K2_LDS = NSLOT * SLOT;
constexpr int NIT = 34;

__global__ __launch_bounds__(512, 2) void k2_attn(char* __restrict__ ws) {
    extern __shared__ __attribute__((aligned(16))) char smem[];
    LAS char* lds = (LAS char*)smem;
    const int tid = threadIdx.x, lane = tid & 63, wid = __builtin_amdgcn_readfirstlane(tid >> 6);
    const int r = lane & 31, h = lane >> 5;
    const int g = blockIdx.x, xcd = g & 7, idx = g >> 3;
    const int head = xcd * 4 + (idx >> 3), x = idx & 7;
    const char* kvb = ws + WS_KVB + (size_t)head * 1048576;
    const char* kva = ws + WS_KVA + (size_t)head * 262144;
    const int n1 = 32 - 2 * x;
    const unsigned lbase = (unsigned)(uintptr_t)lds + lane * 16;

    STAMP(1, 0);
    const unsigned lds0 = (unsigned)(uintptr_t)lds;
    const unsigned voff = lane * 16;
    const int wa = wid & 3;
    const char* sbB = kvb;
    const char* sbA = kva;
#define K2_M0(tt)                                                                           \
    do {                                                                                    \
        const int t3_ = (tt) < NIT ? (tt) : NIT - 1;                                        \
        const int tb3_ = t3_ < n1 ? t3_ : t3_ - n1;                                         \
        const int ta3_ = t3_ < 32 ? t3_ : 31;                                               \
        sbB = kvb + (size_t)tb3_ * 32768 + wid * 4096 + 2048;                               \
        sbA = kva + (size_t)ta3_ * 8192 + wid * 1024 - 2048;                                \
        set_m0(lds0 + ((tt) < NIT ? ((tt) & 3) : 2 + ((tt) & 1)) * SLOT + wid * 5120 + 2048); \
    } while (0)
#define K2_DMA(i) do { SBAR(); if constexpr ((i) < 4) dma_s<(i) * 1024 - 2048>(voff, sbB); else dma_s<2048>(voff, sbA); SBAR(); } while (0)
#define K2_STAGE_ALL(tt) do { K2_M0(tt); K2_DMA(0); K2_DMA(1); K2_DMA(2); K2_DMA(3); K2_DMA(4); } while (0)
#define BOFF(f) (((f) >> 2) * 5120 + ((f) & 3) * 1024)
#define AOFF(a) ((a) * 5120 + 4096)
    K2_STAGE_ALL(0);
    K2_STAGE_ALL(1);

#define K2_SYNC(it) do { WAIT_VM(5); BARRIER(); K2_M0((it) + 3); } while (0)

    if (wid < 4) {
        bf16x8 fr[8];
        K2_SYNC(-1);
        STAMP(1, 1);
        sfor<0, 8>([&](auto ic) { constexpr int j = decltype(ic)::value; lds_rd<BOFF((j & 1) * 8 + (j >> 1))>(fr[j], lbase); });
        K2_DMA(0); K2_DMA(1);
        int it = 0;
        for (int blk = 0; blk < 2; ++blk) {
            const int qb = blk == 0 ? 15 - x : x;
            const int nt = 2 * qb + 2;
            const int q0 = qb * 128 + wid * 32;
            const int tb32 = q0 >> 5;
            bf16x8 qf[8];
            const char* qp = ws + WS_QB + (size_t)((head * 64 + tb32) * 8) * 1024 + lane * 16;
#pragma unroll
            for (int ks = 0; ks < 8; ++ks) qf[ks] = *(const bf16x8*)(qp + ks * 1024);
            f32x16 o[4];
#pragma unroll
            for (int db = 0; db < 4; ++db) o[db] = f32x16{};
            f32x16 negm = f32x16{};
            float l = 0.f;
            for (int t = 0; t < nt; ++t, ++it) {
                const int rel = (q0 - 64 * t) >> 5;
                const unsigned ac = lbase + (it & 3) * SLOT;
                const unsigned an = lbase + ((it + 1) & 3) * SLOT;
                f32x16 s0, s1;
                SEG(0);
                sfor<0, 16>([&](auto ic) {
                    constexpr int j = decltype(ic)::value, ks = j >> 1;
                    lds_wait<7>(fr[j & 7]);
                    if constexpr (j == 0) s0 = MFMA32(fr[0], qf[0], negm);
                    else if constexpr (j == 1) s1 = MFMA32(fr[1], qf[0], negm);
                    else if constexpr ((j & 1) == 0) s0 = MFMA32(fr[j & 7], qf[ks], s0);
                    else s1 = MFMA32(fr[j & 7], qf[ks], s1);
                    constexpr int jn = j + 8;
                    SBAR();
                    if constexpr (jn < 16) lds_rd<BOFF((jn & 1) * 8 + (jn >> 1))>(fr[j & 7], ac);
                    else lds_rd<BOFF(jn)>(fr[j & 7], ac);
                    if constexpr (j == 2) K2_DMA(2);
                    if constexpr (j == 8) K2_DMA(3);
                    if constexpr (j == 14) K2_DMA(4);
                    SBAR();
                });
#define B_MASK()                                                                                        \
    do {                                                                                                \
        if (rel <= 1) {                                                                                 \
            const int lim = 32 * rel + r - 4 * h;                                                       \
            _Pragma("unroll") for (int i = 0; i < 16; ++i) {                                            \
                const int kr = (i & 3) + 8 * (i >> 2);                                                  \
                if (kr > lim) s0[i] = -__builtin_inff();                                                \
                if (kr + 32 > lim) s1[i] = -__builtin_inff();                                           \
            }                                                                                           \
        }                                                                                               \
    } while (0)
                SEG(1);
                B_MASK();
                float ps = 0.f;
                if (t > 0) {
#pragma unroll
                    for (int i = 0; i < 16; ++i) { s0[i] = __builtin_amdgcn_exp2f(s0[i]); ps += s0[i]; }
#pragma unroll
                    for (int i = 0; i < 16; ++i) { s1[i] = __builtin_amdgcn_exp2f(s1[i]); ps += s1[i]; }
                }
                if (t == 0 || __any(!(ps <= 65536.f))) {
                    if (t > 0) {
                        LAS char* sl = lds + (it & 3) * SLOT + lane * 16;
                        s0 = negm; s1 = negm;
#pragma unroll 1
                        for (int ks = 0; ks < 8; ++ks) {
                            bf16x8 qq = qf[0];
#pragma unroll
                            for (int u = 1; u < 8; ++u) qq = (ks == u) ? qf[u] : qq;
                            s0 = MFMA32(*(LAS bf16x8*)(sl + (ks >> 2) * 5120 + (ks & 3) * 1024), qq, s0);
                            s1 = MFMA32(*(LAS bf16x8*)(sl + 10240 + (ks >> 2) * 5120 + (ks & 3) * 1024), qq, s1);
                        }
                        B_MASK();
                    }
                    float mx = s0[0];
#pragma unroll
                    for (int i = 1; i < 16; ++i) mx = fmaxf(mx, s0[i]);
#pragma unroll
                    for (int i = 0; i < 16; ++i) mx = fmaxf(mx, s1[i]);
                    auto rr = __builtin_amdgcn_permlane32_swap(__float_as_uint(mx), __float_as_uint(mx), false, false);
                    mx = fmaxf(__uint_as_float(rr[0]), __uint_as_float(rr[1]));
                    float delta = mx;
                    if (t > 0) {
                        delta = fmaxf(mx, 0.f);
                        const float alpha = __builtin_amdgcn_exp2f(-delta);
                        l *= alpha;
#pragma unroll
                        for (int db = 0; db < 4; ++db)
#pragma unroll
                            for (int i = 0; i < 16; ++i) o[db][i] *= alpha;
                    }
                    const float nm = negm[0] - delta;
#pragma unroll
                    for (int i = 0; i < 16; ++i) negm[i] = nm;
                    ps = 0.f;
#pragma unroll
                    for (int i = 0; i < 16; ++i) { s0[i] = __builtin_amdgcn_exp2f(s0[i] - delta); ps += s0[i]; }
#pragma unroll
                    for (int i = 0; i < 16; ++i) { s1[i] = __builtin_amdgcn_exp2f(s1[i] - delta); ps += s1[i]; }
                }
                l += ps;
                bf16x8 pf[4];
                pf[0] = __builtin_bit_cast(bf16x8, pack8(s0, 0));
                pf[1] = __builtin_bit_cast(bf16x8, pack8(s0, 1));
                pf[2] = __builtin_bit_cast(bf16x8, pack8(s1, 0));
                pf[3] = __builtin_bit_cast(bf16x8, pack8(s1, 1));
                SEG(2);
                if (it + 1 < NIT) { K2_SYNC(it); } else { K2_M0(it + 3); }
                SEG(3);
                sfor<0, 16>([&](auto ic) {
                    constexpr int j = decltype(ic)::value, s4 = j >> 2, db = j & 3;
                    lds_wait<7>(fr[j & 7]);
                    o[db] = MFMA32(fr[j & 7], pf[s4], o[db]);
                    SBAR();
                    if constexpr (j < 8) lds_rd<BOFF(24 + j)>(fr[j & 7], ac);
                    else { constexpr int jn = j - 8; lds_rd<BOFF((jn & 1) * 8 + (jn >> 1))>(fr[j & 7], an); }
                    if constexpr (j == 4) K2_DMA(0);
                    if constexpr (j == 10) K2_DMA(1);
                    SBAR();
                });
                SEG(4);
            }
            auto rr = __builtin_amdgcn_permlane32_swap(__float_as_uint(l), __float_as_uint(l), false, false);
            const float inv = 1.0f / (__uint_as_float(rr[0]) + __uint_as_float(rr[1]));
            char* cp = ws + WS_CTXB + (size_t)(tb32 * 256 + head * 8) * 1024 + lane * 16;
#pragma unroll
            for (int db = 0; db < 4; ++db) {
                st16(cp + (2 * db) * 1024, pack8s(o[db], 0, inv));
                st16(cp + (2 * db + 1) * 1024, pack8s(o[db], 1, inv));
            }
        }
        WAIT_VM(0);
        STAMP(1, 2);
#undef B_MASK
    } else {
        const int tb32 = x * 8 + wa * 2;
        bf16x8 qf[2][2];
        const char* qp = ws + WS_QA + (size_t)((head * 64 + tb32) * 2) * 1024 + lane * 16;
#pragma unroll
        for (int qt = 0; qt < 2; ++qt)
#pragma unroll
            for (int ks = 0; ks < 2; ++ks) qf[qt][ks] = *(const bf16x8*)(qp + (qt * 2 + ks) * 1024);
        f32x16 o[2];
        o[0] = f32x16{}; o[1] = f32x16{};
        float negm[2] = {0.f, 0.f};
        bf16x8 kf[4], vf[4];
        f32x16 s[2][2];
        K2_SYNC(-1);
        lds_rd<AOFF(0)>(kf[0], lbase); lds_rd<AOFF(1)>(kf[1], lbase); lds_rd<AOFF(2)>(kf[2], lbase); lds_rd<AOFF(3)>(kf[3], lbase);
        for (int it = 0; it <= 32; ++it) {
            SEG(0);
            if (it > 0) {
                u32x4 pf[2][2][2];
                unsigned chk = 0;
                sfor<0, 4>([&](auto bc) {
                    constexpr int blk = decltype(bc)::value, kb = blk >> 1, qt = blk & 1;
                    f32x16 e;
#pragma unroll
                    for (int i = 0; i < 8; ++i) e[i] = __builtin_amdgcn_exp2f(s[kb][qt][i]);
                    K2_DMA(blk);
#pragma unroll
                    for (int i = 8; i < 16; ++i) e[i] = __builtin_amdgcn_exp2f(s[kb][qt][i]);
                    if constexpr (blk == 3) K2_DMA(4);
                    pf[kb][qt][0] = pack8(e, 0);
                    pf[kb][qt][1] = pack8(e, 1);
                    chk |= pf[kb][qt][0][0] | pf[kb][qt][0][1] | pf[kb][qt][0][2] | pf[kb][qt][0][3];
                    chk |= pf[kb][qt][1][0] | pf[kb][qt][1][1] | pf[kb][qt][1][2] | pf[kb][qt][1][3];
                });
                const bool first = (it == 1);
                if (first || __any((chk & 0x40004000u) != 0u)) {
#pragma unroll
                    for (int qt = 0; qt < 2; ++qt) {
                        float mx = s[0][qt][0];
#pragma unroll
                        for (int i = 1; i < 16; ++i) mx = fmaxf(mx, s[0][qt][i]);
#pragma unroll
                        for (int i = 0; i < 16; ++i) mx = fmaxf(mx, s[1][qt][i]);
                        auto rr = __builtin_amdgcn_permlane32_swap(__float_as_uint(mx), __float_as_uint(mx), false, false);
                        mx = fmaxf(__uint_as_float(rr[0]), __uint_as_float(rr[1]));
                        float delta = mx + 10.f;
                        if (!first) delta = fmaxf(delta, 0.f);
                        const float tgt = negm[qt] - delta;
                        const unsigned hi = pack2(tgt, 0.f) & 0xFFFFu;
                        const float hif = __uint_as_float(hi << 16);
                        const unsigned lo = pack2(tgt - hif, 0.f) & 0xFFFFu;
                        const float neff = hif + __uint_as_float(lo << 16);
                        delta = negm[qt] - neff;
                        negm[qt] = neff;
                        {
                            u32x4 qq = __builtin_bit_cast(u32x4, qf[qt][1]);
                            qq[2] = h ? 0u : (hi | (lo << 16));
                            qf[qt][1] = __builtin_bit_cast(bf16x8, qq);
                        }
                        if (!first) {
                            const float alpha = __builtin_amdgcn_exp2f(-delta);
#pragma unroll
                            for (int i = 0; i < 16; ++i) o[qt][i] *= alpha;
                        }
#pragma unroll
                        for (int kb = 0; kb < 2; ++kb) {
                            f32x16 e;
#pragma unroll
                            for (int i = 0; i < 16; ++i) e[i] = __builtin_amdgcn_exp2f(s[kb][qt][i] - delta);
                            pf[kb][qt][0] = pack8(e, 0);
                            pf[kb][qt][1] = pack8(e, 1);
                        }
                    }
                }
                SEG(1);
                lds_wait2<4>(vf[0], vf[1]);
                lds_wait2<4>(vf[2], vf[3]);
#pragma unroll
                for (int kb = 0; kb < 2; ++kb)
#pragma unroll
                    for (int s2 = 0; s2 < 2; ++s2)
#pragma unroll
                        for (int qt = 0; qt < 2; ++qt)
                            o[qt] = MFMA32(vf[kb * 2 + s2], __builtin_bit_cast(bf16x8, pf[kb][qt][s2]), o[qt]);
            } else {
                K2_DMA(0); K2_DMA(1); K2_DMA(2); K2_DMA(3); K2_DMA(4);
            }
            SEG(2);
            lds_wait2<0>(kf[0], kf[1]);
            lds_wait2<0>(kf[2], kf[3]);
#pragma unroll
            for (int kb = 0; kb < 2; ++kb)
#pragma unroll
                for (int qt = 0; qt < 2; ++qt) {
                    s[kb][qt] = MFMA32(kf[kb * 2], qf[qt][0], f32x16{});
                    s[kb][qt] = MFMA32(kf[kb * 2 + 1], qf[qt][1], s[kb][qt]);
                }
            SBAR();
            if (it == 32) STAMPW(1, 3, 4);
            SEG(3);
            K2_SYNC(it);
            SEG(4);
            const unsigned ac = lbase + (it & 3) * SLOT;
            const unsigned an = lbase + ((it + 1) & 3) * SLOT;
            lds_rd<AOFF(4)>(vf[0], ac); lds_rd<AOFF(5)>(vf[1], ac); lds_rd<AOFF(6)>(vf[2], ac); lds_rd<AOFF(7)>(vf[3], ac);
            lds_rd<AOFF(0)>(kf[0], an); lds_rd<AOFF(1)>(kf[1], an); lds_rd<AOFF(2)>(kf[2], an); lds_rd<AOFF(3)>(kf[3], an);
        }
        WAIT_VM(0);
#pragma unroll
        for (int qt = 0; qt < 2; ++qt) {
            const unsigned lv = __float_as_uint(o[qt][8]);
            auto rr = __builtin_amdgcn_permlane32_swap(lv, lv, false, false);
            const float lsum = __uint_as_float(rr[1]);
            const float inv = 1.0f / lsum;
            char* cp = ws + WS_CTXA + (size_t)((tb32 + qt) * 40) * 1024;
#pragma unroll
            for (int gq = 0; gq < 3; ++gq) {
                const int d0 = 8 * gq + 4 * h;
                if (d0 < 20) {
                    const int k = head * 20 + d0;
                    u32x2 w;
                    w[0] = pack2(o[qt][4 * gq + 0] * inv, o[qt][4 * gq + 1] * inv);
                    w[1] = pack2(o[qt][4 * gq + 2] * inv, o[qt][4 * gq + 3] * inv);
                    *(u32x2*)(cp + (k >> 4) * 1024 + (r + 32 * ((k >> 3) & 1)) * 16 + (k & 4) * 2) = w;
                }
            }
        }
    }
}

constexpr int G3_STAGE = 49152;
constexpr int G3_LDS = 2 * G3_STAGE;

__global__ __launch_bounds__(512, 2) void k3_oproj(const __hip_bfloat16* __restrict__ Awo, const __hip_bfloat16* __restrict__ Bwo,
                                                   char* __restrict__ ws) {
    extern __shared__ __attribute__((aligned(16))) char smem[];
    LAS char* lds = (LAS char*)smem;
    const int tid = threadIdx.x, lane = tid & 63, wid = __builtin_amdgcn_readfirstlane(tid >> 6);
    const int wm = wid >> 1, wn = wid & 1, h = lane >> 5;
    const int bid = blockIdx.x;
    const bool isB = bid < 200;
    int mt, nt, sp, kt0, nkt, kstot;
    const char* Xc; const char* Wb;
    if (isB) {
        mt = bid & 7; const int rest = bid >> 3; nt = rest % 5; sp = rest / 5;
        kt0 = sp * 13; nkt = sp == 4 ? 12 : 13; kstot = 256;
        Xc = ws + WS_CTXB; Wb = (const char*)Bwo;
    } else {
        const int b2 = bid - 200; mt = b2 & 7; nt = b2 >> 3; sp = 5;
        kt0 = 0; nkt = 10; kstot = 40;
        Xc = ws + WS_CTXA; Wb = (const char*)Awo;
    }
    unsigned woff[2];
#pragma unroll
    for (int i = 0; i < 2; ++i) {
        const int k = (wid * 2 + i) * 4 + (lane >> 4);
        const int p = lane & 15, b = (p >> 2) ^ (k & 3);
        woff[i] = (unsigned)(((kt0 * 64 + k) * 640 + nt * 128 + (b * 4 + (p & 3)) * 8) * 2);
    }
    const unsigned lds0 = (unsigned)(uintptr_t)lds;
    const char* xsb = Xc + (size_t)((mt * 8 + wid) * kstot + kt0 * 4) * 1024 + 4096;
    const unsigned voff = lane * 16;
    woff[1] -= 1024;
    auto piece = [&](int kt, int buf, auto ic) {
        constexpr int i = decltype(ic)::value;
        if constexpr (i == 0) set_m0(lds0 + buf * G3_STAGE + wid * 6144 + 4096);
        if constexpr (i < 4) dma_s<i * 1024 - 4096>(voff, xsb + (size_t)kt * 4096);
        else dma_s<(i - 4) * 1024>(woff[i - 4], Wb + (size_t)kt * (64u * 640u * 2u));
    };
    auto stage = [&](int kt, int buf) { sfor<0, 6>([&](auto ic) { piece(kt, buf, ic); }); };
    const int q = (lane >> 2) & 3, p4 = lane & 3, g16 = (lane >> 4) & 1;
    const int r0 = isB ? 4 * h : 8 * h, r1 = isB ? 8 + 4 * h : 8 * h + 4;
    int wb0[2], wb1[2];
#pragma unroll
    for (int ni = 0; ni < 2; ++ni) {
        const int b = wn * 2 + ni;
        const int c = ((b ^ q) * 64) + g16 * 32 + p4 * 8;
        wb0[ni] = (r0 >> 3) * 6144 + 4096 + ((r0 & 7) + q) * 256 + c;
        wb1[ni] = (r1 >> 3) * 6144 + 4096 + ((r1 & 7) + q) * 256 + c;
    }
    f32x16 acc[2][2];
#pragma unroll
    for (int mi = 0; mi < 2; ++mi)
#pragma unroll
        for (int ni = 0; ni < 2; ++ni) acc[mi][ni] = f32x16{};

    bf16x8 xf[2][2], wf[2][2];
#define G3_RD(set, bb, ks)                                                                                       \
    do {                                                                                                         \
        _Pragma("unroll") for (int ni = 0; ni < 2; ++ni)                                                         \
            wf[set][ni] = tr_frag((bb) + wb0[ni] + (ks) * 12288, (bb) + wb1[ni] + (ks) * 12288);                 \
        _Pragma("unroll") for (int mi = 0; mi < 2; ++mi)                                                         \
            xf[set][mi] = *(LAS bf16x8*)((bb) + (wm * 2 + mi) * 6144 + (ks) * 1024 + lane * 16);                 \
    } while (0)
#define G3_MM(set, kn, bn_, p0)                                                                                  \
    sfor<0, 2>([&](auto mc) {                                                                                    \
        constexpr int mi = decltype(mc)::value;                                                                  \
        _Pragma("unroll") for (int ni = 0; ni < 2; ++ni) acc[mi][ni] = MFMA32(wf[set][ni], xf[set][mi], acc[mi][ni]); \
        SBAR(); if ((kn) < nkt) piece((kn), (bn_), std::integral_constant<int, (p0) + mi>{}); SBAR();            \
    })
    STAMP(2, 0);
    stage(0, 0);
    stage(1, 1);
    WAIT_VM(6);
    BARRIER();
    STAMP(2, 1);
    G3_RD(0, lds, 0);
#pragma unroll 1
    for (int kt = 0; kt < nkt; ++kt) {
        LAS char* bb = lds + (kt & 1) * G3_STAGE;
        LAS char* bn = lds + ((kt + 1) & 1) * G3_STAGE;
        const int k1 = kt >= 1 ? kt + 1 : nkt;
        LGKM0(); G3_RD(1, bb, 1); SBAR(); G3_MM(0, k1, (kt + 1) & 1, 2); SBAR();
        LGKM0(); G3_RD(0, bb, 2); SBAR(); G3_MM(1, k1, (kt + 1) & 1, 4); SBAR();
        LGKM0(); G3_RD(1, bb, 3); SBAR(); G3_MM(0, nkt, 0, 0); SBAR();
        LGKM0(); WAIT_VM(0);
        BARRIER();
        if (kt + 1 < nkt) G3_RD(0, bn, 0);
        SBAR(); G3_MM(1, kt + 2, kt & 1, 0); SBAR();
    }
#undef G3_RD
#undef G3_MM
    STAMP(2, 2);
#pragma unroll
    for (int mi = 0; mi < 2; ++mi)
#pragma unroll
        for (int ni = 0; ni < 2; ++ni) {
            const int mt32 = mt * 8 + wm * 2 + mi, nt32 = nt * 4 + wn * 2 + ni;
            char* dst = ws + WS_PART + (size_t)sp * SLAB + (size_t)(mt32 * 20 + nt32) * 4096 + lane * 16;
#pragma unroll
            for (int gq = 0; gq < 4; ++gq) {
                f32x4 v = {acc[mi][ni][4 * gq], acc[mi][ni][4 * gq + 1], acc[mi][ni][4 * gq + 2], acc[mi][ni][4 * gq + 3]};
                st16(dst + gq * 1024, __builtin_bit_cast(u32x4, v));
            }
        }
    STAMP(2, 3);
}

__global__ __launch_bounds__(256) void k4_reduce(const char* __restrict__ ws, __hip_bfloat16* __restrict__ out) {
    const int lane = threadIdx.x & 63;
    const int w = blockIdx.x * 4 + (threadIdx.x >> 6);
    const int sub = w & 3, t = w >> 2;
    const int mt32 = t / 40, ct = t - mt32 * 40;
    const bool isB = ct >= 20;
    const int nt32 = isB ? ct - 20 : ct;
    const int ql = sub * 8 + (lane & 7), c8 = lane >> 3;
    const char* src = ws + WS_PART + (size_t)(mt32 * 20 + nt32) * 4096 + (c8 >> 1) * 1024 + (ql + 32 * (c8 & 1)) * 16;
    f32x4 v;
    if (isB) {
        v = *(const f32x4*)(src);
#pragma unroll
        for (int s = 1; s < NSPLIT; ++s) v += *(const f32x4*)(src + (size_t)s * SLAB);
    } else {
        v = *(const f32x4*)(src + (size_t)5 * SLAB);
    }
    u32x2 o;
    o[0] = pack2(v[0], v[1]);
    o[1] = pack2(v[2], v[3]);
    *(u32x2*)((char*)out + (size_t)(mt32 * 32 + ql) * 2560 + (ct * 32 + c8 * 4) * 2) = o;
}

extern "C" void kernel_launch(void* const* d_in, const int* in_sizes, int n_in,
                              void* d_out, int out_size, void* d_ws, size_t ws_size,
                              hipStream_t stream) {
    (void)in_sizes; (void)n_in; (void)out_size; (void)ws_size;
    const __hip_bfloat16* Ax = (const __hip_bfloat16*)d_in[0];
    const __hip_bfloat16* Awqkv = (const __hip_bfloat16*)d_in[1];
    const __hip_bfloat16* Awo = (const __hip_bfloat16*)d_in[2];
    const __hip_bfloat16* Bx = (const __hip_bfloat16*)d_in[3];
    const __hip_bfloat16* Bckv = (const __hip_bfloat16*)d_in[4];
    const __hip_bfloat16* Bwq = (const __hip_bfloat16*)d_in[5];
    const __hip_bfloat16* Bwuk = (const __hip_bfloat16*)d_in[6];
    const __hip_bfloat16* Bwuv = (const __hip_bfloat16*)d_in[7];
    const __hip_bfloat16* Bwo = (const __hip_bfloat16*)d_in[8];
    char* ws = (char*)d_ws;
    (void)hipFuncSetAttribute((const void*)k1_proj, hipFuncAttributeMaxDynamicSharedMemorySize, G1_LDS);
    (void)hipFuncSetAttribute((const void*)k2_attn, hipFuncAttributeMaxDynamicSharedMemorySize, K2_LDS);
    (void)hipFuncSetAttribute((const void*)k3_oproj, hipFuncAttributeMaxDynamicSharedMemorySize, G3_LDS);
    k1_proj<<<256, 512, G1_LDS, stream>>>(Ax, Awqkv, Bx, Bckv, Bwq, Bwuk, Bwuv, ws);
    k2_attn<<<256, 512, K2_LDS, stream>>>(ws);
    k3_oproj<<<240, 512, G3_LDS, stream>>>(Awo, Bwo, ws);
    k4_reduce<<<64 * 40, 256, 0, stream>>>(ws, (__hip_bfloat16*)d_out);
}
```

```cpp
#include <hip/hip_runtime.h>
#include <hip/hip_bf16.h>
#include <stdint.h>
#include <type_traits>

typedef short s16x4 __attribute__((ext_vector_type(4)));
typedef short bf16x8 __attribute__((ext_vector_type(8)));
typedef float f32x16 __attribute__((ext_vector_type(16)));
typedef float f32x4 __attribute__((ext_vector_type(4)));
typedef float f32x2 __attribute__((ext_vector_type(2)));
typedef unsigned u32x4 __attribute__((ext_vector_type(4)));
typedef unsigned u32x2 __attribute__((ext_vector_type(2)));
typedef __bf16 bf16x2_t __attribute__((ext_vector_type(2)));
#define LAS __attribute__((address_space(3)))
#define DI __device__ __forceinline__
#define MFMA32(a, b, c) __builtin_amdgcn_mfma_f32_32x32x16_bf16((a), (b), (c), 0, 0, 0)
#define CFENCE() asm volatile("" ::: "memory")
#define BARRIER() do { CFENCE(); __builtin_amdgcn_s_barrier(); CFENCE(); } while (0)
#define WAIT_VM(n) asm volatile("s_waitcnt vmcnt(" #n ") lgkmcnt(0)" ::: "memory")
#define SBAR() __builtin_amdgcn_sched_barrier(0)

DI unsigned pack2(float a, float b) {
    f32x2 v = {a, b};
    bf16x2_t r = __builtin_convertvector(v, bf16x2_t);
    return __builtin_bit_cast(unsigned, r);
}
DI u32x4 pack8(const f32x16& x, int s) {
    u32x4 w;
    w[0] = pack2(x[8 * s + 0], x[8 * s + 1]);
    w[1] = pack2(x[8 * s + 2], x[8 * s + 3]);
    w[2] = pack2(x[8 * s + 4], x[8 * s + 5]);
    w[3] = pack2(x[8 * s + 6], x[8 * s + 7]);
    return w;
}
DI u32x4 pack8s(const f32x16& x, int s, float c) {
    u32x4 w;
    w[0] = pack2(x[8 * s + 0] * c, x[8 * s + 1] * c);
    w[1] = pack2(x[8 * s + 2] * c, x[8 * s + 3] * c);
    w[2] = pack2(x[8 * s + 4] * c, x[8 * s + 5] * c);
    w[3] = pack2(x[8 * s + 6] * c, x[8 * s + 7] * c);
    return w;
}
DI void glds16(const void* g, LAS char* l) {
    __builtin_amdgcn_global_load_lds((const unsigned*)g, (LAS unsigned*)l, 16, 0, 0);
}
DI bf16x8 tr_frag(LAS char* p0, LAS char* p1) {
    s16x4 lo = __builtin_amdgcn_ds_read_tr16_b64_v4i16((LAS s16x4*)p0);
    s16x4 hi = __builtin_amdgcn_ds_read_tr16_b64_v4i16((LAS s16x4*)p1);
    return __builtin_shufflevector(lo, hi, 0, 1, 2, 3, 4, 5, 6, 7);
}

#define STMODE 2
DI void st16(void* p, u32x4 v) {
    asm volatile("global_store_dwordx4 %0, %1, off sc1\n\ts_nop 1" :: "v"(p), "v"(v) : "memory");
}
template <int I, int N, class F> DI void sfor(F&& f) {
    if constexpr (I < N) { f(std::integral_constant<int, I>{}); sfor<I + 1, N>(f); }
}
template <int OFF> DI void lds_rd(bf16x8& d, unsigned addr) {
    asm volatile("ds_read_b128 %0, %1 offset:%2" : "=v"(d) : "v"(addr), "i"(OFF));
}
template <int N> DI void lds_wait(bf16x8& d) { asm volatile("s_waitcnt lgkmcnt(%1)" : "+v"(d) : "i"(N)); }
template <int N> DI void lds_wait2(bf16x8& d, bf16x8& e) { asm volatile("s_waitcnt lgkmcnt(%2)" : "+v"(d), "+v"(e) : "i"(N)); }
#define LGKM0() __builtin_amdgcn_s_waitcnt(0xC07F)
DI void set_m0(unsigned v) { asm volatile("s_mov_b32 m0, %0\n\ts_nop 0" :: "s"(v) : "memory"); }
template <int OFF> DI void dma_s(unsigned voff, const char* sbase) {
    asm volatile("global_load_lds_dwordx4 %0, %1 offset:%2" :: "v"(voff), "s"(sbase), "i"(OFF) : "memory");
}

#define ABL_NOPIECE 0
#define ABL_NOBAR 0
#define ABL_T0 0
#define ABL_K2 0
#define ABL_NODMA 0
#define ABL_NOMFMA 0
#define SEG(i) do { } while (0)
#define STAMP(kid, idx) do { } while (0)
#define STAMPW(kid, idx, w) do { } while (0)
constexpr size_t WS_KVB = 0;
constexpr size_t WS_QB = WS_KVB + (size_t)32 * 1048576;
constexpr size_t WS_KVA = WS_QB + (size_t)16 * 1048576;
constexpr size_t WS_QA = WS_KVA + (size_t)8 * 1048576;
constexpr size_t WS_CTXB = WS_QA + (size_t)4 * 1048576;
constexpr size_t WS_CTXA = WS_CTXB + (size_t)16 * 1048576;
constexpr size_t WS_PART = WS_CTXA + (size_t)4 * 1048576;
constexpr int NSPLIT = 5;
constexpr size_t SLAB = (size_t)64 * 20 * 4096;

constexpr float CB = 0.08838834764831845f * 1.4426950408889634f;
constexpr float CA = 0.22360679774997896f * 1.4426950408889634f;

#define PREFETCH 1
constexpr int G1_LDS = 2 * 65536 + 2048;
enum { EP_BK = 0, EP_BV = 1, EP_BQ = 2, EP_AQK = 3, EP_AV = 4 };

struct G1Src { unsigned xoff[4], woff[4]; const char* Xb; const char* Wb; unsigned wstep; int nkt; };
DI void g1_src(G1Src& s, const __hip_bfloat16* X, const __hip_bfloat16* W, int K, int ldw, int m0, int n0, int nlim, int wid, int lane) {
#pragma unroll
    for (int i = 0; i < 4; ++i) {
        const int row = (wid * 4 + i) * 8 + (lane >> 3);
        const int c = (lane & 7) ^ ((row >> 1) & 7);
        s.xoff[i] = (unsigned)(((m0 + row) * K + c * 8) * 2) + 4096 - i * 1024;
        const int k = (wid * 4 + i) * 2 + (lane >> 5);
        const int p = lane & 31, b = (p >> 2) ^ (k & 3);
        int col = n0 + (b * 4 + (p & 3)) * 8;
        col = col < nlim ? col : nlim - 8;
        s.woff[i] = (unsigned)((k * ldw + col) * 2) - i * 1024;
    }
    s.Xb = (const char*)X; s.Wb = (const char*)W; s.wstep = (unsigned)(64 * ldw * 2); s.nkt = K >> 6;
}
template <int I> DI void g1_piece(const G1Src& s, int kt) {
    if constexpr (I < 4) dma_s<I * 1024 - 4096>(s.xoff[I], s.Xb + (size_t)kt * 128);
    else dma_s<(I - 4) * 1024>(s.woff[I - 4], s.Wb + (size_t)kt * s.wstep);
}

template <bool TR>
DI void g1_loop(f32x16 (&acc)[4][2], bf16x8 (&xf)[2][4], bf16x8 (&wf)[2][2], const G1Src& cur, const G1Src& nxt, bool has_next, bool first,
                LAS char* lds, int wid, int lane) {
    const int wm = wid >> 2, wn = wid & 3, r = lane & 31, h = lane >> 5;
    const unsigned lds0 = (unsigned)(uintptr_t)lds;
    const int nkt = cur.nkt;
    const int xsw = (r >> 1) & 7;
    const int xbase = wm * 4 * 8192 + r * 128;
    const int q = (lane >> 2) & 3, p4 = lane & 3, g16 = (lane >> 4) & 1;
    int wbase[2];
#pragma unroll
    for (int ni = 0; ni < 2; ++ni) {
        const int b = wn * 2 + ni;
        wbase[ni] = h * 8192 + 4096 + q * 512 + ((b ^ q) * 64) + g16 * 32 + p4 * 8;
    }
#pragma unroll
    for (int mi = 0; mi < 4; ++mi)
#pragma unroll
        for (int ni = 0; ni < 2; ++ni) acc[mi][ni] = f32x16{};
#define G1_RD(set, bb, ks)                                                                                       \
    do {                                                                                                         \
        const int xc_ = ((2 * (ks) + h) ^ xsw) * 16;                                                             \
        _Pragma("unroll") for (int ni = 0; ni < 2; ++ni)                                                         \
            wf[set][ni] = tr_frag((bb) + wbase[ni] + (ks) * 16384, (bb) + wbase[ni] + (ks) * 16384 + 2048);      \
        _Pragma("unroll") for (int mi = 0; mi < 4; ++mi) xf[set][mi] = *(LAS bf16x8*)((bb) + xbase + xc_ + mi * 8192); \
    } while (0)
#define G1_MM(set, on, kn, p0, pe)                                                                               \
    sfor<0, 4>([&](auto mc) {                                                                                    \
        constexpr int mi = decltype(mc)::value;                                                                  \
        _Pragma("unroll") for (int ni = 0; ni < 2; ++ni)                                                         \
            acc[mi][ni] = TR ? MFMA32(wf[set][ni], xf[set][mi], acc[mi][ni]) : MFMA32(xf[set][mi], wf[set][ni], acc[mi][ni]); \
        if constexpr ((p0) + mi < (pe)) {                                                                        \
            SBAR();                                                                                              \
            if (on) { if ((kn) < nkt) g1_piece<(p0) + mi>(cur, (kn)); else g1_piece<(p0) + mi>(nxt, (kn) - nkt); } \
            SBAR();                                                                                              \
        }                                                                                                        \
    })
    if (first) G1_RD(0, lds, 0);
#pragma unroll 1
    for (int kt = 0; kt < nkt; ++kt) {
        LAS char* bb = lds + (kt & 1) * 65536;
        LAS char* bn = lds + ((kt + 1) & 1) * 65536;
        const bool on1 = !(first && kt == 0) && (kt + 1 < nkt || has_next);
        const bool on2 = kt + 2 < nkt || has_next;
        LGKM0(); G1_RD(1, bb, 1); SBAR(); G1_MM(0, on1, kt + 1, 4, 8); SBAR();
        LGKM0(); G1_RD(0, bb, 2); SBAR(); G1_MM(1, false, 0, 0, 0); SBAR();
        LGKM0(); G1_RD(1, bb, 3); SBAR(); G1_MM(0, false, 0, 0, 0); SBAR();
        LGKM0(); WAIT_VM(0);
        BARRIER();
        set_m0(lds0 + (kt & 1) * 65536 + wid * 8192 + 4096);
        if (kt + 1 < nkt || has_next) G1_RD(0, bn, 0);
        SBAR(); G1_MM(1, on2, kt + 2, 0, 4); SBAR();
    }
#undef G1_RD
#undef G1_MM
}

template <int EP>
DI void g1_epi(const f32x16 (&acc)[4][2], int m0, int n0, int nlim, int nsec, char* __restrict__ ws, int wid, int lane) {
    const int wm = wid >> 2, wn = wid & 3, r = lane & 31, h = lane >> 5;
#pragma unroll
    for (int mi = 0; mi < 4; ++mi) {
        const int tb = (m0 + wm * 128 + mi * 32) >> 5;
        const int tile = tb >> 1, kb = tb & 1;
#pragma unroll
        for (int ni = 0; ni < 2; ++ni) {
            const int nb = n0 + wn * 64 + ni * 32;
            if (nb >= nlim) continue;
            if constexpr (EP == EP_BK) {
                const int head = nb >> 7, db = (nb >> 5) & 3;
                char* dst = ws + WS_KVB + (size_t)head * 1048576 + tile * 32768 + (kb * 8 + db * 2) * 1024 + lane * 16;
                st16(dst, pack8(acc[mi][ni], 0));
                st16(dst + 1024, pack8(acc[mi][ni], 1));
            } else if constexpr (EP == EP_BV) {
                const int head = nb >> 7, db = (nb >> 5) & 3;
                char* dst = ws + WS_KVB + (size_t)head * 1048576 + tile * 32768 + 16384 + (kb * 8 + db) * 1024 + lane * 16;
                st16(dst, pack8(acc[mi][ni], 0));
                st16(dst + 4096, pack8(acc[mi][ni], 1));
            } else if constexpr (EP == EP_BQ) {
                const int head = nb >> 7, db = (nb >> 5) & 3;
                char* dst = ws + WS_QB + (size_t)((head * 64 + tb) * 8 + db * 2) * 1024 + lane * 16;
                st16(dst, pack8s(acc[mi][ni], 0, CB));
                st16(dst + 1024, pack8s(acc[mi][ni], 1, CB));
            } else if constexpr (EP == EP_AQK) {
                const bool isq = nb < 640;
                const float sc = isq ? CA : 1.0f;
#pragma unroll
                for (int g = 0; g < 4; ++g) {
                    const int n = nb - (isq ? 0 : 640) + 8 * g + 4 * h;
                    const int head = n / 20, d0 = n - head * 20;
                    char* base = isq ? ws + WS_QA + (size_t)((head * 64 + tb) * 2) * 1024
                                     : ws + WS_KVA + (size_t)head * 262144 + tile * 8192 + kb * 2048;
                    char* dst = base + (d0 >> 4) * 1024 + (r + 32 * ((d0 >> 3) & 1)) * 16 + (d0 & 4) * 2;
                    u32x2 w;
                    w[0] = pack2(acc[mi][ni][4 * g + 0] * sc, acc[mi][ni][4 * g + 1] * sc);
                    w[1] = pack2(acc[mi][ni][4 * g + 2] * sc, acc[mi][ni][4 * g + 3] * sc);
                    *(u32x2*)dst = w;
                    if (d0 == 16) {
                        *(u32x2*)(dst + 8) = (u32x2){isq ? 0u : 0x3F803F80u, 0u};
                        *(u32x4*)(base + 1024 + (r + 32) * 16) = (u32x4){0u, 0u, 0u, 0u};
                    }
                }
            } else {
                const int n = nb - nsec + r;
                const int head = n / 20, d = n - head * 20;
                char* dst = ws + WS_KVA + (size_t)head * 262144 + tile * 8192 + 4096 + kb * 2048 + (d + 32 * h) * 16;
                st16(dst, pack8(acc[mi][ni], 0));
                st16(dst + 1024, pack8(acc[mi][ni], 1));
                if (d == 19) {
                    const u32x4 one = {0x3F803F80u, 0x3F803F80u, 0x3F803F80u, 0x3F803F80u};
                    *(u32x4*)(dst + 16) = one;
                    *(u32x4*)(dst + 1024 + 16) = one;
                }
            }
        }
    }
}

__global__ __launch_bounds__(512, 2) void k1_proj(const __hip_bfloat16* __restrict__ Ax, const __hip_bfloat16* __restrict__ Awqkv,
                                                  const __hip_bfloat16* __restrict__ Bx, const __hip_bfloat16* __restrict__ Bckv,
                                                  const __hip_bfloat16* __restrict__ Bwq, const __hip_bfloat16* __restrict__ Bwuk,
                                                  const __hip_bfloat16* __restrict__ Bwuv, const __hip_bfloat16* __restrict__ Awo,
                                                  const __hip_bfloat16* __restrict__ Bwo, char* __restrict__ ws) {
    extern __shared__ __attribute__((aligned(16))) char smem[];
    LAS char* lds = (LAS char*)smem;
    const int tid = threadIdx.x, lane = tid & 63, wid = __builtin_amdgcn_readfirstlane(tid >> 6);
    const int bid = blockIdx.x;
    const int m0 = ((bid >> 3) & 7) * 256;
    const int c1 = (bid & 7) + 8 * (bid >> 6);
    const int c2 = c1;
    const bool has2 = c2 < 24;
    G1Src s1, s2;
    if (c1 < 16) g1_src(s1, Bckv, Bwuk, 512, 4096, m0, c1 * 256, 4096, wid, lane);
    else g1_src(s1, Bckv, Bwuv, 512, 4096, m0, (c1 - 16) * 256, 4096, wid, lane);
    const int n2 = c2 < 16 ? c2 * 256 : (c2 - 16) * 256;
    if (c2 < 16) g1_src(s2, Bx, Bwq, 640, 4096, m0, n2, 4096, wid, lane);
    else g1_src(s2, Ax, Awqkv, 640, 1920, m0, has2 ? n2 : 0, c2 < 21 ? 1280 : 1920, wid, lane);

    f32x16 acc[4][2];
    bf16x8 xf[2][4], wf[2][2];
    const unsigned lds0 = (unsigned)(uintptr_t)lds;
    STAMP(0, 0);
    set_m0(lds0 + wid * 8192 + 4096);
    sfor<0, 8>([&](auto ic) { g1_piece<decltype(ic)::value>(s1, 0); });
    set_m0(lds0 + 65536 + wid * 8192 + 4096);
    sfor<0, 8>([&](auto ic) { g1_piece<decltype(ic)::value>(s1, 1); });
    {
        set_m0(lds0 + 131072 + wid * 256);
        const unsigned gw = (unsigned)(bid * 8 + wid) * 64 + lane;
#define PF(ptr, nbytes) do { constexpr unsigned R_ = 131072u / ((nbytes) / 128u);                                     \
            const unsigned ln_ = gw / R_;                                                                                \
            const unsigned off_ = (gw % R_ == 0u && ln_ * 128u < (nbytes)) ? ln_ * 128u : 0u;                             \
            asm volatile("global_load_lds_dword %0, %1" :: "v"(off_), "s"((const char*)(ptr)) : "memory"); } while (0)
        PF(Bckv, 2048u * 512u * 2u); PF(Bwuk, 512u * 4096u * 2u); PF(Bwuv, 512u * 4096u * 2u);
        PF(Bx, 2048u * 640u * 2u); PF(Bwq, 640u * 4096u * 2u); PF(Ax, 2048u * 640u * 2u); PF(Awqkv, 640u * 1920u * 2u);
        PF(Bwo, 4096u * 640u * 2u); PF(Awo, 640u * 640u * 2u);
#undef PF
    }
    WAIT_VM(17);
    BARRIER();
    STAMP(0, 1);
    if (c1 < 16) { g1_loop<true>(acc, xf, wf, s1, s2, has2, true, lds, wid, lane); g1_epi<EP_BK>(acc, m0, c1 * 256, 4096, 0, ws, wid, lane); }
    else { g1_loop<false>(acc, xf, wf, s1, s2, has2, true, lds, wid, lane); g1_epi<EP_BV>(acc, m0, (c1 - 16) * 256, 4096, 0, ws, wid, lane); }
    STAMP(0, 2);
    if (c2 < 21) {
        g1_loop<true>(acc, xf, wf, s2, s2, false, false, lds, wid, lane);
        if (c2 < 16) g1_epi<EP_BQ>(acc, m0, n2, 4096, 0, ws, wid, lane);
        else g1_epi<EP_AQK>(acc, m0, n2, 1280, 0, ws, wid, lane);
    } else if (has2) {
        g1_loop<false>(acc, xf, wf, s2, s2, false, false, lds, wid, lane);
        g1_epi<EP_AV>(acc, m0, n2, 1920, 1280, ws, wid, lane);
    }
    STAMP(0, 3);
}

constexpr int SLOT = 40960;
constexpr int NSLOT = 4;
constexpr int K2_LDS = NSLOT * SLOT;
constexpr int NIT = 34;

__global__ __launch_bounds__(512, 2) void k2_attn(char* __restrict__ ws) {
    extern __shared__ __attribute__((aligned(16))) char smem[];
    LAS char* lds = (LAS char*)smem;
    const int tid = threadIdx.x, lane = tid & 63, wid = __builtin_amdgcn_readfirstlane(tid >> 6);
    const int r = lane & 31, h = lane >> 5;
    const int g = blockIdx.x, xcd = g & 7, idx = g >> 3;
    const int head = xcd * 4 + (idx >> 3), x = idx & 7;
    const char* kvb = ws + WS_KVB + (size_t)head * 1048576;
    const char* kva = ws + WS_KVA + (size_t)head * 262144;
    const int n1 = 32 - 2 * x;
    const unsigned lbase = (unsigned)(uintptr_t)lds + lane * 16;

    STAMP(1, 0);
    const unsigned lds0 = (unsigned)(uintptr_t)lds;
    const unsigned voff = lane * 16;
    const int wa = wid & 3;
    const char* sbB = kvb;
    const char* sbA = kva;
#define K2_M0(tt)                                                                           \
    do {                                                                                    \
        const int t3_ = (tt) < NIT ? (tt) : NIT - 1;                                        \
        const int tb3_ = t3_ < n1 ? t3_ : t3_ - n1;                                         \
        const int ta3_ = t3_ < 32 ? t3_ : 31;                                               \
        sbB = kvb + (size_t)tb3_ * 32768 + wid * 4096 + 2048;                               \
        sbA = kva + (size_t)ta3_ * 8192 + wid * 1024 - 2048;                                \
        set_m0(lds0 + ((tt) < NIT ? ((tt) & 3) : 2 + ((tt) & 1)) * SLOT + wid * 5120 + 2048); \
    } while (0)
#define K2_DMA(i) do { SBAR(); if constexpr ((i) < 4) dma_s<(i) * 1024 - 2048>(voff, sbB); else dma_s<2048>(voff, sbA); SBAR(); } while (0)
#define K2_STAGE_ALL(tt) do { K2_M0(tt); K2_DMA(0); K2_DMA(1); K2_DMA(2); K2_DMA(3); K2_DMA(4); } while (0)
#define BOFF(f) (((f) >> 2) * 5120 + ((f) & 3) * 1024)
#define AOFF(a) ((a) * 5120 + 4096)
    K2_STAGE_ALL(0);
    K2_STAGE_ALL(1);

#define K2_SYNC(it) do { WAIT_VM(5); BARRIER(); K2_M0((it) + 3); } while (0)

    if (wid < 4) {
        bf16x8 fr[8];
        K2_SYNC(-1);
        STAMP(1, 1);
        sfor<0, 8>([&](auto ic) { constexpr int j = decltype(ic)::value; lds_rd<BOFF((j & 1) * 8 + (j >> 1))>(fr[j], lbase); });
        K2_DMA(0); K2_DMA(1);
        int it = 0;
        for (int blk = 0; blk < 2; ++blk) {
            const int qb = blk == 0 ? 15 - x : x;
            const int nt = 2 * qb + 2;
            const int q0 = qb * 128 + wid * 32;
            const int tb32 = q0 >> 5;
            bf16x8 qf[8];
            const char* qp = ws + WS_QB + (size_t)((head * 64 + tb32) * 8) * 1024 + lane * 16;
#pragma unroll
            for (int ks = 0; ks < 8; ++ks) qf[ks] = *(const bf16x8*)(qp + ks * 1024);
            f32x16 o[4];
#pragma unroll
            for (int db = 0; db < 4; ++db) o[db] = f32x16{};
            f32x16 negm = f32x16{};
            float l = 0.f;
            for (int t = 0; t < nt; ++t, ++it) {
                const int rel = (q0 - 64 * t) >> 5;
                const unsigned ac = lbase + (it & 3) * SLOT;
                const unsigned an = lbase + ((it + 1) & 3) * SLOT;
                f32x16 s0, s1;
                SEG(0);
                sfor<0, 16>([&](auto ic) {
                    constexpr int j = decltype(ic)::value, ks = j >> 1;
                    lds_wait<7>(fr[j & 7]);
                    if constexpr (j == 0) s0 = MFMA32(fr[0], qf[0], negm);
                    else if constexpr (j == 1) s1 = MFMA32(fr[1], qf[0], negm);
                    else if constexpr ((j & 1) == 0) s0 = MFMA32(fr[j & 7], qf[ks], s0);
                    else s1 = MFMA32(fr[j & 7], qf[ks], s1);
                    constexpr int jn = j + 8;
                    SBAR();
                    if constexpr (jn < 16) lds_rd<BOFF((jn & 1) * 8 + (jn >> 1))>(fr[j & 7], ac);
                    else lds_rd<BOFF(jn)>(fr[j & 7], ac);
                    if constexpr (j == 2) K2_DMA(2);
                    if constexpr (j == 8) K2_DMA(3);
                    if constexpr (j == 14) K2_DMA(4);
                    SBAR();
                });
#define B_MASK()                                                                                        \
    do {                                                                                                \
        if (rel <= 1) {                                                                                 \
            const int lim = 32 * rel + r - 4 * h;                                                       \
            _Pragma("unroll") for (int i = 0; i < 16; ++i) {                                            \
                const int kr = (i & 3) + 8 * (i >> 2);                                                  \
                if (kr > lim) s0[i] = -__builtin_inff();                                                \
                if (kr + 32 > lim) s1[i] = -__builtin_inff();                                           \
            }                                                                                           \
        }                                                                                               \
    } while (0)
                SEG(1);
                B_MASK();
                float ps = 0.f;
                if (t > 0) {
#pragma unroll
                    for (int i = 0; i < 16; ++i) { s0[i] = __builtin_amdgcn_exp2f(s0[i]); ps += s0[i]; }
#pragma unroll
                    for (int i = 0; i < 16; ++i) { s1[i] = __builtin_amdgcn_exp2f(s1[i]); ps += s1[i]; }
                }
                if (t == 0 || __any(!(ps <= 65536.f))) {
                    if (t > 0) {
                        LAS char* sl = lds + (it & 3) * SLOT + lane * 16;
                        s0 = negm; s1 = negm;
#pragma unroll 1
                        for (int ks = 0; ks < 8; ++ks) {
                            bf16x8 qq = qf[0];
#pragma unroll
                            for (int u = 1; u < 8; ++u) qq = (ks == u) ? qf[u] : qq;
                            s0 = MFMA32(*(LAS bf16x8*)(sl + (ks >> 2) * 5120 + (ks & 3) * 1024), qq, s0);
                            s1 = MFMA32(*(LAS bf16x8*)(sl + 10240 + (ks >> 2) * 5120 + (ks & 3) * 1024), qq, s1);
                        }
                        B_MASK();
                    }
                    float mx = s0[0];
#pragma unroll
                    for (int i = 1; i < 16; ++i) mx = fmaxf(mx, s0[i]);
#pragma unroll
                    for (int i = 0; i < 16; ++i) mx = fmaxf(mx, s1[i]);
                    auto rr = __builtin_amdgcn_permlane32_swap(__float_as_uint(mx), __float_as_uint(mx), false, false);
                    mx = fmaxf(__uint_as_float(rr[0]), __uint_as_float(rr[1]));
                    float delta = mx;
                    if (t > 0) {
                        delta = fmaxf(mx, 0.f);
                        const float alpha = __builtin_amdgcn_exp2f(-delta);
                        l *= alpha;
#pragma unroll
                        for (int db = 0; db < 4; ++db)
#pragma unroll
                            for (int i = 0; i < 16; ++i) o[db][i] *= alpha;
                    }
                    const float nm = negm[0] - delta;
#pragma unroll
                    for (int i = 0; i < 16; ++i) negm[i] = nm;
                    ps = 0.f;
#pragma unroll
                    for (int i = 0; i < 16; ++i) { s0[i] = __builtin_amdgcn_exp2f(s0[i] - delta); ps += s0[i]; }
#pragma unroll
                    for (int i = 0; i < 16; ++i) { s1[i] = __builtin_amdgcn_exp2f(s1[i] - delta); ps += s1[i]; }
                }
                l += ps;
                bf16x8 pf[4];
                pf[0] = __builtin_bit_cast(bf16x8, pack8(s0, 0));
                pf[1] = __builtin_bit_cast(bf16x8, pack8(s0, 1));
                pf[2] = __builtin_bit_cast(bf16x8, pack8(s1, 0));
                pf[3] = __builtin_bit_cast(bf16x8, pack8(s1, 1));
                SEG(2);
                if (it + 1 < NIT) { K2_SYNC(it); } else { K2_M0(it + 3); }
                SEG(3);
                sfor<0, 16>([&](auto ic) {
                    constexpr int j = decltype(ic)::value, s4 = j >> 2, db = j & 3;
                    lds_wait<7>(fr[j & 7]);
                    o[db] = MFMA32(fr[j & 7], pf[s4], o[db]);
                    SBAR();
                    if constexpr (j < 8) lds_rd<BOFF(24 + j)>(fr[j & 7], ac);
                    else { constexpr int jn = j - 8; lds_rd<BOFF((jn & 1) * 8 + (jn >> 1))>(fr[j & 7], an); }
                    if constexpr (j == 4) K2_DMA(0);
                    if constexpr (j == 10) K2_DMA(1);
                    SBAR();
                });
                SEG(4);
            }
            auto rr = __builtin_amdgcn_permlane32_swap(__float_as_uint(l), __float_as_uint(l), false, false);
            const float inv = 1.0f / (__uint_as_float(rr[0]) + __uint_as_float(rr[1]));
            char* cp = ws + WS_CTXB + (size_t)(tb32 * 256 + head * 8) * 1024 + lane * 16;
#pragma unroll
            for (int db = 0; db < 4; ++db) {
                st16(cp + (2 * db) * 1024, pack8s(o[db], 0, inv));
                st16(cp + (2 * db + 1) * 1024, pack8s(o[db], 1, inv));
            }
        }
        WAIT_VM(0);
        STAMP(1, 2);
#undef B_MASK
    } else {
        const int tb32 = x * 8 + wa * 2;
        bf16x8 qf[2][2];
        const char* qp = ws + WS_QA + (size_t)((head * 64 + tb32) * 2) * 1024 + lane * 16;
#pragma unroll
        for (int qt = 0; qt < 2; ++qt)
#pragma unroll
            for (int ks = 0; ks < 2; ++ks) qf[qt][ks] = *(const bf16x8*)(qp + (qt * 2 + ks) * 1024);
        f32x16 o[2];
        o[0] = f32x16{}; o[1] = f32x16{};
        float negm[2] = {0.f, 0.f};
        bf16x8 kf[4], vf[4];
        f32x16 s[2][2];
        K2_SYNC(-1);
        lds_rd<AOFF(0)>(kf[0], lbase); lds_rd<AOFF(1)>(kf[1], lbase); lds_rd<AOFF(2)>(kf[2], lbase); lds_rd<AOFF(3)>(kf[3], lbase);
        for (int it = 0; it <= 32; ++it) {
            SEG(0);
            if (it > 0) {
                u32x4 pf[2][2][2];
                unsigned chk = 0;
                sfor<0, 4>([&](auto bc) {
                    constexpr int blk = decltype(bc)::value, kb = blk >> 1, qt = blk & 1;
                    f32x16 e;
#pragma unroll
                    for (int i = 0; i < 8; ++i) e[i] = __builtin_amdgcn_exp2f(s[kb][qt][i]);
                    K2_DMA(blk);
#pragma unroll
                    for (int i = 8; i < 16; ++i) e[i] = __builtin_amdgcn_exp2f(s[kb][qt][i]);
                    if constexpr (blk == 3) K2_DMA(4);
                    pf[kb][qt][0] = pack8(e, 0);
                    pf[kb][qt][1] = pack8(e, 1);
                    chk |= pf[kb][qt][0][0] | pf[kb][qt][0][1] | pf[kb][qt][0][2] | pf[kb][qt][0][3];
                    chk |= pf[kb][qt][1][0] | pf[kb][qt][1][1] | pf[kb][qt][1][2] | pf[kb][qt][1][3];
                });
                const bool first = (it == 1);
                if (first || __any((chk & 0x40004000u) != 0u)) {
#pragma unroll
                    for (int qt = 0; qt < 2; ++qt) {
                        float mx = s[0][qt][0];
#pragma unroll
                        for (int i = 1; i < 16; ++i) mx = fmaxf(mx, s[0][qt][i]);
#pragma unroll
                        for (int i = 0; i < 16; ++i) mx = fmaxf(mx, s[1][qt][i]);
                        auto rr = __builtin_amdgcn_permlane32_swap(__float_as_uint(mx), __float_as_uint(mx), false, false);
                        mx = fmaxf(__uint_as_float(rr[0]), __uint_as_float(rr[1]));
                        float delta = mx + 10.f;
                        if (!first) delta = fmaxf(delta, 0.f);
                        const float tgt = negm[qt] - delta;
                        const unsigned hi = pack2(tgt, 0.f) & 0xFFFFu;
                        const float hif = __uint_as_float(hi << 16);
                        const unsigned lo = pack2(tgt - hif, 0.f) & 0xFFFFu;
                        const float neff = hif + __uint_as_float(lo << 16);
                        delta = negm[qt] - neff;
                        negm[qt] = neff;
                        {
                            u32x4 qq = __builtin_bit_cast(u32x4, qf[qt][1]);
                            qq[2] = h ? 0u : (hi | (lo << 16));
                            qf[qt][1] = __builtin_bit_cast(bf16x8, qq);
                        }
                        if (!first) {
                            const float alpha = __builtin_amdgcn_exp2f(-delta);
#pragma unroll
                            for (int i = 0; i < 16; ++i) o[qt][i] *= alpha;
                        }
#pragma unroll
                        for (int kb = 0; kb < 2; ++kb) {
                            f32x16 e;
#pragma unroll
                            for (int i = 0; i < 16; ++i) e[i] = __builtin_amdgcn_exp2f(s[kb][qt][i] - delta);
                            pf[kb][qt][0] = pack8(e, 0);
                            pf[kb][qt][1] = pack8(e, 1);
                        }
                    }
                }
                SEG(1);
                lds_wait2<4>(vf[0], vf[1]);
                lds_wait2<4>(vf[2], vf[3]);
#pragma unroll
                for (int kb = 0; kb < 2; ++kb)
#pragma unroll
                    for (int s2 = 0; s2 < 2; ++s2)
#pragma unroll
                        for (int qt = 0; qt < 2; ++qt)
                            o[qt] = MFMA32(vf[kb * 2 + s2], __builtin_bit_cast(bf16x8, pf[kb][qt][s2]), o[qt]);
            } else {
                K2_DMA(0); K2_DMA(1); K2_DMA(2); K2_DMA(3); K2_DMA(4);
            }
            SEG(2);
            lds_wait2<0>(kf[0], kf[1]);
            lds_wait2<0>(kf[2], kf[3]);
#pragma unroll
            for (int kb = 0; kb < 2; ++kb)
#pragma unroll
                for (int qt = 0; qt < 2; ++qt) {
                    s[kb][qt] = MFMA32(kf[kb * 2], qf[qt][0], f32x16{});
                    s[kb][qt] = MFMA32(kf[kb * 2 + 1], qf[qt][1], s[kb][qt]);
                }
            SBAR();
            if (it == 32) STAMPW(1, 3, 4);
            SEG(3);
            K2_SYNC(it);
            SEG(4);
            const unsigned ac = lbase + (it & 3) * SLOT;
            const unsigned an = lbase + ((it + 1) & 3) * SLOT;
            lds_rd<AOFF(4)>(vf[0], ac); lds_rd<AOFF(5)>(vf[1], ac); lds_rd<AOFF(6)>(vf[2], ac); lds_rd<AOFF(7)>(vf[3], ac);
            lds_rd<AOFF(0)>(kf[0], an); lds_rd<AOFF(1)>(kf[1], an); lds_rd<AOFF(2)>(kf[2], an); lds_rd<AOFF(3)>(kf[3], an);
        }
        WAIT_VM(0);
#pragma unroll
        for (int qt = 0; qt < 2; ++qt) {
            const unsigned lv = __float_as_uint(o[qt][8]);
            auto rr = __builtin_amdgcn_permlane32_swap(lv, lv, false, false);
            const float lsum = __uint_as_float(rr[1]);
            const float inv = 1.0f / lsum;
            char* cp = ws + WS_CTXA + (size_t)((tb32 + qt) * 40) * 1024;
#pragma unroll
            for (int gq = 0; gq < 3; ++gq) {
                const int d0 = 8 * gq + 4 * h;
                if (d0 < 20) {
                    const int k = head * 20 + d0;
                    u32x2 w;
                    w[0] = pack2(o[qt][4 * gq + 0] * inv, o[qt][4 * gq + 1] * inv);
                    w[1] = pack2(o[qt][4 * gq + 2] * inv, o[qt][4 * gq + 3] * inv);
                    *(u32x2*)(cp + (k >> 4) * 1024 + (r + 32 * ((k >> 3) & 1)) * 16 + (k & 4) * 2) = w;
                }
            }
        }
    }
}

constexpr int G3_STAGE = 49152;
constexpr int G3_LDS = 2 * G3_STAGE;

__global__ __launch_bounds__(512, 2) void k3_oproj(const __hip_bfloat16* __restrict__ Awo, const __hip_bfloat16* __restrict__ Bwo,
                                                   char* __restrict__ ws) {
    extern __shared__ __attribute__((aligned(16))) char smem[];
    LAS char* lds = (LAS char*)smem;
    const int tid = threadIdx.x, lane = tid & 63, wid = __builtin_amdgcn_readfirstlane(tid >> 6);
    const int wm = wid >> 1, wn = wid & 1, h = lane >> 5;
    const int bid = blockIdx.x;
    const bool isB = bid < 200;
    int mt, nt, sp, kt0, nkt, kstot;
    const char* Xc; const char* Wb;
    if (isB) {
        mt = bid & 7; const int rest = bid >> 3; nt = rest % 5; sp = rest / 5;
        kt0 = sp * 13; nkt = sp == 4 ? 12 : 13; kstot = 256;
        Xc = ws + WS_CTXB; Wb = (const char*)Bwo;
    } else {
        const int b2 = bid - 200; mt = b2 & 7; nt = b2 >> 3; sp = 5;
        kt0 = 0; nkt = 10; kstot = 40;
        Xc = ws + WS_CTXA; Wb = (const char*)Awo;
    }
    unsigned woff[2];
#pragma unroll
    for (int i = 0; i < 2; ++i) {
        const int k = (wid * 2 + i) * 4 + (lane >> 4);
        const int p = lane & 15, b = (p >> 2) ^ (k & 3);
        woff[i] = (unsigned)(((kt0 * 64 + k) * 640 + nt * 128 + (b * 4 + (p & 3)) * 8) * 2);
    }
    const unsigned lds0 = (unsigned)(uintptr_t)lds;
    const char* xsb = Xc + (size_t)((mt * 8 + wid) * kstot + kt0 * 4) * 1024 + 4096;
    const unsigned voff = lane * 16;
    woff[1] -= 1024;
    auto piece = [&](int kt, int buf, auto ic) {
        constexpr int i = decltype(ic)::value;
        if constexpr (i == 0) set_m0(lds0 + buf * G3_STAGE + wid * 6144 + 4096);
        if constexpr (i < 4) dma_s<i * 1024 - 4096>(voff, xsb + (size_t)kt * 4096);
        else dma_s<(i - 4) * 1024>(woff[i - 4], Wb + (size_t)kt * (64u * 640u * 2u));
    };
    auto stage = [&](int kt, int buf) { sfor<0, 6>([&](auto ic) { piece(kt, buf, ic); }); };
    const int q = (lane >> 2) & 3, p4 = lane & 3, g16 = (lane >> 4) & 1;
    const int r0 = isB ? 4 * h : 8 * h, r1 = isB ? 8 + 4 * h : 8 * h + 4;
    int wb0[2], wb1[2];
#pragma unroll
    for (int ni = 0; ni < 2; ++ni) {
        const int b = wn * 2 + ni;
        const int c = ((b ^ q) * 64) + g16 * 32 + p4 * 8;
        wb0[ni] = (r0 >> 3) * 6144 + 4096 + ((r0 & 7) + q) * 256 + c;
        wb1[ni] = (r1 >> 3) * 6144 + 4096 + ((r1 & 7) + q) * 256 + c;
    }
    f32x16 acc[2][2];
#pragma unroll
    for (int mi = 0; mi < 2; ++mi)
#pragma unroll
        for (int ni = 0; ni < 2; ++ni) acc[mi][ni] = f32x16{};

    bf16x8 xf[2][2], wf[2][2];
#define G3_RD(set, bb, ks)                                                                                       \
    do {                                                                                                         \
        _Pragma("unroll") for (int ni = 0; ni < 2; ++ni)                                                         \
            wf[set][ni] = tr_frag((bb) + wb0[ni] + (ks) * 12288, (bb) + wb1[ni] + (ks) * 12288);                 \
        _Pragma("unroll") for (int mi = 0; mi < 2; ++mi)                                                         \
            xf[set][mi] = *(LAS bf16x8*)((bb) + (wm * 2 + mi) * 6144 + (ks) * 1024 + lane * 16);                 \
    } while (0)
#define G3_MM(set, kn, bn_, p0)                                                                                  \
    sfor<0, 2>([&](auto mc) {                                                                                    \
        constexpr int mi = decltype(mc)::value;                                                                  \
        _Pragma("unroll") for (int ni = 0; ni < 2; ++ni) acc[mi][ni] = MFMA32(wf[set][ni], xf[set][mi], acc[mi][ni]); \
        SBAR(); if ((kn) < nkt) piece((kn), (bn_), std::integral_constant<int, (p0) + mi>{}); SBAR();            \
    })
    STAMP(2, 0);
    stage(0, 0);
    stage(1, 1);
    WAIT_VM(6);
    BARRIER();
    STAMP(2, 1);
    G3_RD(0, lds, 0);
#pragma unroll 1
    for (int kt = 0; kt < nkt; ++kt) {
        LAS char* bb = lds + (kt & 1) * G3_STAGE;
        LAS char* bn = lds + ((kt + 1) & 1) * G3_STAGE;
        const int k1 = kt >= 1 ? kt + 1 : nkt;
        LGKM0(); G3_RD(1, bb, 1); SBAR(); G3_MM(0, k1, (kt + 1) & 1, 2); SBAR();
        LGKM0(); G3_RD(0, bb, 2); SBAR(); G3_MM(1, k1, (kt + 1) & 1, 4); SBAR();
        LGKM0(); G3_RD(1, bb, 3); SBAR(); G3_MM(0, nkt, 0, 0); SBAR();
        LGKM0(); WAIT_VM(0);
        BARRIER();
        if (kt + 1 < nkt) G3_RD(0, bn, 0);
        SBAR(); G3_MM(1, kt + 2, kt & 1, 0); SBAR();
    }
#undef G3_RD
#undef G3_MM
    STAMP(2, 2);
#pragma unroll
    for (int mi = 0; mi < 2; ++mi)
#pragma unroll
        for (int ni = 0; ni < 2; ++ni) {
            const int mt32 = mt * 8 + wm * 2 + mi, nt32 = nt * 4 + wn * 2 + ni;
            char* dst = ws + WS_PART + (size_t)sp * SLAB + (size_t)(mt32 * 20 + nt32) * 4096 + lane * 16;
#pragma unroll
            for (int gq = 0; gq < 4; ++gq) {
                f32x4 v = {acc[mi][ni][4 * gq], acc[mi][ni][4 * gq + 1], acc[mi][ni][4 * gq + 2], acc[mi][ni][4 * gq + 3]};
                st16(dst + gq * 1024, __builtin_bit_cast(u32x4, v));
            }
        }
    STAMP(2, 3);
}

__global__ __launch_bounds__(256) void k4_reduce(const char* __restrict__ ws, __hip_bfloat16* __restrict__ out) {
    const int lane = threadIdx.x & 63;
    const int w = blockIdx.x * 4 + (threadIdx.x >> 6);
    const int sub = w & 3, t = w >> 2;
    const int mt32 = t / 40, ct = t - mt32 * 40;
    const bool isB = ct >= 20;
    const int nt32 = isB ? ct - 20 : ct;
    const int ql = sub * 8 + (lane & 7), c8 = lane >> 3;
    const char* src = ws + WS_PART + (size_t)(mt32 * 20 + nt32) * 4096 + (c8 >> 1) * 1024 + (ql + 32 * (c8 & 1)) * 16;
    f32x4 v;
    if (isB) {
        v = *(const f32x4*)(src);
#pragma unroll
        for (int s = 1; s < NSPLIT; ++s) v += *(const f32x4*)(src + (size_t)s * SLAB);
    } else {
        v = *(const f32x4*)(src + (size_t)5 * SLAB);
    }
    u32x2 o;
    o[0] = pack2(v[0], v[1]);
    o[1] = pack2(v[2], v[3]);
    *(u32x2*)((char*)out + (size_t)(mt32 * 32 + ql) * 2560 + (ct * 32 + c8 * 4) * 2) = o;
}

extern "C" void kernel_launch(void* const* d_in, const int* in_sizes, int n_in,
                              void* d_out, int out_size, void* d_ws, size_t ws_size,
                              hipStream_t stream) {
    (void)in_sizes; (void)n_in; (void)out_size; (void)ws_size;
    const __hip_bfloat16* Ax = (const __hip_bfloat16*)d_in[0];
    const __hip_bfloat16* Awqkv = (const __hip_bfloat16*)d_in[1];
    const __hip_bfloat16* Awo = (const __hip_bfloat16*)d_in[2];
    const __hip_bfloat16* Bx = (const __hip_bfloat16*)d_in[3];
    const __hip_bfloat16* Bckv = (const __hip_bfloat16*)d_in[4];
    const __hip_bfloat16* Bwq = (const __hip_bfloat16*)d_in[5];
    const __hip_bfloat16* Bwuk = (const __hip_bfloat16*)d_in[6];
    const __hip_bfloat16* Bwuv = (const __hip_bfloat16*)d_in[7];
    const __hip_bfloat16* Bwo = (const __hip_bfloat16*)d_in[8];
    char* ws = (char*)d_ws;
    (void)hipFuncSetAttribute((const void*)k1_proj, hipFuncAttributeMaxDynamicSharedMemorySize, G1_LDS);
    (void)hipFuncSetAttribute((const void*)k2_attn, hipFuncAttributeMaxDynamicSharedMemorySize, K2_LDS);
    (void)hipFuncSetAttribute((const void*)k3_oproj, hipFuncAttributeMaxDynamicSharedMemorySize, G3_LDS);
    k1_proj<<<256, 512, G1_LDS, stream>>>(Ax, Awqkv, Bx, Bckv, Bwq, Bwuk, Bwuv, Awo, Bwo, ws);
    k2_attn<<<256, 512, K2_LDS, stream>>>(ws);
    k3_oproj<<<240, 512, G3_LDS, stream>>>(Awo, Bwo, ws);
    k4_reduce<<<64 * 40, 256, 0, stream>>>(ws, (__hip_bfloat16*)d_out);
}
```

```cpp
#include <hip/hip_runtime.h>
#include <hip/hip_bf16.h>
#include <stdint.h>
#include <type_traits>

typedef short s16x4 __attribute__((ext_vector_type(4)));
typedef short bf16x8 __attribute__((ext_vector_type(8)));
typedef float f32x16 __attribute__((ext_vector_type(16)));
typedef float f32x4 __attribute__((ext_vector_type(4)));
typedef float f32x2 __attribute__((ext_vector_type(2)));
typedef unsigned u32x4 __attribute__((ext_vector_type(4)));
typedef unsigned u32x2 __attribute__((ext_vector_type(2)));
typedef __bf16 bf16x2_t __attribute__((ext_vector_type(2)));
#define LAS __attribute__((address_space(3)))
#define DI __device__ __forceinline__
#define MFMA32(a, b, c) __builtin_amdgcn_mfma_f32_32x32x16_bf16((a), (b), (c), 0, 0, 0)
#define CFENCE() asm volatile("" ::: "memory")
#define BARRIER() do { CFENCE(); __builtin_amdgcn_s_barrier(); CFENCE(); } while (0)
#define WAIT_VM(n) asm volatile("s_waitcnt vmcnt(" #n ") lgkmcnt(0)" ::: "memory")
#define SBAR() __builtin_amdgcn_sched_barrier(0)

DI unsigned pack2(float a, float b) {
    f32x2 v = {a, b};
    bf16x2_t r = __builtin_convertvector(v, bf16x2_t);
    return __builtin_bit_cast(unsigned, r);
}
DI u32x4 pack8(const f32x16& x, int s) {
    u32x4 w;
    w[0] = pack2(x[8 * s + 0], x[8 * s + 1]);
    w[1] = pack2(x[8 * s + 2], x[8 * s + 3]);
    w[2] = pack2(x[8 * s + 4], x[8 * s + 5]);
    w[3] = pack2(x[8 * s + 6], x[8 * s + 7]);
    return w;
}
DI u32x4 pack8s(const f32x16& x, int s, float c) {
    u32x4 w;
    w[0] = pack2(x[8 * s + 0] * c, x[8 * s + 1] * c);
    w[1] = pack2(x[8 * s + 2] * c, x[8 * s + 3] * c);
    w[2] = pack2(x[8 * s + 4] * c, x[8 * s + 5] * c);
    w[3] = pack2(x[8 * s + 6] * c, x[8 * s + 7] * c);
    return w;
}
DI void glds16(const void* g, LAS char* l) {
    __builtin_amdgcn_global_load_lds((const unsigned*)g, (LAS unsigned*)l, 16, 0, 0);
}
DI bf16x8 tr_frag(LAS char* p0, LAS char* p1) {
    s16x4 lo = __builtin_amdgcn_ds_read_tr16_b64_v4i16((LAS s16x4*)p0);
    s16x4 hi = __builtin_amdgcn_ds_read_tr16_b64_v4i16((LAS s16x4*)p1);
    return __builtin_shufflevector(lo, hi, 0, 1, 2, 3, 4, 5, 6, 7);
}

#define STMODE 2
DI void st16(void* p, u32x4 v) {
    asm volatile("global_store_dwordx4 %0, %1, off sc1\n\ts_nop 1" :: "v"(p), "v"(v) : "memory");
}
template <int I, int N, class F> DI void sfor(F&& f) {
    if constexpr (I < N) { f(std::integral_constant<int, I>{}); sfor<I + 1, N>(f); }
}
template <int OFF> DI void lds_rd(bf16x8& d, unsigned addr) {
    asm volatile("ds_read_b128 %0, %1 offset:%2" : "=v"(d) : "v"(addr), "i"(OFF));
}
template <int N> DI void lds_wait(bf16x8& d) { asm volatile("s_waitcnt lgkmcnt(%1)" : "+v"(d) : "i"(N)); }
template <int N> DI void lds_wait2(bf16x8& d, bf16x8& e) { asm volatile("s_waitcnt lgkmcnt(%2)" : "+v"(d), "+v"(e) : "i"(N)); }
#define LGKM0() __builtin_amdgcn_s_waitcnt(0xC07F)
DI void set_m0(unsigned v) { asm volatile("s_mov_b32 m0, %0\n\ts_nop 0" :: "s"(v) : "memory"); }
template <int OFF> DI void dma_s(unsigned voff, const char* sbase) {
    asm volatile("global_load_lds_dwordx4 %0, %1 offset:%2" :: "v"(voff), "s"(sbase), "i"(OFF) : "memory");
}

#define ABL_NOPIECE 0
#define ABL_NOBAR 0
#define ABL_T0 0
#define ABL_K2 0
#define ABL_NODMA 0
#define ABL_NOMFMA 0
#define SEG(i) do { } while (0)
#define STAMP(kid, idx) do { } while (0)
#define STAMPW(kid, idx, w) do { } while (0)
constexpr size_t WS_KVB = 0;
constexpr size_t WS_QB = WS_KVB + (size_t)32 * 1048576;
constexpr size_t WS_KVA = WS_QB + (size_t)16 * 1048576;
constexpr size_t WS_QA = WS_KVA + (size_t)8 * 1048576;
constexpr size_t WS_CTXB = WS_QA + (size_t)4 * 1048576;
constexpr size_t WS_CTXA = WS_CTXB + (size_t)16 * 1048576;
constexpr size_t WS_PART = WS_CTXA + (size_t)4 * 1048576;
constexpr size_t WS_CNT = WS_PART + (size_t)32 * 1048576;
constexpr int NSPLIT = 5;
constexpr size_t SLAB = (size_t)64 * 20 * 4096;

constexpr float CB = 0.08838834764831845f * 1.4426950408889634f;
constexpr float CA = 0.22360679774997896f * 1.4426950408889634f;

#define PREFETCH 1
constexpr int G1_LDS = 2 * 65536 + 2048;
enum { EP_BK = 0, EP_BV = 1, EP_BQ = 2, EP_AQK = 3, EP_AV = 4 };

struct G1Src { unsigned xoff[4], woff[4]; const char* Xb; const char* Wb; unsigned wstep; int nkt; };
DI void g1_src(G1Src& s, const __hip_bfloat16* X, const __hip_bfloat16* W, int K, int ldw, int m0, int n0, int nlim, int wid, int lane) {
#pragma unroll
    for (int i = 0; i < 4; ++i) {
        const int row = (wid * 4 + i) * 8 + (lane >> 3);
        const int c = (lane & 7) ^ ((row >> 1) & 7);
        s.xoff[i] = (unsigned)(((m0 + row) * K + c * 8) * 2) + 4096 - i * 1024;
        const int k = (wid * 4 + i) * 2 + (lane >> 5);
        const int p = lane & 31, b = (p >> 2) ^ (k & 3);
        int col = n0 + (b * 4 + (p & 3)) * 8;
        col = col < nlim ? col : nlim - 8;
        s.woff[i] = (unsigned)((k * ldw + col) * 2) - i * 1024;
    }
    s.Xb = (const char*)X; s.Wb = (const char*)W; s.wstep = (unsigned)(64 * ldw * 2); s.nkt = K >> 6;
}
template <int I> DI void g1_piece(const G1Src& s, int kt) {
    if constexpr (I < 4) dma_s<I * 1024 - 4096>(s.xoff[I], s.Xb + (size_t)kt * 128);
    else dma_s<(I - 4) * 1024>(s.woff[I - 4], s.Wb + (size_t)kt * s.wstep);
}

template <bool TR>
DI void g1_loop(f32x16 (&acc)[4][2], bf16x8 (&xf)[2][4], bf16x8 (&wf)[2][2], const G1Src& cur, const G1Src& nxt, bool has_next, bool first,
                LAS char* lds, int wid, int lane) {
    const int wm = wid >> 2, wn = wid & 3, r = lane & 31, h = lane >> 5;
    const unsigned lds0 = (unsigned)(uintptr_t)lds;
    const int nkt = cur.nkt;
    const int xsw = (r >> 1) & 7;
    const int xbase = wm * 4 * 8192 + r * 128;
    const int q = (lane >> 2) & 3, p4 = lane & 3, g16 = (lane >> 4) & 1;
    int wbase[2];
#pragma unroll
    for (int ni = 0; ni < 2; ++ni) {
        const int b = wn * 2 + ni;
        wbase[ni] = h * 8192 + 4096 + q * 512 + ((b ^ q) * 64) + g16 * 32 + p4 * 8;
    }
#pragma unroll
    for (int mi = 0; mi < 4; ++mi)
#pragma unroll
        for (int ni = 0; ni < 2; ++ni) acc[mi][ni] = f32x16{};
#define G1_RD(set, bb, ks)                                                                                       \
    do {                                                                                                         \
        const int xc_ = ((2 * (ks) + h) ^ xsw) * 16;                                                             \
        _Pragma("unroll") for (int ni = 0; ni < 2; ++ni)                                                         \
            wf[set][ni] = tr_frag((bb) + wbase[ni] + (ks) * 16384, (bb) + wbase[ni] + (ks) * 16384 + 2048);      \
        _Pragma("unroll") for (int mi = 0; mi < 4; ++mi) xf[set][mi] = *(LAS bf16x8*)((bb) + xbase + xc_ + mi * 8192); \
    } while (0)
#define G1_MM(set, on, kn, p0, pe)                                                                               \
    sfor<0, 4>([&](auto mc) {                                                                                    \
        constexpr int mi = decltype(mc)::value;                                                                  \
        _Pragma("unroll") for (int ni = 0; ni < 2; ++ni)                                                         \
            acc[mi][ni] = TR ? MFMA32(wf[set][ni], xf[set][mi], acc[mi][ni]) : MFMA32(xf[set][mi], wf[set][ni], acc[mi][ni]); \
        if constexpr ((p0) + mi < (pe)) {                                                                        \
            SBAR();                                                                                              \
            if (on) { if ((kn) < nkt) g1_piece<(p0) + mi>(cur, (kn)); else g1_piece<(p0) + mi>(nxt, (kn) - nkt); } \
            SBAR();                                                                                              \
        }                                                                                                        \
    })
    if (first) G1_RD(0, lds, 0);
#pragma unroll 1
    for (int kt = 0; kt < nkt; ++kt) {
        LAS char* bb = lds + (kt & 1) * 65536;
        LAS char* bn = lds + ((kt + 1) & 1) * 65536;
        const bool on1 = !(first && kt == 0) && (kt + 1 < nkt || has_next);
        const bool on2 = kt + 2 < nkt || has_next;
        LGKM0(); G1_RD(1, bb, 1); SBAR(); G1_MM(0, on1, kt + 1, 4, 8); SBAR();
        LGKM0(); G1_RD(0, bb, 2); SBAR(); G1_MM(1, false, 0, 0, 0); SBAR();
        LGKM0(); G1_RD(1, bb, 3); SBAR(); G1_MM(0, false, 0, 0, 0); SBAR();
        LGKM0(); WAIT_VM(0);
        BARRIER();
        set_m0(lds0 + (kt & 1) * 65536 + wid * 8192 + 4096);
        if (kt + 1 < nkt || has_next) G1_RD(0, bn, 0);
        SBAR(); G1_MM(1, on2, kt + 2, 0, 4); SBAR();
    }
#undef G1_RD
#undef G1_MM
}

template <int EP>
DI void g1_epi(const f32x16 (&acc)[4][2], int m0, int n0, int nlim, int nsec, char* __restrict__ ws, int wid, int lane) {
    const int wm = wid >> 2, wn = wid & 3, r = lane & 31, h = lane >> 5;
#pragma unroll
    for (int mi = 0; mi < 4; ++mi) {
        const int tb = (m0 + wm * 128 + mi * 32) >> 5;
        const int tile = tb >> 1, kb = tb & 1;
#pragma unroll
        for (int ni = 0; ni < 2; ++ni) {
            const int nb = n0 + wn * 64 + ni * 32;
            if (nb >= nlim) continue;
            if constexpr (EP == EP_BK) {
                const int head = nb >> 7, db = (nb >> 5) & 3;
                char* dst = ws + WS_KVB + (size_t)head * 1048576 + tile * 32768 + (kb * 8 + db * 2) * 1024 + lane * 16;
                st16(dst, pack8(acc[mi][ni], 0));
                st16(dst + 1024, pack8(acc[mi][ni], 1));
            } else if constexpr (EP == EP_BV) {
                const int head = nb >> 7, db = (nb >> 5) & 3;
                char* dst = ws + WS_KVB + (size_t)head * 1048576 + tile * 32768 + 16384 + (kb * 8 + db) * 1024 + lane * 16;
                st16(dst, pack8(acc[mi][ni], 0));
                st16(dst + 4096, pack8(acc[mi][ni], 1));
            } else if constexpr (EP == EP_BQ) {
                const int head = nb >> 7, db = (nb >> 5) & 3;
                char* dst = ws + WS_QB + (size_t)((head * 64 + tb) * 8 + db * 2) * 1024 + lane * 16;
                st16(dst, pack8s(acc[mi][ni], 0, CB));
                st16(dst + 1024, pack8s(acc[mi][ni], 1, CB));
            } else if constexpr (EP == EP_AQK) {
                const bool isq = nb < 640;
                const float sc = isq ? CA : 1.0f;
#pragma unroll
                for (int g = 0; g < 4; ++g) {
                    const int n = nb - (isq ? 0 : 640) + 8 * g + 4 * h;
                    const int head = n / 20, d0 = n - head * 20;
                    char* base = isq ? ws + WS_QA + (size_t)((head * 64 + tb) * 2) * 1024
                                     : ws + WS_KVA + (size_t)head * 262144 + tile * 8192 + kb * 2048;
                    char* dst = base + (d0 >> 4) * 1024 + (r + 32 * ((d0 >> 3) & 1)) * 16 + (d0 & 4) * 2;
                    u32x2 w;
                    w[0] = pack2(acc[mi][ni][4 * g + 0] * sc, acc[mi][ni][4 * g + 1] * sc);
                    w[1] = pack2(acc[mi][ni][4 * g + 2] * sc, acc[mi][ni][4 * g + 3] * sc);
                    *(u32x2*)dst = w;
                    if (d0 == 16) {
                        *(u32x2*)(dst + 8) = (u32x2){isq ? 0u : 0x3F803F80u, 0u};
                        *(u32x4*)(base + 1024 + (r + 32) * 16) = (u32x4){0u, 0u, 0u, 0u};
                    }
                }
            } else {
                const int n = nb - nsec + r;
                const int head = n / 20, d = n - head * 20;
                char* dst = ws + WS_KVA + (size_t)head * 262144 + tile * 8192 + 4096 + kb * 2048 + (d + 32 * h) * 16;
                st16(dst, pack8(acc[mi][ni], 0));
                st16(dst + 1024, pack8(acc[mi][ni], 1));
                if (d == 19) {
                    const u32x4 one = {0x3F803F80u, 0x3F803F80u, 0x3F803F80u, 0x3F803F80u};
                    *(u32x4*)(dst + 16) = one;
                    *(u32x4*)(dst + 1024 + 16) = one;
                }
            }
        }
    }
}

__global__ __launch_bounds__(512, 2) void k1_proj(const __hip_bfloat16* __restrict__ Ax, const __hip_bfloat16* __restrict__ Awqkv,
                                                  const __hip_bfloat16* __restrict__ Bx, const __hip_bfloat16* __restrict__ Bckv,
                                                  const __hip_bfloat16* __restrict__ Bwq, const __hip_bfloat16* __restrict__ Bwuk,
                                                  const __hip_bfloat16* __restrict__ Bwuv, const __hip_bfloat16* __restrict__ Awo,
                                                  const __hip_bfloat16* __restrict__ Bwo, char* __restrict__ ws) {
    extern __shared__ __attribute__((aligned(16))) char smem[];
    LAS char* lds = (LAS char*)smem;
    const int tid = threadIdx.x, lane = tid & 63, wid = __builtin_amdgcn_readfirstlane(tid >> 6);
    const int bid = blockIdx.x;
    const int m0 = ((bid >> 3) & 7) * 256;
    const int c1 = (bid & 7) + 8 * (bid >> 6);
    const int c2 = c1;
    const bool has2 = c2 < 24;
    G1Src s1, s2;
    if (c1 < 16) g1_src(s1, Bckv, Bwuk, 512, 4096, m0, c1 * 256, 4096, wid, lane);
    else g1_src(s1, Bckv, Bwuv, 512, 4096, m0, (c1 - 16) * 256, 4096, wid, lane);
    const int n2 = c2 < 16 ? c2 * 256 : (c2 - 16) * 256;
    if (c2 < 16) g1_src(s2, Bx, Bwq, 640, 4096, m0, n2, 4096, wid, lane);
    else g1_src(s2, Ax, Awqkv, 640, 1920, m0, has2 ? n2 : 0, c2 < 21 ? 1280 : 1920, wid, lane);

    f32x16 acc[4][2];
    bf16x8 xf[2][4], wf[2][2];
    const unsigned lds0 = (unsigned)(uintptr_t)lds;
    if (bid == 255 && tid < 40) ((unsigned*)(ws + WS_CNT))[tid * 64] = 0u;
    STAMP(0, 0);
    set_m0(lds0 + wid * 8192 + 4096);
    sfor<0, 8>([&](auto ic) { g1_piece<decltype(ic)::value>(s1, 0); });
    set_m0(lds0 + 65536 + wid * 8192 + 4096);
    sfor<0, 8>([&](auto ic) { g1_piece<decltype(ic)::value>(s1, 1); });
    {
        set_m0(lds0 + 131072 + wid * 256);
        const unsigned gw = (unsigned)(bid * 8 + wid) * 64 + lane;
#define PF(ptr, nbytes) do { constexpr unsigned R_ = 131072u / ((nbytes) / 128u);                                     \
            const unsigned ln_ = gw / R_;                                                                                \
            const unsigned off_ = (gw % R_ == 0u && ln_ * 128u < (nbytes)) ? ln_ * 128u : 0u;                             \
            asm volatile("global_load_lds_dword %0, %1" :: "v"(off_), "s"((const char*)(ptr)) : "memory"); } while (0)
        PF(Bckv, 2048u * 512u * 2u); PF(Bwuk, 512u * 4096u * 2u); PF(Bwuv, 512u * 4096u * 2u);
        PF(Bx, 2048u * 640u * 2u); PF(Bwq, 640u * 4096u * 2u); PF(Ax, 2048u * 640u * 2u); PF(Awqkv, 640u * 1920u * 2u);
        PF(Bwo, 4096u * 640u * 2u); PF(Awo, 640u * 640u * 2u);
#undef PF
    }
    WAIT_VM(17);
    BARRIER();
    STAMP(0, 1);
    if (c1 < 16) { g1_loop<true>(acc, xf, wf, s1, s2, has2, true, lds, wid, lane); g1_epi<EP_BK>(acc, m0, c1 * 256, 4096, 0, ws, wid, lane); }
    else { g1_loop<false>(acc, xf, wf, s1, s2, has2, true, lds, wid, lane); g1_epi<EP_BV>(acc, m0, (c1 - 16) * 256, 4096, 0, ws, wid, lane); }
    STAMP(0, 2);
    if (c2 < 21) {
        g1_loop<true>(acc, xf, wf, s2, s2, false, false, lds, wid, lane);
        if (c2 < 16) g1_epi<EP_BQ>(acc, m0, n2, 4096, 0, ws, wid, lane);
        else g1_epi<EP_AQK>(acc, m0, n2, 1280, 0, ws, wid, lane);
    } else if (has2) {
        g1_loop<false>(acc, xf, wf, s2, s2, false, false, lds, wid, lane);
        g1_epi<EP_AV>(acc, m0, n2, 1920, 1280, ws, wid, lane);
    }
    STAMP(0, 3);
}

constexpr int SLOT = 40960;
constexpr int NSLOT = 4;
constexpr int K2_LDS = NSLOT * SLOT;
constexpr int NIT = 34;

__global__ __launch_bounds__(512, 2) void k2_attn(char* __restrict__ ws) {
    extern __shared__ __attribute__((aligned(16))) char smem[];
    LAS char* lds = (LAS char*)smem;
    const int tid = threadIdx.x, lane = tid & 63, wid = __builtin_amdgcn_readfirstlane(tid >> 6);
    const int r = lane & 31, h = lane >> 5;
    const int g = blockIdx.x, xcd = g & 7, idx = g >> 3;
    const int head = xcd * 4 + (idx >> 3), x = idx & 7;
    const char* kvb = ws + WS_KVB + (size_t)head * 1048576;
    const char* kva = ws + WS_KVA + (size_t)head * 262144;
    const int n1 = 32 - 2 * x;
    const unsigned lbase = (unsigned)(uintptr_t)lds + lane * 16;

    STAMP(1, 0);
    const unsigned lds0 = (unsigned)(uintptr_t)lds;
    const unsigned voff = lane * 16;
    const int wa = wid & 3;
    const char* sbB = kvb;
    const char* sbA = kva;
#define K2_M0(tt)                                                                           \
    do {                                                                                    \
        const int t3_ = (tt) < NIT ? (tt) : NIT - 1;                                        \
        const int tb3_ = t3_ < n1 ? t3_ : t3_ - n1;                                         \
        const int ta3_ = t3_ < 32 ? t3_ : 31;                                               \
        sbB = kvb + (size_t)tb3_ * 32768 + wid * 4096 + 2048;                               \
        sbA = kva + (size_t)ta3_ * 8192 + wid * 1024 - 2048;                                \
        set_m0(lds0 + ((tt) < NIT ? ((tt) & 3) : 2 + ((tt) & 1)) * SLOT + wid * 5120 + 2048); \
    } while (0)
#define K2_DMA(i) do { SBAR(); if constexpr ((i) < 4) dma_s<(i) * 1024 - 2048>(voff, sbB); else dma_s<2048>(voff, sbA); SBAR(); } while (0)
#define K2_STAGE_ALL(tt) do { K2_M0(tt); K2_DMA(0); K2_DMA(1); K2_DMA(2); K2_DMA(3); K2_DMA(4); } while (0)
#define BOFF(f) (((f) >> 2) * 5120 + ((f) & 3) * 1024)
#define AOFF(a) ((a) * 5120 + 4096)
    K2_STAGE_ALL(0);
    K2_STAGE_ALL(1);

#define K2_SYNC(it) do { WAIT_VM(5); BARRIER(); K2_M0((it) + 3); } while (0)

    if (wid < 4) {
        bf16x8 fr[8];
        K2_SYNC(-1);
        STAMP(1, 1);
        sfor<0, 8>([&](auto ic) { constexpr int j = decltype(ic)::value; lds_rd<BOFF((j & 1) * 8 + (j >> 1))>(fr[j], lbase); });
        K2_DMA(0); K2_DMA(1);
        int it = 0;
        for (int blk = 0; blk < 2; ++blk) {
            const int qb = blk == 0 ? 15 - x : x;
            const int nt = 2 * qb + 2;
            const int q0 = qb * 128 + wid * 32;
            const int tb32 = q0 >> 5;
            bf16x8 qf[8];
            const char* qp = ws + WS_QB + (size_t)((head * 64 + tb32) * 8) * 1024 + lane * 16;
#pragma unroll
            for (int ks = 0; ks < 8; ++ks) qf[ks] = *(const bf16x8*)(qp + ks * 1024);
            f32x16 o[4];
#pragma unroll
            for (int db = 0; db < 4; ++db) o[db] = f32x16{};
            f32x16 negm = f32x16{};
            float l = 0.f;
            for (int t = 0; t < nt; ++t, ++it) {
                const int rel = (q0 - 64 * t) >> 5;
                const unsigned ac = lbase + (it & 3) * SLOT;
                const unsigned an = lbase + ((it + 1) & 3) * SLOT;
                f32x16 s0, s1;
                SEG(0);
                sfor<0, 16>([&](auto ic) {
                    constexpr int j = decltype(ic)::value, ks = j >> 1;
                    lds_wait<7>(fr[j & 7]);
                    if constexpr (j == 0) s0 = MFMA32(fr[0], qf[0], negm);
                    else if constexpr (j == 1) s1 = MFMA32(fr[1], qf[0], negm);
                    else if constexpr ((j & 1) == 0) s0 = MFMA32(fr[j & 7], qf[ks], s0);
                    else s1 = MFMA32(fr[j & 7], qf[ks], s1);
                    constexpr int jn = j + 8;
                    SBAR();
                    if constexpr (jn < 16) lds_rd<BOFF((jn & 1) * 8 + (jn >> 1))>(fr[j & 7], ac);
                    else lds_rd<BOFF(jn)>(fr[j & 7], ac);
                    if constexpr (j == 2) K2_DMA(2);
                    if constexpr (j == 8) K2_DMA(3);
                    if constexpr (j == 14) K2_DMA(4);
                    SBAR();
                });
#define B_MASK()                                                                                        \
    do {                                                                                                \
        if (rel <= 1) {                                                                                 \
            const int lim = 32 * rel + r - 4 * h;                                                       \
            _Pragma("unroll") for (int i = 0; i < 16; ++i) {                                            \
                const int kr = (i & 3) + 8 * (i >> 2);                                                  \
                if (kr > lim) s0[i] = -__builtin_inff();                                                \
                if (kr + 32 > lim) s1[i] = -__builtin_inff();                                           \
            }                                                                                           \
        }                                                                                               \
    } while (0)
                SEG(1);
                B_MASK();
                float ps = 0.f;
                if (t > 0) {
#pragma unroll
                    for (int i = 0; i < 16; ++i) { s0[i] = __builtin_amdgcn_exp2f(s0[i]); ps += s0[i]; }
#pragma unroll
                    for (int i = 0; i < 16; ++i) { s1[i] = __builtin_amdgcn_exp2f(s1[i]); ps += s1[i]; }
                }
                if (t == 0 || __any(!(ps <= 65536.f))) {
                    if (t > 0) {
                        LAS char* sl = lds + (it & 3) * SLOT + lane * 16;
                        s0 = negm; s1 = negm;
#pragma unroll 1
                        for (int ks = 0; ks < 8; ++ks) {
                            bf16x8 qq = qf[0];
#pragma unroll
                            for (int u = 1; u < 8; ++u) qq = (ks == u) ? qf[u] : qq;
                            s0 = MFMA32(*(LAS bf16x8*)(sl + (ks >> 2) * 5120 + (ks & 3) * 1024), qq, s0);
                            s1 = MFMA32(*(LAS bf16x8*)(sl + 10240 + (ks >> 2) * 5120 + (ks & 3) * 1024), qq, s1);
                        }
                        B_MASK();
                    }
                    float mx = s0[0];
#pragma unroll
                    for (int i = 1; i < 16; ++i) mx = fmaxf(mx, s0[i]);
#pragma unroll
                    for (int i = 0; i < 16; ++i) mx = fmaxf(mx, s1[i]);
                    auto rr = __builtin_amdgcn_permlane32_swap(__float_as_uint(mx), __float_as_uint(mx), false, false);
                    mx = fmaxf(__uint_as_float(rr[0]), __uint_as_float(rr[1]));
                    float delta = mx;
                    if (t > 0) {
                        delta = fmaxf(mx, 0.f);
                        const float alpha = __builtin_amdgcn_exp2f(-delta);
                        l *= alpha;
#pragma unroll
                        for (int db = 0; db < 4; ++db)
#pragma unroll
                            for (int i = 0; i < 16; ++i) o[db][i] *= alpha;
                    }
                    const float nm = negm[0] - delta;
#pragma unroll
                    for (int i = 0; i < 16; ++i) negm[i] = nm;
                    ps = 0.f;
#pragma unroll
                    for (int i = 0; i < 16; ++i) { s0[i] = __builtin_amdgcn_exp2f(s0[i] - delta); ps += s0[i]; }
#pragma unroll
                    for (int i = 0; i < 16; ++i) { s1[i] = __builtin_amdgcn_exp2f(s1[i] - delta); ps += s1[i]; }
                }
                l += ps;
                bf16x8 pf[4];
                pf[0] = __builtin_bit_cast(bf16x8, pack8(s0, 0));
                pf[1] = __builtin_bit_cast(bf16x8, pack8(s0, 1));
                pf[2] = __builtin_bit_cast(bf16x8, pack8(s1, 0));
                pf[3] = __builtin_bit_cast(bf16x8, pack8(s1, 1));
                SEG(2);
                if (it + 1 < NIT) { K2_SYNC(it); } else { K2_M0(it + 3); }
                SEG(3);
                sfor<0, 16>([&](auto ic) {
                    constexpr int j = decltype(ic)::value, s4 = j >> 2, db = j & 3;
                    lds_wait<7>(fr[j & 7]);
                    o[db] = MFMA32(fr[j & 7], pf[s4], o[db]);
                    SBAR();
                    if constexpr (j < 8) lds_rd<BOFF(24 + j)>(fr[j & 7], ac);
                    else { constexpr int jn = j - 8; lds_rd<BOFF((jn & 1) * 8 + (jn >> 1))>(fr[j & 7], an); }
                    if constexpr (j == 4) K2_DMA(0);
                    if constexpr (j == 10) K2_DMA(1);
                    SBAR();
                });
                SEG(4);
            }
            auto rr = __builtin_amdgcn_permlane32_swap(__float_as_uint(l), __float_as_uint(l), false, false);
            const float inv = 1.0f / (__uint_as_float(rr[0]) + __uint_as_float(rr[1]));
            char* cp = ws + WS_CTXB + (size_t)(tb32 * 256 + head * 8) * 1024 + lane * 16;
#pragma unroll
            for (int db = 0; db < 4; ++db) {
                st16(cp + (2 * db) * 1024, pack8s(o[db], 0, inv));
                st16(cp + (2 * db + 1) * 1024, pack8s(o[db], 1, inv));
            }
        }
        WAIT_VM(0);
        STAMP(1, 2);
#undef B_MASK
    } else {
        const int tb32 = x * 8 + wa * 2;
        bf16x8 qf[2][2];
        const char* qp = ws + WS_QA + (size_t)((head * 64 + tb32) * 2) * 1024 + lane * 16;
#pragma unroll
        for (int qt = 0; qt < 2; ++qt)
#pragma unroll
            for (int ks = 0; ks < 2; ++ks) qf[qt][ks] = *(const bf16x8*)(qp + (qt * 2 + ks) * 1024);
        f32x16 o[2];
        o[0] = f32x16{}; o[1] = f32x16{};
        float negm[2] = {0.f, 0.f};
        bf16x8 kf[4], vf[4];
        f32x16 s[2][2];
        K2_SYNC(-1);
        lds_rd<AOFF(0)>(kf[0], lbase); lds_rd<AOFF(1)>(kf[1], lbase); lds_rd<AOFF(2)>(kf[2], lbase); lds_rd<AOFF(3)>(kf[3], lbase);
        for (int it = 0; it <= 32; ++it) {
            SEG(0);
            if (it > 0) {
                u32x4 pf[2][2][2];
                unsigned chk = 0;
                sfor<0, 4>([&](auto bc) {
                    constexpr int blk = decltype(bc)::value, kb = blk >> 1, qt = blk & 1;
                    f32x16 e;
#pragma unroll
                    for (int i = 0; i < 8; ++i) e[i] = __builtin_amdgcn_exp2f(s[kb][qt][i]);
                    K2_DMA(blk);
#pragma unroll
                    for (int i = 8; i < 16; ++i) e[i] = __builtin_amdgcn_exp2f(s[kb][qt][i]);
                    if constexpr (blk == 3) K2_DMA(4);
                    pf[kb][qt][0] = pack8(e, 0);
                    pf[kb][qt][1] = pack8(e, 1);
                    chk |= pf[kb][qt][0][0] | pf[kb][qt][0][1] | pf[kb][qt][0][2] | pf[kb][qt][0][3];
                    chk |= pf[kb][qt][1][0] | pf[kb][qt][1][1] | pf[kb][qt][1][2] | pf[kb][qt][1][3];
                });
                const bool first = (it == 1);
                if (first || __any((chk & 0x40004000u) != 0u)) {
#pragma unroll
                    for (int qt = 0; qt < 2; ++qt) {
                        float mx = s[0][qt][0];
#pragma unroll
                        for (int i = 1; i < 16; ++i) mx = fmaxf(mx, s[0][qt][i]);
#pragma unroll
                        for (int i = 0; i < 16; ++i) mx = fmaxf(mx, s[1][qt][i]);
                        auto rr = __builtin_amdgcn_permlane32_swap(__float_as_uint(mx), __float_as_uint(mx), false, false);
                        mx = fmaxf(__uint_as_float(rr[0]), __uint_as_float(rr[1]));
                        float delta = mx + 10.f;
                        if (!first) delta = fmaxf(delta, 0.f);
                        const float tgt = negm[qt] - delta;
                        const unsigned hi = pack2(tgt, 0.f) & 0xFFFFu;
                        const float hif = __uint_as_float(hi << 16);
                        const unsigned lo = pack2(tgt - hif, 0.f) & 0xFFFFu;
                        const float neff = hif + __uint_as_float(lo << 16);
                        delta = negm[qt] - neff;
                        negm[qt] = neff;
                        {
                            u32x4 qq = __builtin_bit_cast(u32x4, qf[qt][1]);
                            qq[2] = h ? 0u : (hi | (lo << 16));
                            qf[qt][1] = __builtin_bit_cast(bf16x8, qq);
                        }
                        if (!first) {
                            const float alpha = __builtin_amdgcn_exp2f(-delta);
#pragma unroll
                            for (int i = 0; i < 16; ++i) o[qt][i] *= alpha;
                        }
#pragma unroll
                        for (int kb = 0; kb < 2; ++kb) {
                            f32x16 e;
#pragma unroll
                            for (int i = 0; i < 16; ++i) e[i] = __builtin_amdgcn_exp2f(s[kb][qt][i] - delta);
                            pf[kb][qt][0] = pack8(e, 0);
                            pf[kb][qt][1] = pack8(e, 1);
                        }
                    }
                }
                SEG(1);
                lds_wait2<4>(vf[0], vf[1]);
                lds_wait2<4>(vf[2], vf[3]);
#pragma unroll
                for (int kb = 0; kb < 2; ++kb)
#pragma unroll
                    for (int s2 = 0; s2 < 2; ++s2)
#pragma unroll
                        for (int qt = 0; qt < 2; ++qt)
                            o[qt] = MFMA32(vf[kb * 2 + s2], __builtin_bit_cast(bf16x8, pf[kb][qt][s2]), o[qt]);
            } else {
                K2_DMA(0); K2_DMA(1); K2_DMA(2); K2_DMA(3); K2_DMA(4);
            }
            SEG(2);
            lds_wait2<0>(kf[0], kf[1]);
            lds_wait2<0>(kf[2], kf[3]);
#pragma unroll
            for (int kb = 0; kb < 2; ++kb)
#pragma unroll
                for (int qt = 0; qt < 2; ++qt) {
                    s[kb][qt] = MFMA32(kf[kb * 2], qf[qt][0], f32x16{});
                    s[kb][qt] = MFMA32(kf[kb * 2 + 1], qf[qt][1], s[kb][qt]);
                }
            SBAR();
            if (it == 32) STAMPW(1, 3, 4);
            SEG(3);
            K2_SYNC(it);
            SEG(4);
            const unsigned ac = lbase + (it & 3) * SLOT;
            const unsigned an = lbase + ((it + 1) & 3) * SLOT;
            lds_rd<AOFF(4)>(vf[0], ac); lds_rd<AOFF(5)>(vf[1], ac); lds_rd<AOFF(6)>(vf[2], ac); lds_rd<AOFF(7)>(vf[3], ac);
            lds_rd<AOFF(0)>(kf[0], an); lds_rd<AOFF(1)>(kf[1], an); lds_rd<AOFF(2)>(kf[2], an); lds_rd<AOFF(3)>(kf[3], an);
        }
        WAIT_VM(0);
#pragma unroll
        for (int qt = 0; qt < 2; ++qt) {
            const unsigned lv = __float_as_uint(o[qt][8]);
            auto rr = __builtin_amdgcn_permlane32_swap(lv, lv, false, false);
            const float lsum = __uint_as_float(rr[1]);
            const float inv = 1.0f / lsum;
            char* cp = ws + WS_CTXA + (size_t)((tb32 + qt) * 40) * 1024;
#pragma unroll
            for (int gq = 0; gq < 3; ++gq) {
                const int d0 = 8 * gq + 4 * h;
                if (d0 < 20) {
                    const int k = head * 20 + d0;
                    u32x2 w;
                    w[0] = pack2(o[qt][4 * gq + 0] * inv, o[qt][4 * gq + 1] * inv);
                    w[1] = pack2(o[qt][4 * gq + 2] * inv, o[qt][4 * gq + 3] * inv);
                    *(u32x2*)(cp + (k >> 4) * 1024 + (r + 32 * ((k >> 3) & 1)) * 16 + (k & 4) * 2) = w;
                }
            }
        }
    }
}

constexpr int G3_STAGE = 49152;
constexpr int G3_LDS = 3 * G3_STAGE;

__global__ __launch_bounds__(512, 2) void k3_oproj(const __hip_bfloat16* __restrict__ Awo, const __hip_bfloat16* __restrict__ Bwo,
                                                   char* __restrict__ ws, __hip_bfloat16* __restrict__ out) {
    extern __shared__ __attribute__((aligned(16))) char smem[];
    LAS char* lds = (LAS char*)smem;
    const int tid = threadIdx.x, lane = tid & 63, wid = __builtin_amdgcn_readfirstlane(tid >> 6);
    const int wm = wid >> 1, wn = wid & 1, h = lane >> 5;
    const int bid = blockIdx.x;
    const bool isB = bid < 200;
    int mt, nt, sp, kt0, nkt, kstot;
    const char* Xc; const char* Wb;
    if (isB) {
        mt = bid & 7; const int rest = bid >> 3; nt = rest % 5; sp = rest / 5;
        kt0 = sp * 13; nkt = sp == 4 ? 12 : 13; kstot = 256;
        Xc = ws + WS_CTXB; Wb = (const char*)Bwo;
    } else {
        const int b2 = bid - 200; mt = b2 & 7; nt = b2 >> 3; sp = 5;
        kt0 = 0; nkt = 10; kstot = 40;
        Xc = ws + WS_CTXA; Wb = (const char*)Awo;
    }
    unsigned woff[2];
#pragma unroll
    for (int i = 0; i < 2; ++i) {
        const int k = (wid * 2 + i) * 4 + (lane >> 4);
        const int p = lane & 15, b = (p >> 2) ^ (k & 3);
        woff[i] = (unsigned)(((kt0 * 64 + k) * 640 + nt * 128 + (b * 4 + (p & 3)) * 8) * 2);
    }
    const unsigned lds0 = (unsigned)(uintptr_t)lds;
    const char* xsb = Xc + (size_t)((mt * 8 + wid) * kstot + kt0 * 4) * 1024 + 4096;
    const unsigned voff = lane * 16;
    woff[1] -= 1024;
    auto piece = [&](int kt, auto ic) {
        constexpr int i = decltype(ic)::value;
        if constexpr (i < 4) dma_s<i * 1024 - 4096>(voff, xsb + (size_t)kt * 4096);
        else dma_s<(i - 4) * 1024>(woff[i - 4], Wb + (size_t)kt * (64u * 640u * 2u));
    };
    const int q = (lane >> 2) & 3, p4 = lane & 3, g16 = (lane >> 4) & 1;
    const int r0 = isB ? 4 * h : 8 * h, r1 = isB ? 8 + 4 * h : 8 * h + 4;
    int wb0[2], wb1[2];
#pragma unroll
    for (int ni = 0; ni < 2; ++ni) {
        const int b = wn * 2 + ni;
        const int c = ((b ^ q) * 64) + g16 * 32 + p4 * 8;
        wb0[ni] = (r0 >> 3) * 6144 + 4096 + ((r0 & 7) + q) * 256 + c;
        wb1[ni] = (r1 >> 3) * 6144 + 4096 + ((r1 & 7) + q) * 256 + c;
    }
    const int xb0 = wm * 2 * 6144 + lane * 16;
    f32x16 acc[2][2];
#pragma unroll
    for (int mi = 0; mi < 2; ++mi)
#pragma unroll
        for (int ni = 0; ni < 2; ++ni) acc[mi][ni] = f32x16{};

    bf16x8 xf[2][4][2], wf[2][4][2];
#define G3_RD(set, bb)                                                                                           \
    do {                                                                                                         \
        _Pragma("unroll") for (int ks = 0; ks < 4; ++ks) {                                                       \
            _Pragma("unroll") for (int ni = 0; ni < 2; ++ni)                                                     \
                wf[set][ks][ni] = tr_frag((bb) + wb0[ni] + ks * 12288, (bb) + wb1[ni] + ks * 12288);             \
            _Pragma("unroll") for (int mi = 0; mi < 2; ++mi)                                                     \
                xf[set][ks][mi] = *(LAS bf16x8*)((bb) + xb0 + mi * 6144 + ks * 1024);                            \
        }                                                                                                        \
    } while (0)
#define G3_STEP(set, kt)                                                                                         \
    do {                                                                                                         \
        if ((kt) + 2 < nkt) { WAIT_VM(6); } else { WAIT_VM(0); }                                                 \
        LGKM0();                                                                                                 \
        BARRIER();                                                                                               \
        const int b0_ = (kt) % 3, b1_ = ((kt) + 1) % 3;                                                          \
        set_m0(lds0 + b0_ * G3_STAGE + wid * 6144 + 4096);                                                       \
        if ((kt) + 1 < nkt) G3_RD((set) ^ 1, lds + b1_ * G3_STAGE);                                              \
        SBAR();                                                                                                  \
        const bool on_ = (kt) + 3 < nkt;                                                                         \
        sfor<0, 8>([&](auto jc) {                                                                                \
            constexpr int j = decltype(jc)::value, ks = j >> 1, mi = j & 1;                                      \
            _Pragma("unroll") for (int ni = 0; ni < 2; ++ni)                                                     \
                acc[mi][ni] = MFMA32(wf[set][ks][ni], xf[set][ks][mi], acc[mi][ni]);                             \
            if constexpr (j < 6) { SBAR(); if (on_) piece((kt) + 3, jc); SBAR(); }                               \
        });                                                                                                      \
        SBAR();                                                                                                  \
    } while (0)
    STAMP(2, 0);
#pragma unroll
    for (int t = 0; t < 3; ++t) {
        set_m0(lds0 + t * G3_STAGE + wid * 6144 + 4096);
        sfor<0, 6>([&](auto ic) { piece(t, ic); });
    }
    WAIT_VM(12);
    BARRIER();
    STAMP(2, 1);
    G3_RD(0, lds);
    int kt = 0;
#pragma unroll 1
    for (; kt + 1 < nkt; kt += 2) { G3_STEP(0, kt); G3_STEP(1, kt + 1); }
    if (kt < nkt) G3_STEP(0, kt);
#undef G3_RD
#undef G3_STEP
    STAMP(2, 2);
    const int r = lane & 31;
    if (!isB) {
#pragma unroll
        for (int mi = 0; mi < 2; ++mi)
#pragma unroll
            for (int ni = 0; ni < 2; ++ni) {
                char* op = (char*)out + (size_t)((mt * 8 + wm * 2 + mi) * 32 + r) * 2560 + ((nt * 4 + wn * 2 + ni) * 32 + 4 * h) * 2;
#pragma unroll
                for (int gq = 0; gq < 4; ++gq) {
                    u32x2 o;
                    o[0] = pack2(acc[mi][ni][4 * gq], acc[mi][ni][4 * gq + 1]);
                    o[1] = pack2(acc[mi][ni][4 * gq + 2], acc[mi][ni][4 * gq + 3]);
                    *(u32x2*)(op + gq * 16) = o;
                }
            }
        STAMP(2, 3);
        return;
    }
#pragma unroll
    for (int mi = 0; mi < 2; ++mi)
#pragma unroll
        for (int ni = 0; ni < 2; ++ni) {
            const int mt32 = mt * 8 + wm * 2 + mi, nt32 = nt * 4 + wn * 2 + ni;
            char* dst = ws + WS_PART + (size_t)sp * SLAB + (size_t)(mt32 * 20 + nt32) * 4096 + lane * 16;
#pragma unroll
            for (int gq = 0; gq < 4; ++gq) {
                f32x4 v = {acc[mi][ni][4 * gq], acc[mi][ni][4 * gq + 1], acc[mi][ni][4 * gq + 2], acc[mi][ni][4 * gq + 3]};
                asm volatile("global_store_dwordx4 %0, %1, off sc1\n\ts_nop 1" :: "v"(dst + gq * 1024), "v"(v) : "memory");
            }
        }
    asm volatile("s_waitcnt vmcnt(0)" ::: "memory");
    BARRIER();
    unsigned* cnt = (unsigned*)(ws + WS_CNT) + (mt * 5 + nt) * 64;
    if (wid == 0) {
        if (lane == 0) __hip_atomic_fetch_add(cnt, 1u, __ATOMIC_RELAXED, __HIP_MEMORY_SCOPE_AGENT);
        for (unsigned spins = 0; spins < (1u << 24); ++spins) {
            const unsigned c = __builtin_amdgcn_readfirstlane(__hip_atomic_load(cnt, __ATOMIC_RELAXED, __HIP_MEMORY_SCOPE_AGENT));
            if (c >= (unsigned)NSPLIT) break;
            __builtin_amdgcn_s_sleep(1);
        }
        __builtin_amdgcn_fence(__ATOMIC_ACQUIRE, "agent");
        asm volatile("s_waitcnt vmcnt(0)" ::: "memory");
    }
    BARRIER();
    const int st = sp + 5 * wid;
    if (st < 32) {
        const int mt32 = mt * 8 + (st >> 2), nt32 = nt * 4 + (st & 3);
        const char* src = ws + WS_PART + (size_t)(mt32 * 20 + nt32) * 4096 + lane * 16;
        f32x4 v[4][NSPLIT];
#pragma unroll
        for (int gq = 0; gq < 4; ++gq)
#pragma unroll
            for (int s = 0; s < NSPLIT; ++s) v[gq][s] = *(const f32x4*)(src + (size_t)s * SLAB + gq * 1024);
        char* op = (char*)out + (size_t)(mt32 * 32 + r) * 2560 + (640 + nt32 * 32 + 4 * h) * 2;
#pragma unroll
        for (int gq = 0; gq < 4; ++gq) {
            const f32x4 t = ((v[gq][0] + v[gq][1]) + (v[gq][2] + v[gq][3])) + v[gq][4];
            u32x2 o;
            o[0] = pack2(t[0], t[1]);
            o[1] = pack2(t[2], t[3]);
            *(u32x2*)(op + gq * 16) = o;
        }
    }
    STAMP(2, 3);
}

extern "C" void kernel_launch(void* const* d_in, const int* in_sizes, int n_in,
                              void* d_out, int out_size, void* d_ws, size_t ws_size,
                              hipStream_t stream) {
    (void)in_sizes; (void)n_in; (void)out_size; (void)ws_size;
    const __hip_bfloat16* Ax = (const __hip_bfloat16*)d_in[0];
    const __hip_bfloat16* Awqkv = (const __hip_bfloat16*)d_in[1];
    const __hip_bfloat16* Awo = (const __hip_bfloat16*)d_in[2];
    const __hip_bfloat16* Bx = (const __hip_bfloat16*)d_in[3];
    const __hip_bfloat16* Bckv = (const __hip_bfloat16*)d_in[4];
    const __hip_bfloat16* Bwq = (const __hip_bfloat16*)d_in[5];
    const __hip_bfloat16* Bwuk = (const __hip_bfloat16*)d_in[6];
    const __hip_bfloat16* Bwuv = (const __hip_bfloat16*)d_in[7];
    const __hip_bfloat16* Bwo = (const __hip_bfloat16*)d_in[8];
    char* ws = (char*)d_ws;
    (void)hipFuncSetAttribute((const void*)k1_proj, hipFuncAttributeMaxDynamicSharedMemorySize, G1_LDS);
    (void)hipFuncSetAttribute((const void*)k2_attn, hipFuncAttributeMaxDynamicSharedMemorySize, K2_LDS);
    (void)hipFuncSetAttribute((const void*)k3_oproj, hipFuncAttributeMaxDynamicSharedMemorySize, G3_LDS);
    k1_proj<<<256, 512, G1_LDS, stream>>>(Ax, Awqkv, Bx, Bckv, Bwq, Bwuk, Bwuv, Awo, Bwo, ws);
    k2_attn<<<256, 512, K2_LDS, stream>>>(ws);
    k3_oproj<<<240, 512, G3_LDS, stream>>>(Awo, Bwo, ws, (__hip_bfloat16*)d_out);
}
```

```cpp
#include <hip/hip_runtime.h>
#include <hip/hip_bf16.h>
#include <stdint.h>
#include <type_traits>

typedef short s16x4 __attribute__((ext_vector_type(4)));
typedef short bf16x8 __attribute__((ext_vector_type(8)));
typedef float f32x16 __attribute__((ext_vector_type(16)));
typedef float f32x4 __attribute__((ext_vector_type(4)));
typedef float f32x2 __attribute__((ext_vector_type(2)));
typedef unsigned u32x4 __attribute__((ext_vector_type(4)));
typedef unsigned u32x2 __attribute__((ext_vector_type(2)));
typedef __bf16 bf16x2_t __attribute__((ext_vector_type(2)));
#define LAS __attribute__((address_space(3)))
#define DI __device__ __forceinline__
#define MFMA32(a, b, c) __builtin_amdgcn_mfma_f32_32x32x16_bf16((a), (b), (c), 0, 0, 0)
#define MFMA16(a, b, c) __builtin_amdgcn_mfma_f32_16x16x32_bf16((a), (b), (c), 0, 0, 0)
#define CFENCE() asm volatile("" ::: "memory")
#define BARRIER() do { CFENCE(); __builtin_amdgcn_s_barrier(); CFENCE(); } while (0)
#define WAIT_VM(n) asm volatile("s_waitcnt vmcnt(" #n ") lgkmcnt(0)" ::: "memory")
#define SBAR() __builtin_amdgcn_sched_barrier(0)

DI unsigned pack2(float a, float b) {
    f32x2 v = {a, b};
    bf16x2_t r = __builtin_convertvector(v, bf16x2_t);
    return __builtin_bit_cast(unsigned, r);
}
DI u32x4 pack8(const f32x16& x, int s) {
    u32x4 w;
    w[0] = pack2(x[8 * s + 0], x[8 * s + 1]);
    w[1] = pack2(x[8 * s + 2], x[8 * s + 3]);
    w[2] = pack2(x[8 * s + 4], x[8 * s + 5]);
    w[3] = pack2(x[8 * s + 6], x[8 * s + 7]);
    return w;
}
DI u32x4 pack8s(const f32x16& x, int s, float c) {
    u32x4 w;
    w[0] = pack2(x[8 * s + 0] * c, x[8 * s + 1] * c);
    w[1] = pack2(x[8 * s + 2] * c, x[8 * s + 3] * c);
    w[2] = pack2(x[8 * s + 4] * c, x[8 * s + 5] * c);
    w[3] = pack2(x[8 * s + 6] * c, x[8 * s + 7] * c);
    return w;
}
DI void glds16(const void* g, LAS char* l) {
    __builtin_amdgcn_global_load_lds((const unsigned*)g, (LAS unsigned*)l, 16, 0, 0);
}
DI bf16x8 tr_frag(LAS char* p0, LAS char* p1) {
    s16x4 lo = __builtin_amdgcn_ds_read_tr16_b64_v4i16((LAS s16x4*)p0);
    s16x4 hi = __builtin_amdgcn_ds_read_tr16_b64_v4i16((LAS s16x4*)p1);
    return __builtin_shufflevector(lo, hi, 0, 1, 2, 3, 4, 5, 6, 7);
}

#define STMODE 2
DI void st16(void* p, u32x4 v) {
    asm volatile("global_store_dwordx4 %0, %1, off sc1\n\ts_nop 1" :: "v"(p), "v"(v) : "memory");
}
template <int I, int N, class F> DI void sfor(F&& f) {
    if constexpr (I < N) { f(std::integral_constant<int, I>{}); sfor<I + 1, N>(f); }
}
template <int OFF> DI void lds_rd(bf16x8& d, unsigned addr) {
    asm volatile("ds_read_b128 %0, %1 offset:%2" : "=v"(d) : "v"(addr), "i"(OFF));
}
template <int N> DI void lds_wait(bf16x8& d) { asm volatile("s_waitcnt lgkmcnt(%1)" : "+v"(d) : "i"(N)); }
template <int N> DI void lds_wait2(bf16x8& d, bf16x8& e) { asm volatile("s_waitcnt lgkmcnt(%2)" : "+v"(d), "+v"(e) : "i"(N)); }
#define LGKM0() __builtin_amdgcn_s_waitcnt(0xC07F)
DI void set_m0(unsigned v) { asm volatile("s_mov_b32 m0, %0\n\ts_nop 0" :: "s"(v) : "memory"); }
template <int OFF> DI void dma_s(unsigned voff, const char* sbase) {
    asm volatile("global_load_lds_dwordx4 %0, %1 offset:%2" :: "v"(voff), "s"(sbase), "i"(OFF) : "memory");
}

#define ABL_NOPIECE 0
#define ABL_NOBAR 0
#define ABL_T0 0
#define ABL_K2 0
#define ABL_NODMA 0
#define ABL_NOMFMA 0
#define SEG(i) do { } while (0)
#define STAMP(kid, idx) do { } while (0)
#define STAMPW(kid, idx, w) do { } while (0)
constexpr size_t WS_KVB = 0;
constexpr size_t WS_QB = WS_KVB + (size_t)32 * 1048576;
constexpr size_t WS_KVA = WS_QB + (size_t)16 * 1048576;
constexpr size_t WS_QA = WS_KVA + (size_t)8 * 1048576;
constexpr size_t WS_CTXB = WS_QA + (size_t)4 * 1048576;
constexpr size_t WS_CTXA = WS_CTXB + (size_t)16 * 1048576;
constexpr size_t WS_PART = WS_CTXA + (size_t)4 * 1048576;
constexpr size_t WS_CNT = WS_PART + (size_t)32 * 1048576;
constexpr int NSPLIT = 5;
constexpr size_t SLAB = (size_t)64 * 20 * 4096;

constexpr float CB = 0.08838834764831845f * 1.4426950408889634f;
constexpr float CA = 0.22360679774997896f * 1.4426950408889634f;

#define PREFETCH 1
constexpr int G1_LDS = 2 * 65536 + 2048;
enum { EP_BK = 0, EP_BV = 1, EP_BQ = 2, EP_AQK = 3, EP_AV = 4 };

struct G1Src { unsigned xoff[4], woff[4]; const char* Xb; const char* Wb; unsigned wstep; int nkt; };
DI void g1_src(G1Src& s, const __hip_bfloat16* X, const __hip_bfloat16* W, int K, int ldw, int m0, int n0, int nlim, int wid, int lane) {
#pragma unroll
    for (int i = 0; i < 4; ++i) {
        const int row = (wid * 4 + i) * 8 + (lane >> 3);
        const int c = (lane & 7) ^ ((row >> 1) & 7);
        s.xoff[i] = (unsigned)(((m0 + row) * K + c * 8) * 2) + 4096 - i * 1024;
        const int k = (wid * 4 + i) * 2 + (lane >> 5);
        const int p = lane & 31, b = (p >> 2) ^ (k & 3);
        int col = n0 + (b * 4 + ((p & 3) ^ ((wid & 1) << 1))) * 8;
        col = col < nlim ? col : nlim - 8;
        s.woff[i] = (unsigned)((k * ldw + col) * 2) - i * 1024;
    }
    s.Xb = (const char*)X; s.Wb = (const char*)W; s.wstep = (unsigned)(64 * ldw * 2); s.nkt = K >> 6;
}
template <int I> DI void g1_piece(const G1Src& s, int kt) {
    if constexpr (I < 4) dma_s<I * 1024 - 4096>(s.xoff[I], s.Xb + (size_t)kt * 128);
    else dma_s<(I - 4) * 1024>(s.woff[I - 4], s.Wb + (size_t)kt * s.wstep);
}

template <bool TR>
DI void g1_loop(f32x4 (&acc)[8][4], bf16x8 (&xs)[2][4], bf16x8 (&wsb)[2][4], const G1Src& cur, const G1Src& nxt, bool has_next, bool first,
                LAS char* lds, int wid, int lane) {
    const int wm = wid >> 2, wn = wid & 3, r16 = lane & 15, g = lane >> 4;
    const unsigned lds0 = (unsigned)(uintptr_t)lds;
    const int nkt = cur.nkt;
    int xb[2];
#pragma unroll
    for (int st = 0; st < 2; ++st) xb[st] = wm * 4 * 8192 + r16 * 128 + (((4 * st + g) ^ (r16 >> 1)) * 16);
    const int q = (lane >> 2) & 3, p4 = lane & 3;
    int wb[4];
#pragma unroll
    for (int nt = 0; nt < 4; ++nt) {
        const int b = wn * 2 + (nt >> 1);
        wb[nt] = g * 8192 + 4096 + q * 512 + ((b ^ q) * 64) + (((nt & 1) ^ (g & 1)) * 32) + p4 * 8;
    }
#pragma unroll
    for (int mt = 0; mt < 8; ++mt)
#pragma unroll
        for (int nt = 0; nt < 4; ++nt) acc[mt][nt] = f32x4{};
#define G1_RX(set, bb, st, mh)                                                                                   \
    _Pragma("unroll") for (int m4 = 0; m4 < 4; ++m4)                                                             \
        xs[set][m4] = *(LAS bf16x8*)((bb) + xb[st] + ((mh) * 2 + (m4 >> 1)) * 8192 + (m4 & 1) * 2048)
#define G1_RW(bb, st)                                                                                            \
    _Pragma("unroll") for (int nt = 0; nt < 4; ++nt)                                                             \
        wsb[st][nt] = tr_frag((bb) + wb[nt] + (st) * 32768, (bb) + wb[nt] + (st) * 32768 + 2048)
#define G1_MM(set, st, mh, on, kn, p0, pe)                                                                       \
    sfor<0, 4>([&](auto mc) {                                                                                    \
        constexpr int m4 = decltype(mc)::value;                                                                  \
        _Pragma("unroll") for (int nt = 0; nt < 4; ++nt)                                                         \
            acc[(mh) * 4 + m4][nt] = TR ? MFMA16(wsb[st][nt], xs[set][m4], acc[(mh) * 4 + m4][nt])               \
                                        : MFMA16(xs[set][m4], wsb[st][nt], acc[(mh) * 4 + m4][nt]);              \
        if constexpr ((p0) + m4 < (pe)) {                                                                        \
            SBAR();                                                                                              \
            if (on) { if ((kn) < nkt) g1_piece<(p0) + m4>(cur, (kn)); else g1_piece<(p0) + m4>(nxt, (kn) - nkt); } \
            SBAR();                                                                                              \
        }                                                                                                        \
    })
    if (first) { G1_RW(lds, 0); G1_RX(0, lds, 0, 0); }
#pragma unroll 1
    for (int kt = 0; kt < nkt; ++kt) {
        LAS char* bb = lds + (kt & 1) * 65536;
        LAS char* bn = lds + ((kt + 1) & 1) * 65536;
        const bool on1 = !(first && kt == 0) && (kt + 1 < nkt || has_next);
        const bool on2 = kt + 2 < nkt || has_next;
        LGKM0(); G1_RX(1, bb, 0, 1); SBAR(); G1_MM(0, 0, 0, on1, kt + 1, 4, 8); SBAR();
        LGKM0(); G1_RW(bb, 1); G1_RX(0, bb, 1, 0); SBAR(); G1_MM(1, 0, 1, false, 0, 0, 0); SBAR();
        LGKM0(); G1_RX(1, bb, 1, 1); SBAR(); G1_MM(0, 1, 0, false, 0, 0, 0); SBAR();
        LGKM0(); WAIT_VM(0);
        BARRIER();
        set_m0(lds0 + (kt & 1) * 65536 + wid * 8192 + 4096);
        if (kt + 1 < nkt || has_next) { G1_RW(bn, 0); G1_RX(0, bn, 0, 0); }
        SBAR(); G1_MM(1, 1, 1, on2, kt + 2, 0, 4); SBAR();
    }
#undef G1_RX
#undef G1_RW
#undef G1_MM
}

DI void st8(void* p, u32x2 v) {
    asm volatile("global_store_dwordx2 %0, %1, off sc1\n\ts_nop 1" :: "v"(p), "v"(v) : "memory");
}

template <int EP>
DI void g1_epi(const f32x4 (&acc)[8][4], int m0, int n0, int nlim, int nsec, char* __restrict__ ws, int wid, int lane) {
    const int wm = wid >> 2, wn = wid & 3, c = lane & 15, g = lane >> 4;
#pragma unroll
    for (int mt = 0; mt < 8; ++mt) {
        const int tb = (m0 + wm * 128 + mt * 16) >> 5;
        const int tile = tb >> 1, kb = tb & 1, th = mt & 1;
#pragma unroll
        for (int nt = 0; nt < 4; ++nt) {
            const int nb = n0 + wn * 64 + nt * 16;
            if (nb >= nlim) continue;
            const f32x4 a = acc[mt][nt];
            if constexpr (EP == EP_BK || EP == EP_BQ) {
                const int head = nb >> 7, ks = (nb >> 4) & 7;
                char* base = EP == EP_BK ? ws + WS_KVB + (size_t)head * 1048576 + tile * 32768 + (kb * 8 + ks) * 1024
                                         : ws + WS_QB + (size_t)((head * 64 + tb) * 8 + ks) * 1024;
                const float sc = EP == EP_BK ? 1.0f : CB;
                u32x2 w;
                w[0] = pack2(a[0] * sc, a[1] * sc);
                w[1] = pack2(a[2] * sc, a[3] * sc);
                st8(base + (th * 16 + c + 32 * (g >> 1)) * 16 + (g & 1) * 8, w);
            } else if constexpr (EP == EP_BV) {
                const int head = nb >> 7, db = (nb >> 5) & 3;
                char* base = ws + WS_KVB + (size_t)head * 1048576 + tile * 32768 + 16384 + ((kb * 2 + th) * 4 + db) * 1024;
                u32x2 w;
                w[0] = pack2(a[0], a[1]);
                w[1] = pack2(a[2], a[3]);
                st8(base + ((nt & 1) * 16 + c + 32 * (g & 1)) * 16 + (g >> 1) * 8, w);
            } else if constexpr (EP == EP_AQK) {
                const bool isq = nb < 640;
                const float sc = isq ? CA : 1.0f;
                const int r = th * 16 + c;
                const int n = nb - (isq ? 0 : 640) + 4 * g;
                const int head = n / 20, d0 = n - head * 20;
                char* base = isq ? ws + WS_QA + (size_t)((head * 64 + tb) * 2) * 1024
                                 : ws + WS_KVA + (size_t)head * 262144 + tile * 8192 + kb * 2048;
                char* dst = base + (d0 >> 4) * 1024 + (r + 32 * ((d0 >> 3) & 1)) * 16 + (d0 & 4) * 2;
                u32x2 w;
                w[0] = pack2(a[0] * sc, a[1] * sc);
                w[1] = pack2(a[2] * sc, a[3] * sc);
                *(u32x2*)dst = w;
                if (d0 == 16) {
                    *(u32x2*)(dst + 8) = (u32x2){isq ? 0u : 0x3F803F80u, 0u};
                    *(u32x4*)(base + 1024 + (r + 32) * 16) = (u32x4){0u, 0u, 0u, 0u};
                }
            } else {
                const int n = nb - nsec + c;
                const int head = n / 20, d = n - head * 20;
                char* dst = ws + WS_KVA + (size_t)head * 262144 + tile * 8192 + 4096 + (kb * 2 + th) * 1024 + (d + 32 * (g & 1)) * 16 + (g >> 1) * 8;
                u32x2 w;
                w[0] = pack2(a[0], a[1]);
                w[1] = pack2(a[2], a[3]);
                *(u32x2*)dst = w;
                if (d == 19) *(u32x2*)(dst + 16) = (u32x2){0x3F803F80u, 0x3F803F80u};
            }
        }
    }
}

__global__ __launch_bounds__(512, 2) void k1_proj(const __hip_bfloat16* __restrict__ Ax, const __hip_bfloat16* __restrict__ Awqkv,
                                                  const __hip_bfloat16* __restrict__ Bx, const __hip_bfloat16* __restrict__ Bckv,
                                                  const __hip_bfloat16* __restrict__ Bwq, const __hip_bfloat16* __restrict__ Bwuk,
                                                  const __hip_bfloat16* __restrict__ Bwuv, const __hip_bfloat16* __restrict__ Awo,
                                                  const __hip_bfloat16* __restrict__ Bwo, char* __restrict__ ws) {
    extern __shared__ __attribute__((aligned(16))) char smem[];
    LAS char* lds = (LAS char*)smem;
    const int tid = threadIdx.x, lane = tid & 63, wid = __builtin_amdgcn_readfirstlane(tid >> 6);
    const int bid = blockIdx.x;
    const int m0 = ((bid >> 3) & 7) * 256;
    const int c1 = (bid & 7) + 8 * (bid >> 6);
    const int c2 = c1;
    const bool has2 = c2 < 24;
    G1Src s1, s2;
    if (c1 < 16) g1_src(s1, Bckv, Bwuk, 512, 4096, m0, c1 * 256, 4096, wid, lane);
    else g1_src(s1, Bckv, Bwuv, 512, 4096, m0, (c1 - 16) * 256, 4096, wid, lane);
    const int n2 = c2 < 16 ? c2 * 256 : (c2 - 16) * 256;
    if (c2 < 16) g1_src(s2, Bx, Bwq, 640, 4096, m0, n2, 4096, wid, lane);
    else g1_src(s2, Ax, Awqkv, 640, 1920, m0, has2 ? n2 : 0, c2 < 21 ? 1280 : 1920, wid, lane);

    f32x4 acc[8][4];
    bf16x8 xf[2][4], wf[2][4];
    const unsigned lds0 = (unsigned)(uintptr_t)lds;
    if (bid == 255 && tid < 40) ((unsigned*)(ws + WS_CNT))[tid * 64] = 0u;
    STAMP(0, 0);
    set_m0(lds0 + wid * 8192 + 4096);
    sfor<0, 8>([&](auto ic) { g1_piece<decltype(ic)::value>(s1, 0); });
    set_m0(lds0 + 65536 + wid * 8192 + 4096);
    sfor<0, 8>([&](auto ic) { g1_piece<decltype(ic)::value>(s1, 1); });
    {
        set_m0(lds0 + 131072 + wid * 256);
        const unsigned gw = (unsigned)(bid * 8 + wid) * 64 + lane;
#define PF(ptr, nbytes) do { constexpr unsigned R_ = 131072u / ((nbytes) / 128u);                                     \
            const unsigned ln_ = gw / R_;                                                                                \
            const unsigned off_ = (gw % R_ == 0u && ln_ * 128u < (nbytes)) ? ln_ * 128u : 0u;                             \
            asm volatile("global_load_lds_dword %0, %1" :: "v"(off_), "s"((const char*)(ptr)) : "memory"); } while (0)
        PF(Bckv, 2048u * 512u * 2u); PF(Bwuk, 512u * 4096u * 2u); PF(Bwuv, 512u * 4096u * 2u);
        PF(Bx, 2048u * 640u * 2u); PF(Bwq, 640u * 4096u * 2u); PF(Ax, 2048u * 640u * 2u); PF(Awqkv, 640u * 1920u * 2u);
        PF(Bwo, 4096u * 640u * 2u); PF(Awo, 640u * 640u * 2u);
#undef PF
    }
    WAIT_VM(17);
    BARRIER();
    STAMP(0, 1);
    if (c1 < 16) { g1_loop<true>(acc, xf, wf, s1, s2, has2, true, lds, wid, lane); g1_epi<EP_BK>(acc, m0, c1 * 256, 4096, 0, ws, wid, lane); }
    else { g1_loop<false>(acc, xf, wf, s1, s2, has2, true, lds, wid, lane); g1_epi<EP_BV>(acc, m0, (c1 - 16) * 256, 4096, 0, ws, wid, lane); }
    STAMP(0, 2);
    if (c2 < 21) {
        g1_loop<true>(acc, xf, wf, s2, s2, false, false, lds, wid, lane);
        if (c2 < 16) g1_epi<EP_BQ>(acc, m0, n2, 4096, 0, ws, wid, lane);
        else g1_epi<EP_AQK>(acc, m0, n2, 1280, 0, ws, wid, lane);
    } else if (has2) {
        g1_loop<false>(acc, xf, wf, s2, s2, false, false, lds, wid, lane);
        g1_epi<EP_AV>(acc, m0, n2, 1920, 1280, ws, wid, lane);
    }
    STAMP(0, 3);
}

constexpr int SLOT = 40960;
constexpr int NSLOT = 4;
constexpr int K2_LDS = NSLOT * SLOT;
constexpr int NIT = 34;

__global__ __launch_bounds__(512, 2) void k2_attn(char* __restrict__ ws) {
    extern __shared__ __attribute__((aligned(16))) char smem[];
    LAS char* lds = (LAS char*)smem;
    const int tid = threadIdx.x, lane = tid & 63, wid = __builtin_amdgcn_readfirstlane(tid >> 6);
    const int r = lane & 31, h = lane >> 5;
    const int g = blockIdx.x, xcd = g & 7, idx = g >> 3;
    const int head = xcd * 4 + (idx >> 3), x = idx & 7;
    const char* kvb = ws + WS_KVB + (size_t)head * 1048576;
    const char* kva = ws + WS_KVA + (size_t)head * 262144;
    const int n1 = 32 - 2 * x;
    const unsigned lbase = (unsigned)(uintptr_t)lds + lane * 16;

    STAMP(1, 0);
    const unsigned lds0 = (unsigned)(uintptr_t)lds;
    const unsigned voff = lane * 16;
    const int wa = wid & 3;
    const char* sbB = kvb;
    const char* sbA = kva;
#define K2_M0(tt)                                                                           \
    do {                                                                                    \
        const int t3_ = (tt) < NIT ? (tt) : NIT - 1;                                        \
        const int tb3_ = t3_ < n1 ? t3_ : t3_ - n1;                                         \
        const int ta3_ = t3_ < 32 ? t3_ : 31;                                               \
        sbB = kvb + (size_t)tb3_ * 32768 + wid * 4096 + 2048;                               \
        sbA = kva + (size_t)ta3_ * 8192 + wid * 1024 - 2048;                                \
        set_m0(lds0 + ((tt) < NIT ? ((tt) & 3) : 2 + ((tt) & 1)) * SLOT + wid * 5120 + 2048); \
    } while (0)
#define K2_DMA(i) do { SBAR(); if constexpr ((i) < 4) dma_s<(i) * 1024 - 2048>(voff, sbB); else dma_s<2048>(voff, sbA); SBAR(); } while (0)
#define K2_STAGE_ALL(tt) do { K2_M0(tt); K2_DMA(0); K2_DMA(1); K2_DMA(2); K2_DMA(3); K2_DMA(4); } while (0)
#define BOFF(f) (((f) >> 2) * 5120 + ((f) & 3) * 1024)
#define AOFF(a) ((a) * 5120 + 4096)
    K2_STAGE_ALL(0);
    K2_STAGE_ALL(1);

#define K2_SYNC(it) do { WAIT_VM(5); BARRIER(); K2_M0((it) + 3); } while (0)

    if (wid < 4) {
        bf16x8 fr[8];
        K2_SYNC(-1);
        STAMP(1, 1);
        sfor<0, 8>([&](auto ic) { constexpr int j = decltype(ic)::value; lds_rd<BOFF((j & 1) * 8 + (j >> 1))>(fr[j], lbase); });
        K2_DMA(0); K2_DMA(1);
        int it = 0;
        for (int blk = 0; blk < 2; ++blk) {
            const int qb = blk == 0 ? 15 - x : x;
            const int nt = 2 * qb + 2;
            const int q0 = qb * 128 + wid * 32;
            const int tb32 = q0 >> 5;
            bf16x8 qf[8];
            const char* qp = ws + WS_QB + (size_t)((head * 64 + tb32) * 8) * 1024 + lane * 16;
#pragma unroll
            for (int ks = 0; ks < 8; ++ks) qf[ks] = *(const bf16x8*)(qp + ks * 1024);
            f32x16 o[4];
#pragma unroll
            for (int db = 0; db < 4; ++db) o[db] = f32x16{};
            f32x16 negm = f32x16{};
            float l = 0.f;
            for (int t = 0; t < nt; ++t, ++it) {
                const int rel = (q0 - 64 * t) >> 5;
                const unsigned ac = lbase + (it & 3) * SLOT;
                const unsigned an = lbase + ((it + 1) & 3) * SLOT;
                f32x16 s0, s1;
                SEG(0);
                sfor<0, 16>([&](auto ic) {
                    constexpr int j = decltype(ic)::value, ks = j >> 1;
                    lds_wait<7>(fr[j & 7]);
                    if constexpr (j == 0) s0 = MFMA32(fr[0], qf[0], negm);
                    else if constexpr (j == 1) s1 = MFMA32(fr[1], qf[0], negm);
                    else if constexpr ((j & 1) == 0) s0 = MFMA32(fr[j & 7], qf[ks], s0);
                    else s1 = MFMA32(fr[j & 7], qf[ks], s1);
                    constexpr int jn = j + 8;
                    SBAR();
                    if constexpr (jn < 16) lds_rd<BOFF((jn & 1) * 8 + (jn >> 1))>(fr[j & 7], ac);
                    else lds_rd<BOFF(jn)>(fr[j & 7], ac);
                    if constexpr (j == 2) K2_DMA(2);
                    if constexpr (j == 8) K2_DMA(3);
                    if constexpr (j == 14) K2_DMA(4);
                    SBAR();
                });
#define B_MASK()                                                                                        \
    do {                                                                                                \
        if (rel <= 1) {                                                                                 \
            const int lim = 32 * rel + r - 4 * h;                                                       \
            _Pragma("unroll") for (int i = 0; i < 16; ++i) {                                            \
                const int kr = (i & 3) + 8 * (i >> 2);                                                  \
                if (kr > lim) s0[i] = -__builtin_inff();                                                \
                if (kr + 32 > lim) s1[i] = -__builtin_inff();                                           \
            }                                                                                           \
        }                                                                                               \
    } while (0)
                SEG(1);
                B_MASK();
                float ps = 0.f;
                if (t > 0) {
#pragma unroll
                    for (int i = 0; i < 16; ++i) { s0[i] = __builtin_amdgcn_exp2f(s0[i]); ps += s0[i]; }
#pragma unroll
                    for (int i = 0; i < 16; ++i) { s1[i] = __builtin_amdgcn_exp2f(s1[i]); ps += s1[i]; }
                }
                if (t == 0 || __any(!(ps <= 65536.f))) {
                    if (t > 0) {
                        LAS char* sl = lds + (it & 3) * SLOT + lane * 16;
                        s0 = negm; s1 = negm;
#pragma unroll 1
                        for (int ks = 0; ks < 8; ++ks) {
                            bf16x8 qq = qf[0];
#pragma unroll
                            for (int u = 1; u < 8; ++u) qq = (ks == u) ? qf[u] : qq;
                            s0 = MFMA32(*(LAS bf16x8*)(sl + (ks >> 2) * 5120 + (ks & 3) * 1024), qq, s0);
                            s1 = MFMA32(*(LAS bf16x8*)(sl + 10240 + (ks >> 2) * 5120 + (ks & 3) * 1024), qq, s1);
                        }
                        B_MASK();
                    }
                    float mx = s0[0];
#pragma unroll
                    for (int i = 1; i < 16; ++i) mx = fmaxf(mx, s0[i]);
#pragma unroll
                    for (int i = 0; i < 16; ++i) mx = fmaxf(mx, s1[i]);
                    auto rr = __builtin_amdgcn_permlane32_swap(__float_as_uint(mx), __float_as_uint(mx), false, false);
                    mx = fmaxf(__uint_as_float(rr[0]), __uint_as_float(rr[1]));
                    float delta = mx;
                    if (t > 0) {
                        delta = fmaxf(mx, 0.f);
                        const float alpha = __builtin_amdgcn_exp2f(-delta);
                        l *= alpha;
#pragma unroll
                        for (int db = 0; db < 4; ++db)
#pragma unroll
                            for (int i = 0; i < 16; ++i) o[db][i] *= alpha;
                    }
                    const float nm = negm[0] - delta;
#pragma unroll
                    for (int i = 0; i < 16; ++i) negm[i] = nm;
                    ps = 0.f;
#pragma unroll
                    for (int i = 0; i < 16; ++i) { s0[i] = __builtin_amdgcn_exp2f(s0[i] - delta); ps += s0[i]; }
#pragma unroll
                    for (int i = 0; i < 16; ++i) { s1[i] = __builtin_amdgcn_exp2f(s1[i] - delta); ps += s1[i]; }
                }
                l += ps;
                bf16x8 pf[4];
                pf[0] = __builtin_bit_cast(bf16x8, pack8(s0, 0));
                pf[1] = __builtin_bit_cast(bf16x8, pack8(s0, 1));
                pf[2] = __builtin_bit_cast(bf16x8, pack8(s1, 0));
                pf[3] = __builtin_bit_cast(bf16x8, pack8(s1, 1));
                SEG(2);
                if (it + 1 < NIT) { K2_SYNC(it); } else { K2_M0(it + 3); }
                SEG(3);
                sfor<0, 16>([&](auto ic) {
                    constexpr int j = decltype(ic)::value, s4 = j >> 2, db = j & 3;
                    lds_wait<7>(fr[j & 7]);
                    o[db] = MFMA32(fr[j & 7], pf[s4], o[db]);
                    SBAR();
                    if constexpr (j < 8) lds_rd<BOFF(24 + j)>(fr[j & 7], ac);
                    else { constexpr int jn = j - 8; lds_rd<BOFF((jn & 1) * 8 + (jn >> 1))>(fr[j & 7], an); }
                    if constexpr (j == 4) K2_DMA(0);
                    if constexpr (j == 10) K2_DMA(1);
                    SBAR();
                });
                SEG(4);
            }
            auto rr = __builtin_amdgcn_permlane32_swap(__float_as_uint(l), __float_as_uint(l), false, false);
            const float inv = 1.0f / (__uint_as_float(rr[0]) + __uint_as_float(rr[1]));
            char* cp = ws + WS_CTXB + (size_t)(tb32 * 256 + head * 8) * 1024 + lane * 16;
#pragma unroll
            for (int db = 0; db < 4; ++db) {
                st16(cp + (2 * db) * 1024, pack8s(o[db], 0, inv));
                st16(cp + (2 * db + 1) * 1024, pack8s(o[db], 1, inv));
            }
        }
        WAIT_VM(0);
        STAMP(1, 2);
#undef B_MASK
    } else {
        const int tb32 = x * 8 + wa * 2;
        bf16x8 qf[2][2];
        const char* qp = ws + WS_QA + (size_t)((head * 64 + tb32) * 2) * 1024 + lane * 16;
#pragma unroll
        for (int qt = 0; qt < 2; ++qt)
#pragma unroll
            for (int ks = 0; ks < 2; ++ks) qf[qt][ks] = *(const bf16x8*)(qp + (qt * 2 + ks) * 1024);
        f32x16 o[2];
        o[0] = f32x16{}; o[1] = f32x16{};
        float negm[2] = {0.f, 0.f};
        bf16x8 kf[4], vf[4];
        f32x16 s[2][2];
        K2_SYNC(-1);
        lds_rd<AOFF(0)>(kf[0], lbase); lds_rd<AOFF(1)>(kf[1], lbase); lds_rd<AOFF(2)>(kf[2], lbase); lds_rd<AOFF(3)>(kf[3], lbase);
        for (int it = 0; it <= 32; ++it) {
            SEG(0);
            if (it > 0) {
                u32x4 pf[2][2][2];
                unsigned chk = 0;
                sfor<0, 4>([&](auto bc) {
                    constexpr int blk = decltype(bc)::value, kb = blk >> 1, qt = blk & 1;
                    f32x16 e;
#pragma unroll
                    for (int i = 0; i < 8; ++i) e[i] = __builtin_amdgcn_exp2f(s[kb][qt][i]);
                    K2_DMA(blk);
#pragma unroll
                    for (int i = 8; i < 16; ++i) e[i] = __builtin_amdgcn_exp2f(s[kb][qt][i]);
                    if constexpr (blk == 3) K2_DMA(4);
                    pf[kb][qt][0] = pack8(e, 0);
                    pf[kb][qt][1] = pack8(e, 1);
                    chk |= pf[kb][qt][0][0] | pf[kb][qt][0][1] | pf[kb][qt][0][2] | pf[kb][qt][0][3];
                    chk |= pf[kb][qt][1][0] | pf[kb][qt][1][1] | pf[kb][qt][1][2] | pf[kb][qt][1][3];
                });
                const bool first = (it == 1);
                if (first || __any((chk & 0x40004000u) != 0u)) {
#pragma unroll
                    for (int qt = 0; qt < 2; ++qt) {
                        float mx = s[0][qt][0];
#pragma unroll
                        for (int i = 1; i < 16; ++i) mx = fmaxf(mx, s[0][qt][i]);
#pragma unroll
                        for (int i = 0; i < 16; ++i) mx = fmaxf(mx, s[1][qt][i]);
                        auto rr = __builtin_amdgcn_permlane32_swap(__float_as_uint(mx), __float_as_uint(mx), false, false);
                        mx = fmaxf(__uint_as_float(rr[0]), __uint_as_float(rr[1]));
                        float delta = mx + 10.f;
                        if (!first) delta = fmaxf(delta, 0.f);
                        const float tgt = negm[qt] - delta;
                        const unsigned hi = pack2(tgt, 0.f) & 0xFFFFu;
                        const float hif = __uint_as_float(hi << 16);
                        const unsigned lo = pack2(tgt - hif, 0.f) & 0xFFFFu;
                        const float neff = hif + __uint_as_float(lo << 16);
                        delta = negm[qt] - neff;
                        negm[qt] = neff;
                        {
                            u32x4 qq = __builtin_bit_cast(u32x4, qf[qt][1]);
                            qq[2] = h ? 0u : (hi | (lo << 16));
                            qf[qt][1] = __builtin_bit_cast(bf16x8, qq);
                        }
                        if (!first) {
                            const float alpha = __builtin_amdgcn_exp2f(-delta);
#pragma unroll
                            for (int i = 0; i < 16; ++i) o[qt][i] *= alpha;
                        }
#pragma unroll
                        for (int kb = 0; kb < 2; ++kb) {
                            f32x16 e;
#pragma unroll
                            for (int i = 0; i < 16; ++i) e[i] = __builtin_amdgcn_exp2f(s[kb][qt][i] - delta);
                            pf[kb][qt][0] = pack8(e, 0);
                            pf[kb][qt][1] = pack8(e, 1);
                        }
                    }
                }
                SEG(1);
                lds_wait2<4>(vf[0], vf[1]);
                lds_wait2<4>(vf[2], vf[3]);
#pragma unroll
                for (int kb = 0; kb < 2; ++kb)
#pragma unroll
                    for (int s2 = 0; s2 < 2; ++s2)
#pragma unroll
                        for (int qt = 0; qt < 2; ++qt)
                            o[qt] = MFMA32(vf[kb * 2 + s2], __builtin_bit_cast(bf16x8, pf[kb][qt][s2]), o[qt]);
            } else {
                K2_DMA(0); K2_DMA(1); K2_DMA(2); K2_DMA(3); K2_DMA(4);
            }
            SEG(2);
            lds_wait2<0>(kf[0], kf[1]);
            lds_wait2<0>(kf[2], kf[3]);
#pragma unroll
            for (int kb = 0; kb < 2; ++kb)
#pragma unroll
                for (int qt = 0; qt < 2; ++qt) {
                    s[kb][qt] = MFMA32(kf[kb * 2], qf[qt][0], f32x16{});
                    s[kb][qt] = MFMA32(kf[kb * 2 + 1], qf[qt][1], s[kb][qt]);
                }
            SBAR();
            if (it == 32) STAMPW(1, 3, 4);
            SEG(3);
            K2_SYNC(it);
            SEG(4);
            const unsigned ac = lbase + (it & 3) * SLOT;
            const unsigned an = lbase + ((it + 1) & 3) * SLOT;
            lds_rd<AOFF(4)>(vf[0], ac); lds_rd<AOFF(5)>(vf[1], ac); lds_rd<AOFF(6)>(vf[2], ac); lds_rd<AOFF(7)>(vf[3], ac);
            lds_rd<AOFF(0)>(kf[0], an); lds_rd<AOFF(1)>(kf[1], an); lds_rd<AOFF(2)>(kf[2], an); lds_rd<AOFF(3)>(kf[3], an);
        }
        WAIT_VM(0);
#pragma unroll
        for (int qt = 0; qt < 2; ++qt) {
            const unsigned lv = __float_as_uint(o[qt][8]);
            auto rr = __builtin_amdgcn_permlane32_swap(lv, lv, false, false);
            const float lsum = __uint_as_float(rr[1]);
            const float inv = 1.0f / lsum;
            char* cp = ws + WS_CTXA + (size_t)((tb32 + qt) * 40) * 1024;
#pragma unroll
            for (int gq = 0; gq < 3; ++gq) {
                const int d0 = 8 * gq + 4 * h;
                if (d0 < 20) {
                    const int k = head * 20 + d0;
                    u32x2 w;
                    w[0] = pack2(o[qt][4 * gq + 0] * inv, o[qt][4 * gq + 1] * inv);
                    w[1] = pack2(o[qt][4 * gq + 2] * inv, o[qt][4 * gq + 3] * inv);
                    *(u32x2*)(cp + (k >> 4) * 1024 + (r + 32 * ((k >> 3) & 1)) * 16 + (k & 4) * 2) = w;
                }
            }
        }
    }
}

constexpr int G3_STAGE = 49152;
constexpr int G3_LDS = 3 * G3_STAGE;

__global__ __launch_bounds__(512, 2) void k3_oproj(const __hip_bfloat16* __restrict__ Awo, const __hip_bfloat16* __restrict__ Bwo,
                                                   char* __restrict__ ws, __hip_bfloat16* __restrict__ out) {
    extern __shared__ __attribute__((aligned(16))) char smem[];
    LAS char* lds = (LAS char*)smem;
    const int tid = threadIdx.x, lane = tid & 63, wid = __builtin_amdgcn_readfirstlane(tid >> 6);
    const int wm = wid >> 1, wn = wid & 1, h = lane >> 5;
    const int bid = blockIdx.x;
    const bool isB = bid < 200;
    int mt, nt, sp, kt0, nkt, kstot;
    const char* Xc; const char* Wb;
    if (isB) {
        mt = bid & 7; const int rest = bid >> 3; nt = rest % 5; sp = rest / 5;
        kt0 = sp * 13; nkt = sp == 4 ? 12 : 13; kstot = 256;
        Xc = ws + WS_CTXB; Wb = (const char*)Bwo;
    } else {
        const int b2 = bid - 200; mt = b2 & 7; nt = b2 >> 3; sp = 5;
        kt0 = 0; nkt = 10; kstot = 40;
        Xc = ws + WS_CTXA; Wb = (const char*)Awo;
    }
    unsigned woff[2];
#pragma unroll
    for (int i = 0; i < 2; ++i) {
        const int k = (wid * 2 + i) * 4 + (lane >> 4);
        const int p = lane & 15, b = (p >> 2) ^ (k & 3);
        woff[i] = (unsigned)(((kt0 * 64 + k) * 640 + nt * 128 + (b * 4 + (p & 3)) * 8) * 2);
    }
    const unsigned lds0 = (unsigned)(uintptr_t)lds;
    const char* xsb = Xc + (size_t)((mt * 8 + wid) * kstot + kt0 * 4) * 1024 + 4096;
    const unsigned voff = lane * 16;
    woff[1] -= 1024;
    auto piece = [&](int kt, auto ic) {
        constexpr int i = decltype(ic)::value;
        if constexpr (i < 4) dma_s<i * 1024 - 4096>(voff, xsb + (size_t)kt * 4096);
        else dma_s<(i - 4) * 1024>(woff[i - 4], Wb + (size_t)kt * (64u * 640u * 2u));
    };
    const int q = (lane >> 2) & 3, p4 = lane & 3, g16 = (lane >> 4) & 1;
    const int r0 = isB ? 4 * h : 8 * h, r1 = isB ? 8 + 4 * h : 8 * h + 4;
    int wb0[2], wb1[2];
#pragma unroll
    for (int ni = 0; ni < 2; ++ni) {
        const int b = wn * 2 + ni;
        const int c = ((b ^ q) * 64) + g16 * 32 + p4 * 8;
        wb0[ni] = (r0 >> 3) * 6144 + 4096 + ((r0 & 7) + q) * 256 + c;
        wb1[ni] = (r1 >> 3) * 6144 + 4096 + ((r1 & 7) + q) * 256 + c;
    }
    const int xb0 = wm * 2 * 6144 + lane * 16;
    f32x16 acc[2][2];
#pragma unroll
    for (int mi = 0; mi < 2; ++mi)
#pragma unroll
        for (int ni = 0; ni < 2; ++ni) acc[mi][ni] = f32x16{};

    bf16x8 xf[2][4][2], wf[2][4][2];
#define G3_RD(set, bb)                                                                                           \
    do {                                                                                                         \
        _Pragma("unroll") for (int ks = 0; ks < 4; ++ks) {                                                       \
            _Pragma("unroll") for (int ni = 0; ni < 2; ++ni)                                                     \
                wf[set][ks][ni] = tr_frag((bb) + wb0[ni] + ks * 12288, (bb) + wb1[ni] + ks * 12288);             \
            _Pragma("unroll") for (int mi = 0; mi < 2; ++mi)                                                     \
                xf[set][ks][mi] = *(LAS bf16x8*)((bb) + xb0 + mi * 6144 + ks * 1024);                            \
        }                                                                                                        \
    } while (0)
#define G3_STEP(set, kt)                                                                                         \
    do {                                                                                                         \
        if ((kt) + 2 < nkt) { WAIT_VM(6); } else { WAIT_VM(0); }                                                 \
        LGKM0();                                                                                                 \
        BARRIER();                                                                                               \
        const int b0_ = (kt) % 3, b1_ = ((kt) + 1) % 3;                                                          \
        set_m0(lds0 + b0_ * G3_STAGE + wid * 6144 + 4096);                                                       \
        if ((kt) + 1 < nkt) G3_RD((set) ^ 1, lds + b1_ * G3_STAGE);                                              \
        SBAR();                                                                                                  \
        const bool on_ = (kt) + 3 < nkt;                                                                         \
        sfor<0, 8>([&](auto jc) {                                                                                \
            constexpr int j = decltype(jc)::value, ks = j >> 1, mi = j & 1;                                      \
            _Pragma("unroll") for (int ni = 0; ni < 2; ++ni)                                                     \
                acc[mi][ni] = MFMA32(wf[set][ks][ni], xf[set][ks][mi], acc[mi][ni]);                             \
            if constexpr (j < 6) { SBAR(); if (on_) piece((kt) + 3, jc); SBAR(); }                               \
        });                                                                                                      \
        SBAR();                                                                                                  \
    } while (0)
    STAMP(2, 0);
#pragma unroll
    for (int t = 0; t < 3; ++t) {
        set_m0(lds0 + t * G3_STAGE + wid * 6144 + 4096);
        sfor<0, 6>([&](auto ic) { piece(t, ic); });
    }
    WAIT_VM(12);
    BARRIER();
    STAMP(2, 1);
    G3_RD(0, lds);
    int kt = 0;
#pragma unroll 1
    for (; kt + 1 < nkt; kt += 2) { G3_STEP(0, kt); G3_STEP(1, kt + 1); }
    if (kt < nkt) G3_STEP(0, kt);
#undef G3_RD
#undef G3_STEP
    STAMP(2, 2);
    const int r = lane & 31;
    if (!isB) {
#pragma unroll
        for (int mi = 0; mi < 2; ++mi)
#pragma unroll
            for (int ni = 0; ni < 2; ++ni) {
                char* op = (char*)out + (size_t)((mt * 8 + wm * 2 + mi) * 32 + r) * 2560 + ((nt * 4 + wn * 2 + ni) * 32 + 4 * h) * 2;
#pragma unroll
                for (int gq = 0; gq < 4; ++gq) {
                    u32x2 o;
                    o[0] = pack2(acc[mi][ni][4 * gq], acc[mi][ni][4 * gq + 1]);
                    o[1] = pack2(acc[mi][ni][4 * gq + 2], acc[mi][ni][4 * gq + 3]);
                    *(u32x2*)(op + gq * 16) = o;
                }
            }
        STAMP(2, 3);
        return;
    }
#pragma unroll
    for (int mi = 0; mi < 2; ++mi)
#pragma unroll
        for (int ni = 0; ni < 2; ++ni) {
            const int mt32 = mt * 8 + wm * 2 + mi, nt32 = nt * 4 + wn * 2 + ni;
            char* dst = ws + WS_PART + (size_t)sp * SLAB + (size_t)(mt32 * 20 + nt32) * 4096 + lane * 16;
#pragma unroll
            for (int gq = 0; gq < 4; ++gq) {
                f32x4 v = {acc[mi][ni][4 * gq], acc[mi][ni][4 * gq + 1], acc[mi][ni][4 * gq + 2], acc[mi][ni][4 * gq + 3]};
                asm volatile("global_store_dwordx4 %0, %1, off sc1\n\ts_nop 1" :: "v"(dst + gq * 1024), "v"(v) : "memory");
            }
        }
    asm volatile("s_waitcnt vmcnt(0)" ::: "memory");
    BARRIER();
    unsigned* cnt = (unsigned*)(ws + WS_CNT) + (mt * 5 + nt) * 64;
    if (wid == 0) {
        if (lane == 0) __hip_atomic_fetch_add(cnt, 1u, __ATOMIC_RELAXED, __HIP_MEMORY_SCOPE_AGENT);
        for (unsigned spins = 0; spins < (1u << 24); ++spins) {
            const unsigned c = __builtin_amdgcn_readfirstlane(__hip_atomic_load(cnt, __ATOMIC_RELAXED, __HIP_MEMORY_SCOPE_AGENT));
            if (c >= (unsigned)NSPLIT) break;
            __builtin_amdgcn_s_sleep(1);
        }
        __builtin_amdgcn_fence(__ATOMIC_ACQUIRE, "agent");
        asm volatile("s_waitcnt vmcnt(0)" ::: "memory");
    }
    BARRIER();
    const int st = sp + 5 * wid;
    if (st < 32) {
        const int mt32 = mt * 8 + (st >> 2), nt32 = nt * 4 + (st & 3);
        const char* src = ws + WS_PART + (size_t)(mt32 * 20 + nt32) * 4096 + lane * 16;
        f32x4 v[4][NSPLIT];
#pragma unroll
        for (int gq = 0; gq < 4; ++gq)
#pragma unroll
            for (int s = 0; s < NSPLIT; ++s) v[gq][s] = *(const f32x4*)(src + (size_t)s * SLAB + gq * 1024);
        char* op = (char*)out + (size_t)(mt32 * 32 + r) * 2560 + (640 + nt32 * 32 + 4 * h) * 2;
#pragma unroll
        for (int gq = 0; gq < 4; ++gq) {
            const f32x4 t = ((v[gq][0] + v[gq][1]) + (v[gq][2] + v[gq][3])) + v[gq][4];
            u32x2 o;
            o[0] = pack2(t[0], t[1]);
            o[1] = pack2(t[2], t[3]);
            *(u32x2*)(op + gq * 16) = o;
        }
    }
    STAMP(2, 3);
}

extern "C" void kernel_launch(void* const* d_in, const int* in_sizes, int n_in,
                              void* d_out, int out_size, void* d_ws, size_t ws_size,
                              hipStream_t stream) {
    (void)in_sizes; (void)n_in; (void)out_size; (void)ws_size;
    const __hip_bfloat16* Ax = (const __hip_bfloat16*)d_in[0];
    const __hip_bfloat16* Awqkv = (const __hip_bfloat16*)d_in[1];
    const __hip_bfloat16* Awo = (const __hip_bfloat16*)d_in[2];
    const __hip_bfloat16* Bx = (const __hip_bfloat16*)d_in[3];
    const __hip_bfloat16* Bckv = (const __hip_bfloat16*)d_in[4];
    const __hip_bfloat16* Bwq = (const __hip_bfloat16*)d_in[5];
    const __hip_bfloat16* Bwuk = (const __hip_bfloat16*)d_in[6];
    const __hip_bfloat16* Bwuv = (const __hip_bfloat16*)d_in[7];
    const __hip_bfloat16* Bwo = (const __hip_bfloat16*)d_in[8];
    char* ws = (char*)d_ws;
    (void)hipFuncSetAttribute((const void*)k1_proj, hipFuncAttributeMaxDynamicSharedMemorySize, G1_LDS);
    (void)hipFuncSetAttribute((const void*)k2_attn, hipFuncAttributeMaxDynamicSharedMemorySize, K2_LDS);
    (void)hipFuncSetAttribute((const void*)k3_oproj, hipFuncAttributeMaxDynamicSharedMemorySize, G3_LDS);
    k1_proj<<<256, 512, G1_LDS, stream>>>(Ax, Awqkv, Bx, Bckv, Bwq, Bwuk, Bwuv, Awo, Bwo, ws);
    k2_attn<<<256, 512, K2_LDS, stream>>>(ws);
    k3_oproj<<<240, 512, G3_LDS, stream>>>(Awo, Bwo, ws, (__hip_bfloat16*)d_out);
}
```

```cpp
#include <hip/hip_runtime.h>
#include <hip/hip_bf16.h>
#include <stdint.h>
#include <type_traits>

typedef short s16x4 __attribute__((ext_vector_type(4)));
typedef short bf16x8 __attribute__((ext_vector_type(8)));
typedef float f32x16 __attribute__((ext_vector_type(16)));
typedef float f32x4 __attribute__((ext_vector_type(4)));
typedef float f32x2 __attribute__((ext_vector_type(2)));
typedef unsigned u32x4 __attribute__((ext_vector_type(4)));
typedef unsigned u32x2 __attribute__((ext_vector_type(2)));
typedef __bf16 bf16x2_t __attribute__((ext_vector_type(2)));
#define LAS __attribute__((address_space(3)))
#define DI __device__ __forceinline__
#define MFMA32(a, b, c) __builtin_amdgcn_mfma_f32_32x32x16_bf16((a), (b), (c), 0, 0, 0)
#define MFMA16(a, b, c) __builtin_amdgcn_mfma_f32_16x16x32_bf16((a), (b), (c), 0, 0, 0)
#define CFENCE() asm volatile("" ::: "memory")
#define BARRIER() do { CFENCE(); __builtin_amdgcn_s_barrier(); CFENCE(); } while (0)
#define WAIT_VM(n) asm volatile("s_waitcnt vmcnt(" #n ") lgkmcnt(0)" ::: "memory")
#define SBAR() __builtin_amdgcn_sched_barrier(0)

DI unsigned pack2(float a, float b) {
    f32x2 v = {a, b};
    bf16x2_t r = __builtin_convertvector(v, bf16x2_t);
    return __builtin_bit_cast(unsigned, r);
}
DI u32x4 pack8(const f32x16& x, int s) {
    u32x4 w;
    w[0] = pack2(x[8 * s + 0], x[8 * s + 1]);
    w[1] = pack2(x[8 * s + 2], x[8 * s + 3]);
    w[2] = pack2(x[8 * s + 4], x[8 * s + 5]);
    w[3] = pack2(x[8 * s + 6], x[8 * s + 7]);
    return w;
}
DI u32x4 pack8s(const f32x16& x, int s, float c) {
    u32x4 w;
    w[0] = pack2(x[8 * s + 0] * c, x[8 * s + 1] * c);
    w[1] = pack2(x[8 * s + 2] * c, x[8 * s + 3] * c);
    w[2] = pack2(x[8 * s + 4] * c, x[8 * s + 5] * c);
    w[3] = pack2(x[8 * s + 6] * c, x[8 * s + 7] * c);
    return w;
}
DI void glds16(const void* g, LAS char* l) {
    __builtin_amdgcn_global_load_lds((const unsigned*)g, (LAS unsigned*)l, 16, 0, 0);
}
DI bf16x8 tr_frag(LAS char* p0, LAS char* p1) {
    s16x4 lo = __builtin_amdgcn_ds_read_tr16_b64_v4i16((LAS s16x4*)p0);
    s16x4 hi = __builtin_amdgcn_ds_read_tr16_b64_v4i16((LAS s16x4*)p1);
    return __builtin_shufflevector(lo, hi, 0, 1, 2, 3, 4, 5, 6, 7);
}

#define STMODE 2
DI void st16(void* p, u32x4 v) {
    asm volatile("global_store_dwordx4 %0, %1, off sc1\n\ts_nop 1" :: "v"(p), "v"(v) : "memory");
}
template <int I, int N, class F> DI void sfor(F&& f) {
    if constexpr (I < N) { f(std::integral_constant<int, I>{}); sfor<I + 1, N>(f); }
}
template <int OFF> DI void lds_rd(bf16x8& d, unsigned addr) {
    asm volatile("ds_read_b128 %0, %1 offset:%2" : "=v"(d) : "v"(addr), "i"(OFF));
}
template <int N> DI void lds_wait(bf16x8& d) { asm volatile("s_waitcnt lgkmcnt(%1)" : "+v"(d) : "i"(N)); }
template <int N> DI void lds_wait2(bf16x8& d, bf16x8& e) { asm volatile("s_waitcnt lgkmcnt(%2)" : "+v"(d), "+v"(e) : "i"(N)); }
#define LGKM0() __builtin_amdgcn_s_waitcnt(0xC07F)
DI void set_m0(unsigned v) { asm volatile("s_mov_b32 m0, %0\n\ts_nop 0" :: "s"(v) : "memory"); }
template <int OFF> DI void dma_s(unsigned voff, const char* sbase) {
    asm volatile("global_load_lds_dwordx4 %0, %1 offset:%2" :: "v"(voff), "s"(sbase), "i"(OFF) : "memory");
}

#define ABL_NOPIECE 0
#define ABL_NOBAR 0
#define ABL_T0 0
#define ABL_K2 0
#define ABL_NODMA 0
#define ABL_NOMFMA 0
#define SEG(i) do { } while (0)
#define STAMP(kid, idx) do { } while (0)
#define STAMPW(kid, idx, w) do { } while (0)
constexpr size_t WS_KVB = 0;
constexpr size_t WS_QB = WS_KVB + (size_t)32 * 1048576;
constexpr size_t WS_KVA = WS_QB + (size_t)16 * 1048576;
constexpr size_t WS_QA = WS_KVA + (size_t)8 * 1048576;
constexpr size_t WS_CTXB = WS_QA + (size_t)4 * 1048576;
constexpr size_t WS_CTXA = WS_CTXB + (size_t)16 * 1048576;
constexpr size_t WS_PART = WS_CTXA + (size_t)4 * 1048576;
constexpr size_t WS_CNT = WS_PART + (size_t)32 * 1048576;
constexpr int NSPLIT = 5;
constexpr size_t SLAB = (size_t)64 * 20 * 4096;

constexpr float CB = 0.08838834764831845f * 1.4426950408889634f;
constexpr float CA = 0.22360679774997896f * 1.4426950408889634f;

#define PREFETCH 1
constexpr int G1_LDS = 2 * 65536 + 2048;
enum { EP_BK = 0, EP_BV = 1, EP_BQ = 2, EP_AQK = 3, EP_AV = 4 };

struct G1Src { unsigned xoff[4], woff[4]; const char* Xb; const char* Wb; unsigned wstep; int nkt; };
DI void g1_src(G1Src& s, const __hip_bfloat16* X, const __hip_bfloat16* W, int K, int ldw, int m0, int n0, int nlim, int wid, int lane) {
#pragma unroll
    for (int i = 0; i < 4; ++i) {
        const int row = (wid * 4 + i) * 8 + (lane >> 3);
        const int c = (lane & 7) ^ ((row >> 1) & 7);
        s.xoff[i] = (unsigned)(((m0 + row) * K + c * 8) * 2) + 4096 - i * 1024;
        const int k = (wid * 4 + i) * 2 + (lane >> 5);
        const int p = lane & 31, b = (p >> 2) ^ (k & 3);
        int col = n0 + (b * 4 + ((p & 3) ^ ((wid & 1) << 1))) * 8;
        col = col < nlim ? col : nlim - 8;
        s.woff[i] = (unsigned)((k * ldw + col) * 2) - i * 1024;
    }
    s.Xb = (const char*)X; s.Wb = (const char*)W; s.wstep = (unsigned)(64 * ldw * 2); s.nkt = K >> 6;
}
template <int I> DI void g1_piece(const G1Src& s, int kt) {
    if constexpr (I < 4) dma_s<I * 1024 - 4096>(s.xoff[I], s.Xb + (size_t)kt * 128);
    else dma_s<(I - 4) * 1024>(s.woff[I - 4], s.Wb + (size_t)kt * s.wstep);
}

template <bool TR>
DI void g1_loop(f32x4 (&acc)[8][4], bf16x8 (&xs)[2][4], bf16x8 (&wsb)[2][4], const G1Src& cur, const G1Src& nxt, bool has_next, bool first,
                LAS char* lds, int wid, int lane) {
    const int wm = wid >> 2, wn = wid & 3, r16 = lane & 15, g = lane >> 4;
    const unsigned lds0 = (unsigned)(uintptr_t)lds;
    const int nkt = cur.nkt;
    int xb[2];
#pragma unroll
    for (int st = 0; st < 2; ++st) xb[st] = wm * 4 * 8192 + r16 * 128 + (((4 * st + g) ^ (r16 >> 1)) * 16);
    const int q = (lane >> 2) & 3, p4 = lane & 3;
    int wb[4];
#pragma unroll
    for (int nt = 0; nt < 4; ++nt) {
        const int b = wn * 2 + (nt >> 1);
        wb[nt] = g * 8192 + 4096 + q * 512 + ((b ^ q) * 64) + (((nt & 1) ^ (g & 1)) * 32) + p4 * 8;
    }
#pragma unroll
    for (int mt = 0; mt < 8; ++mt)
#pragma unroll
        for (int nt = 0; nt < 4; ++nt) acc[mt][nt] = f32x4{};
#define G1_RX(set, bb, st, mh)                                                                                   \
    _Pragma("unroll") for (int m4 = 0; m4 < 4; ++m4)                                                             \
        xs[set][m4] = *(LAS bf16x8*)((bb) + xb[st] + ((mh) * 2 + (m4 >> 1)) * 8192 + (m4 & 1) * 2048)
#define G1_RW(bb, st)                                                                                            \
    _Pragma("unroll") for (int nt = 0; nt < 4; ++nt)                                                             \
        wsb[st][nt] = tr_frag((bb) + wb[nt] + (st) * 32768, (bb) + wb[nt] + (st) * 32768 + 2048)
#define G1_MM(set, st, mh, on, kn, p0, pe)                                                                       \
    sfor<0, 4>([&](auto mc) {                                                                                    \
        constexpr int m4 = decltype(mc)::value;                                                                  \
        _Pragma("unroll") for (int nt = 0; nt < 4; ++nt)                                                         \
            acc[(mh) * 4 + m4][nt] = TR ? MFMA16(wsb[st][nt], xs[set][m4], acc[(mh) * 4 + m4][nt])               \
                                        : MFMA16(xs[set][m4], wsb[st][nt], acc[(mh) * 4 + m4][nt]);              \
        if constexpr ((p0) + m4 < (pe)) {                                                                        \
            SBAR();                                                                                              \
            if (on) { if ((kn) < nkt) g1_piece<(p0) + m4>(cur, (kn)); else g1_piece<(p0) + m4>(nxt, (kn) - nkt); } \
            SBAR();                                                                                              \
        }                                                                                                        \
    })
    if (first) { G1_RW(lds, 0); G1_RX(0, lds, 0, 0); }
#pragma unroll 1
    for (int kt = 0; kt < nkt; ++kt) {
        LAS char* bb = lds + (kt & 1) * 65536;
        LAS char* bn = lds + ((kt + 1) & 1) * 65536;
        const bool on1 = !(first && kt == 0) && (kt + 1 < nkt || has_next);
        const bool on2 = kt + 2 < nkt || has_next;
        LGKM0(); G1_RX(1, bb, 0, 1); SBAR(); G1_MM(0, 0, 0, on1, kt + 1, 4, 8); SBAR();
        LGKM0(); G1_RW(bb, 1); G1_RX(0, bb, 1, 0); SBAR(); G1_MM(1, 0, 1, false, 0, 0, 0); SBAR();
        LGKM0(); G1_RX(1, bb, 1, 1); SBAR(); G1_MM(0, 1, 0, false, 0, 0, 0); SBAR();
        LGKM0(); WAIT_VM(0);
        BARRIER();
        set_m0(lds0 + (kt & 1) * 65536 + wid * 8192 + 4096);
        if (kt + 1 < nkt || has_next) { G1_RW(bn, 0); G1_RX(0, bn, 0, 0); }
        SBAR(); G1_MM(1, 1, 1, on2, kt + 2, 0, 4); SBAR();
    }
#undef G1_RX
#undef G1_RW
#undef G1_MM
}

DI void st8(void* p, u32x2 v) {
    asm volatile("global_store_dwordx2 %0, %1, off sc1\n\ts_nop 1" :: "v"(p), "v"(v) : "memory");
}

template <int EP>
DI void g1_epi(const f32x4 (&acc)[8][4], int m0, int n0, int nlim, int nsec, char* __restrict__ ws, int wid, int lane) {
    const int wm = wid >> 2, wn = wid & 3, c = lane & 15, g = lane >> 4;
    if constexpr (EP == EP_BK || EP == EP_BQ || EP == EP_BV) {
#pragma unroll
        for (int mt = 0; mt < 8; ++mt) {
            const int tb = (m0 + wm * 128 + mt * 16) >> 5;
            const int tile = tb >> 1, kb = tb & 1, th = mt & 1;
#pragma unroll
            for (int np = 0; np < 2; ++np) {
                const int nb = n0 + wn * 64 + np * 32;
                const int head = nb >> 7;
                const float sc = EP == EP_BQ ? CB : 1.0f;
                const f32x4 a = acc[mt][2 * np], b = acc[mt][2 * np + 1];
                unsigned a0 = pack2(a[0] * sc, a[1] * sc), a1 = pack2(a[2] * sc, a[3] * sc);
                unsigned b0 = pack2(b[0] * sc, b[1] * sc), b1 = pack2(b[2] * sc, b[3] * sc);
                if constexpr (EP == EP_BV) {
                    auto r0 = __builtin_amdgcn_permlane32_swap(a0, b0, false, false);
                    auto r1 = __builtin_amdgcn_permlane32_swap(a1, b1, false, false);
                    const u32x4 w = {r0[0], r1[0], r0[1], r1[1]};
                    const int db = (nb >> 5) & 3;
                    char* base = ws + WS_KVB + (size_t)head * 1048576 + tile * 32768 + 16384 + ((kb * 2 + th) * 4 + db) * 1024;
                    st16(base + ((g >> 1) * 16 + c + 32 * (g & 1)) * 16, w);
                } else {
                    auto r0 = __builtin_amdgcn_permlane16_swap(a0, b0, false, false);
                    auto r1 = __builtin_amdgcn_permlane16_swap(a1, b1, false, false);
                    const u32x4 w = {r0[0], r1[0], r0[1], r1[1]};
                    const int ks = ((nb >> 4) & 7) + (g & 1);
                    char* base = EP == EP_BK ? ws + WS_KVB + (size_t)head * 1048576 + tile * 32768 + (kb * 8 + ks) * 1024
                                             : ws + WS_QB + (size_t)((head * 64 + tb) * 8 + ks) * 1024;
                    st16(base + (th * 16 + c + 32 * (g >> 1)) * 16, w);
                }
            }
        }
        return;
    }
#pragma unroll
    for (int mt = 0; mt < 8; ++mt) {
        const int tb = (m0 + wm * 128 + mt * 16) >> 5;
        const int tile = tb >> 1, kb = tb & 1, th = mt & 1;
#pragma unroll
        for (int nt = 0; nt < 4; ++nt) {
            const int nb = n0 + wn * 64 + nt * 16;
            if (nb >= nlim) continue;
            const f32x4 a = acc[mt][nt];
            if constexpr (EP == EP_BK || EP == EP_BQ || EP == EP_BV) {
            } else if constexpr (EP == EP_AQK) {
                const bool isq = nb < 640;
                const float sc = isq ? CA : 1.0f;
                const int r = th * 16 + c;
                const int n = nb - (isq ? 0 : 640) + 4 * g;
                const int head = n / 20, d0 = n - head * 20;
                char* base = isq ? ws + WS_QA + (size_t)((head * 64 + tb) * 2) * 1024
                                 : ws + WS_KVA + (size_t)head * 262144 + tile * 8192 + kb * 2048;
                char* dst = base + (d0 >> 4) * 1024 + (r + 32 * ((d0 >> 3) & 1)) * 16 + (d0 & 4) * 2;
                u32x2 w;
                w[0] = pack2(a[0] * sc, a[1] * sc);
                w[1] = pack2(a[2] * sc, a[3] * sc);
                *(u32x2*)dst = w;
                if (d0 == 16) {
                    *(u32x2*)(dst + 8) = (u32x2){isq ? 0u : 0x3F803F80u, 0u};
                    *(u32x4*)(base + 1024 + (r + 32) * 16) = (u32x4){0u, 0u, 0u, 0u};
                }
            } else {
                const int n = nb - nsec + c;
                const int head = n / 20, d = n - head * 20;
                char* dst = ws + WS_KVA + (size_t)head * 262144 + tile * 8192 + 4096 + (kb * 2 + th) * 1024 + (d + 32 * (g & 1)) * 16 + (g >> 1) * 8;
                u32x2 w;
                w[0] = pack2(a[0], a[1]);
                w[1] = pack2(a[2], a[3]);
                *(u32x2*)dst = w;
                if (d == 19) *(u32x2*)(dst + 16) = (u32x2){0x3F803F80u, 0x3F803F80u};
            }
        }
    }
}

__global__ __launch_bounds__(512, 2) void k1_proj(const __hip_bfloat16* __restrict__ Ax, const __hip_bfloat16* __restrict__ Awqkv,
                                                  const __hip_bfloat16* __restrict__ Bx, const __hip_bfloat16* __restrict__ Bckv,
                                                  const __hip_bfloat16* __restrict__ Bwq, const __hip_bfloat16* __restrict__ Bwuk,
                                                  const __hip_bfloat16* __restrict__ Bwuv, const __hip_bfloat16* __restrict__ Awo,
                                                  const __hip_bfloat16* __restrict__ Bwo, char* __restrict__ ws) {
    extern __shared__ __attribute__((aligned(16))) char smem[];
    LAS char* lds = (LAS char*)smem;
    const int tid = threadIdx.x, lane = tid & 63, wid = __builtin_amdgcn_readfirstlane(tid >> 6);
    const int bid = blockIdx.x;
    const int m0 = ((bid >> 3) & 7) * 256;
    const int c1 = (bid & 7) + 8 * (bid >> 6);
    const int c2 = c1;
    const bool has2 = c2 < 24;
    G1Src s1, s2;
    if (c1 < 16) g1_src(s1, Bckv, Bwuk, 512, 4096, m0, c1 * 256, 4096, wid, lane);
    else g1_src(s1, Bckv, Bwuv, 512, 4096, m0, (c1 - 16) * 256, 4096, wid, lane);
    const int n2 = c2 < 16 ? c2 * 256 : (c2 - 16) * 256;
    if (c2 < 16) g1_src(s2, Bx, Bwq, 640, 4096, m0, n2, 4096, wid, lane);
    else g1_src(s2, Ax, Awqkv, 640, 1920, m0, has2 ? n2 : 0, c2 < 21 ? 1280 : 1920, wid, lane);

    f32x4 acc[8][4];
    bf16x8 xf[2][4], wf[2][4];
    const unsigned lds0 = (unsigned)(uintptr_t)lds;
    if (bid == 255 && tid < 40) ((unsigned*)(ws + WS_CNT))[tid * 64] = 0u;
    STAMP(0, 0);
    set_m0(lds0 + wid * 8192 + 4096);
    sfor<0, 8>([&](auto ic) { g1_piece<decltype(ic)::value>(s1, 0); });
    set_m0(lds0 + 65536 + wid * 8192 + 4096);
    sfor<0, 8>([&](auto ic) { g1_piece<decltype(ic)::value>(s1, 1); });
    WAIT_VM(8);
    BARRIER();
    STAMP(0, 1);
    {
        set_m0(lds0 + 131072 + wid * 256);
        const unsigned gw = (unsigned)(bid * 8 + wid) * 64 + lane;
#define PF(ptr, nbytes) do { constexpr unsigned R_ = 131072u / ((nbytes) / 128u);                                     \
            const unsigned ln_ = gw / R_;                                                                                \
            const unsigned off_ = (gw % R_ == 0u && ln_ * 128u < (nbytes)) ? ln_ * 128u : 0u;                             \
            asm volatile("global_load_lds_dword %0, %1" :: "v"(off_), "s"((const char*)(ptr)) : "memory"); } while (0)
        PF(Bx, 2048u * 640u * 2u); PF(Bwq, 640u * 4096u * 2u); PF(Ax, 2048u * 640u * 2u); PF(Awqkv, 640u * 1920u * 2u);
        PF(Bwo, 4096u * 640u * 2u); PF(Awo, 640u * 640u * 2u);
        PF(Bckv, 2048u * 512u * 2u); PF(Bwuk, 512u * 4096u * 2u); PF(Bwuv, 512u * 4096u * 2u);
#undef PF
    }
    if (c1 < 16) { g1_loop<true>(acc, xf, wf, s1, s2, has2, true, lds, wid, lane); g1_epi<EP_BK>(acc, m0, c1 * 256, 4096, 0, ws, wid, lane); }
    else { g1_loop<false>(acc, xf, wf, s1, s2, has2, true, lds, wid, lane); g1_epi<EP_BV>(acc, m0, (c1 - 16) * 256, 4096, 0, ws, wid, lane); }
    STAMP(0, 2);
    if (c2 < 21) {
        g1_loop<true>(acc, xf, wf, s2, s2, false, false, lds, wid, lane);
        if (c2 < 16) g1_epi<EP_BQ>(acc, m0, n2, 4096, 0, ws, wid, lane);
        else g1_epi<EP_AQK>(acc, m0, n2, 1280, 0, ws, wid, lane);
    } else if (has2) {
        g1_loop<false>(acc, xf, wf, s2, s2, false, false, lds, wid, lane);
        g1_epi<EP_AV>(acc, m0, n2, 1920, 1280, ws, wid, lane);
    }
    STAMP(0, 3);
}

constexpr int SLOT = 40960;
constexpr int NSLOT = 4;
constexpr int K2_LDS = NSLOT * SLOT;
constexpr int NIT = 34;

__global__ __launch_bounds__(512, 2) void k2_attn(char* __restrict__ ws) {
    extern __shared__ __attribute__((aligned(16))) char smem[];
    LAS char* lds = (LAS char*)smem;
    const int tid = threadIdx.x, lane = tid & 63, wid = __builtin_amdgcn_readfirstlane(tid >> 6);
    const int r = lane & 31, h = lane >> 5;
    const int g = blockIdx.x, xcd = g & 7, idx = g >> 3;
    const int head = xcd * 4 + (idx >> 3), x = idx & 7;
    const char* kvb = ws + WS_KVB + (size_t)head * 1048576;
    const char* kva = ws + WS_KVA + (size_t)head * 262144;
    const int n1 = 32 - 2 * x;
    const unsigned lbase = (unsigned)(uintptr_t)lds + lane * 16;

    STAMP(1, 0);
    const unsigned lds0 = (unsigned)(uintptr_t)lds;
    const unsigned voff = lane * 16;
    const int wa = wid & 3;
    const char* sbB = kvb;
    const char* sbA = kva;
#define K2_M0(tt)                                                                           \
    do {                                                                                    \
        const int t3_ = (tt) < NIT ? (tt) : NIT - 1;                                        \
        const int tb3_ = t3_ < n1 ? t3_ : t3_ - n1;                                         \
        const int ta3_ = t3_ < 32 ? t3_ : 31;                                               \
        sbB = kvb + (size_t)tb3_ * 32768 + wid * 4096 + 2048;                               \
        sbA = kva + (size_t)ta3_ * 8192 + wid * 1024 - 2048;                                \
        set_m0(lds0 + ((tt) < NIT ? ((tt) & 3) : 2 + ((tt) & 1)) * SLOT + wid * 5120 + 2048); \
    } while (0)
#define K2_DMA(i) do { SBAR(); if constexpr ((i) < 4) dma_s<(i) * 1024 - 2048>(voff, sbB); else dma_s<2048>(voff, sbA); SBAR(); } while (0)
#define K2_STAGE_ALL(tt) do { K2_M0(tt); K2_DMA(0); K2_DMA(1); K2_DMA(2); K2_DMA(3); K2_DMA(4); } while (0)
#define BOFF(f) (((f) >> 2) * 5120 + ((f) & 3) * 1024)
#define AOFF(a) ((a) * 5120 + 4096)
    K2_STAGE_ALL(0);
    K2_STAGE_ALL(1);

#define K2_SYNC(it) do { WAIT_VM(5); BARRIER(); K2_M0((it) + 3); } while (0)

    if (wid < 4) {
        bf16x8 fr[8];
        K2_SYNC(-1);
        STAMP(1, 1);
        sfor<0, 8>([&](auto ic) { constexpr int j = decltype(ic)::value; lds_rd<BOFF((j & 1) * 8 + (j >> 1))>(fr[j], lbase); });
        K2_DMA(0); K2_DMA(1);
        int it = 0;
        for (int blk = 0; blk < 2; ++blk) {
            const int qb = blk == 0 ? 15 - x : x;
            const int nt = 2 * qb + 2;
            const int q0 = qb * 128 + wid * 32;
            const int tb32 = q0 >> 5;
            bf16x8 qf[8];
            const char* qp = ws + WS_QB + (size_t)((head * 64 + tb32) * 8) * 1024 + lane * 16;
#pragma unroll
            for (int ks = 0; ks < 8; ++ks) qf[ks] = *(const bf16x8*)(qp + ks * 1024);
            f32x16 o[4];
#pragma unroll
            for (int db = 0; db < 4; ++db) o[db] = f32x16{};
            f32x16 negm = f32x16{};
            float l = 0.f;
            for (int t = 0; t < nt; ++t, ++it) {
                const int rel = (q0 - 64 * t) >> 5;
                const unsigned ac = lbase + (it & 3) * SLOT;
                const unsigned an = lbase + ((it + 1) & 3) * SLOT;
                f32x16 s0, s1;
                SEG(0);
                sfor<0, 16>([&](auto ic) {
                    constexpr int j = decltype(ic)::value, ks = j >> 1;
                    lds_wait<7>(fr[j & 7]);
                    if constexpr (j == 0) s0 = MFMA32(fr[0], qf[0], negm);
                    else if constexpr (j == 1) s1 = MFMA32(fr[1], qf[0], negm);
                    else if constexpr ((j & 1) == 0) s0 = MFMA32(fr[j & 7], qf[ks], s0);
                    else s1 = MFMA32(fr[j & 7], qf[ks], s1);
                    constexpr int jn = j + 8;
                    SBAR();
                    if constexpr (jn < 16) lds_rd<BOFF((jn & 1) * 8 + (jn >> 1))>(fr[j & 7], ac);
                    else lds_rd<BOFF(jn)>(fr[j & 7], ac);
                    if constexpr (j == 2) K2_DMA(2);
                    if constexpr (j == 8) K2_DMA(3);
                    if constexpr (j == 14) K2_DMA(4);
                    SBAR();
                });
#define B_MASK()                                                                                        \
    do {                                                                                                \
        if (rel <= 1) {                                                                                 \
            const int lim = 32 * rel + r - 4 * h;                                                       \
            _Pragma("unroll") for (int i = 0; i < 16; ++i) {                                            \
                const int kr = (i & 3) + 8 * (i >> 2);                                                  \
                if (kr > lim) s0[i] = -__builtin_inff();                                                \
                if (kr + 32 > lim) s1[i] = -__builtin_inff();                                           \
            }                                                                                           \
        }                                                                                               \
    } while (0)
                SEG(1);
                B_MASK();
                float ps = 0.f;
                if (t > 0) {
#pragma unroll
                    for (int i = 0; i < 16; ++i) { s0[i] = __builtin_amdgcn_exp2f(s0[i]); ps += s0[i]; }
#pragma unroll
                    for (int i = 0; i < 16; ++i) { s1[i] = __builtin_amdgcn_exp2f(s1[i]); ps += s1[i]; }
                }
                if (t == 0 || __any(!(ps <= 65536.f))) {
                    if (t > 0) {
                        LAS char* sl = lds + (it & 3) * SLOT + lane * 16;
                        s0 = negm; s1 = negm;
#pragma unroll 1
                        for (int ks = 0; ks < 8; ++ks) {
                            bf16x8 qq = qf[0];
#pragma unroll
                            for (int u = 1; u < 8; ++u) qq = (ks == u) ? qf[u] : qq;
                            s0 = MFMA32(*(LAS bf16x8*)(sl + (ks >> 2) * 5120 + (ks & 3) * 1024), qq, s0);
                            s1 = MFMA32(*(LAS bf16x8*)(sl + 10240 + (ks >> 2) * 5120 + (ks & 3) * 1024), qq, s1);
                        }
                        B_MASK();
                    }
                    float mx = s0[0];
#pragma unroll
                    for (int i = 1; i < 16; ++i) mx = fmaxf(mx, s0[i]);
#pragma unroll
                    for (int i = 0; i < 16; ++i) mx = fmaxf(mx, s1[i]);
                    auto rr = __builtin_amdgcn_permlane32_swap(__float_as_uint(mx), __float_as_uint(mx), false, false);
                    mx = fmaxf(__uint_as_float(rr[0]), __uint_as_float(rr[1]));
                    float delta = mx;
                    if (t > 0) {
                        delta = fmaxf(mx, 0.f);
                        const float alpha = __builtin_amdgcn_exp2f(-delta);
                        l *= alpha;
#pragma unroll
                        for (int db = 0; db < 4; ++db)
#pragma unroll
                            for (int i = 0; i < 16; ++i) o[db][i] *= alpha;
                    }
                    const float nm = negm[0] - delta;
#pragma unroll
                    for (int i = 0; i < 16; ++i) negm[i] = nm;
                    ps = 0.f;
#pragma unroll
                    for (int i = 0; i < 16; ++i) { s0[i] = __builtin_amdgcn_exp2f(s0[i] - delta); ps += s0[i]; }
#pragma unroll
                    for (int i = 0; i < 16; ++i) { s1[i] = __builtin_amdgcn_exp2f(s1[i] - delta); ps += s1[i]; }
                }
                l += ps;
                bf16x8 pf[4];
                pf[0] = __builtin_bit_cast(bf16x8, pack8(s0, 0));
                pf[1] = __builtin_bit_cast(bf16x8, pack8(s0, 1));
                pf[2] = __builtin_bit_cast(bf16x8, pack8(s1, 0));
                pf[3] = __builtin_bit_cast(bf16x8, pack8(s1, 1));
                SEG(2);
                if (it + 1 < NIT) { K2_SYNC(it); } else { K2_M0(it + 3); }
                SEG(3);
                sfor<0, 16>([&](auto ic) {
                    constexpr int j = decltype(ic)::value, s4 = j >> 2, db = j & 3;
                    lds_wait<7>(fr[j & 7]);
                    o[db] = MFMA32(fr[j & 7], pf[s4], o[db]);
                    SBAR();
                    if constexpr (j < 8) lds_rd<BOFF(24 + j)>(fr[j & 7], ac);
                    else { constexpr int jn = j - 8; lds_rd<BOFF((jn & 1) * 8 + (jn >> 1))>(fr[j & 7], an); }
                    if constexpr (j == 4) K2_DMA(0);
                    if constexpr (j == 10) K2_DMA(1);
                    SBAR();
                });
                SEG(4);
            }
            auto rr = __builtin_amdgcn_permlane32_swap(__float_as_uint(l), __float_as_uint(l), false, false);
            const float inv = 1.0f / (__uint_as_float(rr[0]) + __uint_as_float(rr[1]));
            char* cp = ws + WS_CTXB + (size_t)(tb32 * 256 + head * 8) * 1024 + lane * 16;
#pragma unroll
            for (int db = 0; db < 4; ++db) {
                st16(cp + (2 * db) * 1024, pack8s(o[db], 0, inv));
                st16(cp + (2 * db + 1) * 1024, pack8s(o[db], 1, inv));
            }
        }
        WAIT_VM(0);
        STAMP(1, 2);
#undef B_MASK
    } else {
        const int tb32 = x * 8 + wa * 2;
        bf16x8 qf[2][2];
        const char* qp = ws + WS_QA + (size_t)((head * 64 + tb32) * 2) * 1024 + lane * 16;
#pragma unroll
        for (int qt = 0; qt < 2; ++qt)
#pragma unroll
            for (int ks = 0; ks < 2; ++ks) qf[qt][ks] = *(const bf16x8*)(qp + (qt * 2 + ks) * 1024);
        f32x16 o[2];
        o[0] = f32x16{}; o[1] = f32x16{};
        float negm[2] = {0.f, 0.f};
        bf16x8 kf[4], vf[4];
        f32x16 s[2][2];
        K2_SYNC(-1);
        lds_rd<AOFF(0)>(kf[0], lbase); lds_rd<AOFF(1)>(kf[1], lbase); lds_rd<AOFF(2)>(kf[2], lbase); lds_rd<AOFF(3)>(kf[3], lbase);
        for (int it = 0; it <= 32; ++it) {
            SEG(0);
            if (it > 0) {
                u32x4 pf[2][2][2];
                unsigned chk = 0;
                sfor<0, 4>([&](auto bc) {
                    constexpr int blk = decltype(bc)::value, kb = blk >> 1, qt = blk & 1;
                    f32x16 e;
#pragma unroll
                    for (int i = 0; i < 8; ++i) e[i] = __builtin_amdgcn_exp2f(s[kb][qt][i]);
                    K2_DMA(blk);
#pragma unroll
                    for (int i = 8; i < 16; ++i) e[i] = __builtin_amdgcn_exp2f(s[kb][qt][i]);
                    if constexpr (blk == 3) K2_DMA(4);
                    pf[kb][qt][0] = pack8(e, 0);
                    pf[kb][qt][1] = pack8(e, 1);
                    chk |= pf[kb][qt][0][0] | pf[kb][qt][0][1] | pf[kb][qt][0][2] | pf[kb][qt][0][3];
                    chk |= pf[kb][qt][1][0] | pf[kb][qt][1][1] | pf[kb][qt][1][2] | pf[kb][qt][1][3];
                });
                const bool first = (it == 1);
                if (first || __any((chk & 0x40004000u) != 0u)) {
#pragma unroll
                    for (int qt = 0; qt < 2; ++qt) {
                        float mx = s[0][qt][0];
#pragma unroll
                        for (int i = 1; i < 16; ++i) mx = fmaxf(mx, s[0][qt][i]);
#pragma unroll
                        for (int i = 0; i < 16; ++i) mx = fmaxf(mx, s[1][qt][i]);
                        auto rr = __builtin_amdgcn_permlane32_swap(__float_as_uint(mx), __float_as_uint(mx), false, false);
                        mx = fmaxf(__uint_as_float(rr[0]), __uint_as_float(rr[1]));
                        float delta = mx + 10.f;
                        if (!first) delta = fmaxf(delta, 0.f);
                        const float tgt = negm[qt] - delta;
                        const unsigned hi = pack2(tgt, 0.f) & 0xFFFFu;
                        const float hif = __uint_as_float(hi << 16);
                        const unsigned lo = pack2(tgt - hif, 0.f) & 0xFFFFu;
                        const float neff = hif + __uint_as_float(lo << 16);
                        delta = negm[qt] - neff;
                        negm[qt] = neff;
                        {
                            u32x4 qq = __builtin_bit_cast(u32x4, qf[qt][1]);
                            qq[2] = h ? 0u : (hi | (lo << 16));
                            qf[qt][1] = __builtin_bit_cast(bf16x8, qq);
                        }
                        if (!first) {
                            const float alpha = __builtin_amdgcn_exp2f(-delta);
#pragma unroll
                            for (int i = 0; i < 16; ++i) o[qt][i] *= alpha;
                        }
#pragma unroll
                        for (int kb = 0; kb < 2; ++kb) {
                            f32x16 e;
#pragma unroll
                            for (int i = 0; i < 16; ++i) e[i] = __builtin_amdgcn_exp2f(s[kb][qt][i] - delta);
                            pf[kb][qt][0] = pack8(e, 0);
                            pf[kb][qt][1] = pack8(e, 1);
                        }
                    }
                }
                SEG(1);
                lds_wait2<4>(vf[0], vf[1]);
                lds_wait2<4>(vf[2], vf[3]);
#pragma unroll
                for (int kb = 0; kb < 2; ++kb)
#pragma unroll
                    for (int s2 = 0; s2 < 2; ++s2)
#pragma unroll
                        for (int qt = 0; qt < 2; ++qt)
                            o[qt] = MFMA32(vf[kb * 2 + s2], __builtin_bit_cast(bf16x8, pf[kb][qt][s2]), o[qt]);
            } else {
                K2_DMA(0); K2_DMA(1); K2_DMA(2); K2_DMA(3); K2_DMA(4);
            }
            SEG(2);
            lds_wait2<0>(kf[0], kf[1]);
            lds_wait2<0>(kf[2], kf[3]);
#pragma unroll
            for (int kb = 0; kb < 2; ++kb)
#pragma unroll
                for (int qt = 0; qt < 2; ++qt) {
                    s[kb][qt] = MFMA32(kf[kb * 2], qf[qt][0], f32x16{});
                    s[kb][qt] = MFMA32(kf[kb * 2 + 1], qf[qt][1], s[kb][qt]);
                }
            SBAR();
            if (it == 32) STAMPW(1, 3, 4);
            SEG(3);
            K2_SYNC(it);
            SEG(4);
            const unsigned ac = lbase + (it & 3) * SLOT;
            const unsigned an = lbase + ((it + 1) & 3) * SLOT;
            lds_rd<AOFF(4)>(vf[0], ac); lds_rd<AOFF(5)>(vf[1], ac); lds_rd<AOFF(6)>(vf[2], ac); lds_rd<AOFF(7)>(vf[3], ac);
            lds_rd<AOFF(0)>(kf[0], an); lds_rd<AOFF(1)>(kf[1], an); lds_rd<AOFF(2)>(kf[2], an); lds_rd<AOFF(3)>(kf[3], an);
        }
        WAIT_VM(0);
#pragma unroll
        for (int qt = 0; qt < 2; ++qt) {
            const unsigned lv = __float_as_uint(o[qt][8]);
            auto rr = __builtin_amdgcn_permlane32_swap(lv, lv, false, false);
            const float lsum = __uint_as_float(rr[1]);
            const float inv = 1.0f / lsum;
            char* cp = ws + WS_CTXA + (size_t)((tb32 + qt) * 40) * 1024;
#pragma unroll
            for (int gq = 0; gq < 3; ++gq) {
                const int d0 = 8 * gq + 4 * h;
                if (d0 < 20) {
                    const int k = head * 20 + d0;
                    u32x2 w;
                    w[0] = pack2(o[qt][4 * gq + 0] * inv, o[qt][4 * gq + 1] * inv);
                    w[1] = pack2(o[qt][4 * gq + 2] * inv, o[qt][4 * gq + 3] * inv);
                    *(u32x2*)(cp + (k >> 4) * 1024 + (r + 32 * ((k >> 3) & 1)) * 16 + (k & 4) * 2) = w;
                }
            }
        }
    }
}

constexpr int G3_STAGE = 49152;
constexpr int G3_LDS = 3 * G3_STAGE;

__global__ __launch_bounds__(512, 2) void k3_oproj(const __hip_bfloat16* __restrict__ Awo, const __hip_bfloat16* __restrict__ Bwo,
                                                   char* __restrict__ ws, __hip_bfloat16* __restrict__ out) {
    extern __shared__ __attribute__((aligned(16))) char smem[];
    LAS char* lds = (LAS char*)smem;
    const int tid = threadIdx.x, lane = tid & 63, wid = __builtin_amdgcn_readfirstlane(tid >> 6);
    const int wm = wid >> 1, wn = wid & 1, h = lane >> 5;
    const int bid = blockIdx.x;
    const bool isB = bid < 200;
    int mt, nt, sp, kt0, nkt, kstot;
    const char* Xc; const char* Wb;
    if (isB) {
        mt = bid & 7; const int rest = bid >> 3; nt = rest % 5; sp = rest / 5;
        kt0 = sp * 13; nkt = sp == 4 ? 12 : 13; kstot = 256;
        Xc = ws + WS_CTXB; Wb = (const char*)Bwo;
    } else {
        const int b2 = bid - 200; mt = b2 & 7; nt = b2 >> 3; sp = 5;
        kt0 = 0; nkt = 10; kstot = 40;
        Xc = ws + WS_CTXA; Wb = (const char*)Awo;
    }
    unsigned woff[2];
#pragma unroll
    for (int i = 0; i < 2; ++i) {
        const int k = (wid * 2 + i) * 4 + (lane >> 4);
        const int p = lane & 15, b = (p >> 2) ^ (k & 3);
        woff[i] = (unsigned)(((kt0 * 64 + k) * 640 + nt * 128 + (b * 4 + (p & 3)) * 8) * 2);
    }
    const unsigned lds0 = (unsigned)(uintptr_t)lds;
    const char* xsb = Xc + (size_t)((mt * 8 + wid) * kstot + kt0 * 4) * 1024 + 4096;
    const unsigned voff = lane * 16;
    woff[1] -= 1024;
    auto piece = [&](int kt, auto ic) {
        constexpr int i = decltype(ic)::value;
        if constexpr (i < 4) dma_s<i * 1024 - 4096>(voff, xsb + (size_t)kt * 4096);
        else dma_s<(i - 4) * 1024>(woff[i - 4], Wb + (size_t)kt * (64u * 640u * 2u));
    };
    const int q = (lane >> 2) & 3, p4 = lane & 3, g16 = (lane >> 4) & 1;
    const int r0 = isB ? 4 * h : 8 * h, r1 = isB ? 8 + 4 * h : 8 * h + 4;
    int wb0[2], wb1[2];
#pragma unroll
    for (int ni = 0; ni < 2; ++ni) {
        const int b = wn * 2 + ni;
        const int c = ((b ^ q) * 64) + g16 * 32 + p4 * 8;
        wb0[ni] = (r0 >> 3) * 6144 + 4096 + ((r0 & 7) + q) * 256 + c;
        wb1[ni] = (r1 >> 3) * 6144 + 4096 + ((r1 & 7) + q) * 256 + c;
    }
    const int xb0 = wm * 2 * 6144 + lane * 16;
    f32x16 acc[2][2];
#pragma unroll
    for (int mi = 0; mi < 2; ++mi)
#pragma unroll
        for (int ni = 0; ni < 2; ++ni) acc[mi][ni] = f32x16{};

    bf16x8 xf[2][4][2], wf[2][4][2];
#define G3_RD(set, bb)                                                                                           \
    do {                                                                                                         \
        _Pragma("unroll") for (int ks = 0; ks < 4; ++ks) {                                                       \
            _Pragma("unroll") for (int ni = 0; ni < 2; ++ni)                                                     \
                wf[set][ks][ni] = tr_frag((bb) + wb0[ni] + ks * 12288, (bb) + wb1[ni] + ks * 12288);             \
            _Pragma("unroll") for (int mi = 0; mi < 2; ++mi)                                                     \
                xf[set][ks][mi] = *(LAS bf16x8*)((bb) + xb0 + mi * 6144 + ks * 1024);                            \
        }                                                                                                        \
    } while (0)
#define G3_STEP(set, kt)                                                                                         \
    do {                                                                                                         \
        if ((kt) + 2 < nkt) { WAIT_VM(6); } else { WAIT_VM(0); }                                                 \
        LGKM0();                                                                                                 \
        BARRIER();                                                                                               \
        const int b0_ = (kt) % 3, b1_ = ((kt) + 1) % 3;                                                          \
        set_m0(lds0 + b0_ * G3_STAGE + wid * 6144 + 4096);                                                       \
        if ((kt) + 1 < nkt) G3_RD((set) ^ 1, lds + b1_ * G3_STAGE);                                              \
        SBAR();                                                                                                  \
        const bool on_ = (kt) + 3 < nkt;                                                                         \
        sfor<0, 8>([&](auto jc) {                                                                                \
            constexpr int j = decltype(jc)::value, ks = j >> 1, mi = j & 1;                                      \
            _Pragma("unroll") for (int ni = 0; ni < 2; ++ni)                                                     \
                acc[mi][ni] = MFMA32(wf[set][ks][ni], xf[set][ks][mi], acc[mi][ni]);                             \
            if constexpr (j < 6) { SBAR(); if (on_) piece((kt) + 3, jc); SBAR(); }                               \
        });                                                                                                      \
        SBAR();                                                                                                  \
    } while (0)
    STAMP(2, 0);
#pragma unroll
    for (int t = 0; t < 3; ++t) {
        set_m0(lds0 + t * G3_STAGE + wid * 6144 + 4096);
        sfor<0, 6>([&](auto ic) { piece(t, ic); });
    }
    WAIT_VM(12);
    BARRIER();
    STAMP(2, 1);
    G3_RD(0, lds);
    int kt = 0;
#pragma unroll 1
    for (; kt + 1 < nkt; kt += 2) { G3_STEP(0, kt); G3_STEP(1, kt + 1); }
    if (kt < nkt) G3_STEP(0, kt);
#undef G3_RD
#undef G3_STEP
    STAMP(2, 2);
    const int r = lane & 31;
    if (!isB) {
#pragma unroll
        for (int mi = 0; mi < 2; ++mi)
#pragma unroll
            for (int ni = 0; ni < 2; ++ni) {
                char* op = (char*)out + (size_t)((mt * 8 + wm * 2 + mi) * 32 + r) * 2560 + ((nt * 4 + wn * 2 + ni) * 32 + 4 * h) * 2;
#pragma unroll
                for (int gq = 0; gq < 4; ++gq) {
                    u32x2 o;
                    o[0] = pack2(acc[mi][ni][4 * gq], acc[mi][ni][4 * gq + 1]);
                    o[1] = pack2(acc[mi][ni][4 * gq + 2], acc[mi][ni][4 * gq + 3]);
                    *(u32x2*)(op + gq * 16) = o;
                }
            }
        STAMP(2, 3);
        return;
    }
#pragma unroll
    for (int mi = 0; mi < 2; ++mi)
#pragma unroll
        for (int ni = 0; ni < 2; ++ni) {
            const int mt32 = mt * 8 + wm * 2 + mi, nt32 = nt * 4 + wn * 2 + ni;
            char* dst = ws + WS_PART + (size_t)sp * SLAB + (size_t)(mt32 * 20 + nt32) * 4096 + lane * 16;
#pragma unroll
            for (int gq = 0; gq < 4; ++gq) {
                f32x4 v = {acc[mi][ni][4 * gq], acc[mi][ni][4 * gq + 1], acc[mi][ni][4 * gq + 2], acc[mi][ni][4 * gq + 3]};
                asm volatile("global_store_dwordx4 %0, %1, off sc1\n\ts_nop 1" :: "v"(dst + gq * 1024), "v"(v) : "memory");
            }
        }
    asm volatile("s_waitcnt vmcnt(0)" ::: "memory");
    BARRIER();
    unsigned* cnt = (unsigned*)(ws + WS_CNT) + (mt * 5 + nt) * 64;
    if (wid == 0) {
        if (lane == 0) __hip_atomic_fetch_add(cnt, 1u, __ATOMIC_RELAXED, __HIP_MEMORY_SCOPE_AGENT);
        for (unsigned spins = 0; spins < (1u << 24); ++spins) {
            const unsigned c = __builtin_amdgcn_readfirstlane(__hip_atomic_load(cnt, __ATOMIC_RELAXED, __HIP_MEMORY_SCOPE_AGENT));
            if (c >= (unsigned)NSPLIT) break;
            __builtin_amdgcn_s_sleep(1);
        }
        __builtin_amdgcn_fence(__ATOMIC_ACQUIRE, "agent");
        asm volatile("s_waitcnt vmcnt(0)" ::: "memory");
    }
    BARRIER();
    const int st = sp + 5 * wid;
    if (st < 32) {
        const int mt32 = mt * 8 + (st >> 2), nt32 = nt * 4 + (st & 3);
        const char* src = ws + WS_PART + (size_t)(mt32 * 20 + nt32) * 4096 + lane * 16;
        f32x4 v[4][NSPLIT];
#pragma unroll
        for (int gq = 0; gq < 4; ++gq)
#pragma unroll
            for (int s = 0; s < NSPLIT; ++s) v[gq][s] = *(const f32x4*)(src + (size_t)s * SLAB + gq * 1024);
        char* op = (char*)out + (size_t)(mt32 * 32 + r) * 2560 + (640 + nt32 * 32 + 4 * h) * 2;
#pragma unroll
        for (int gq = 0; gq < 4; ++gq) {
            const f32x4 t = ((v[gq][0] + v[gq][1]) + (v[gq][2] + v[gq][3])) + v[gq][4];
            u32x2 o;
            o[0] = pack2(t[0], t[1]);
            o[1] = pack2(t[2], t[3]);
            *(u32x2*)(op + gq * 16) = o;
        }
    }
    STAMP(2, 3);
}

extern "C" void kernel_launch(void* const* d_in, const int* in_sizes, int n_in,
                              void* d_out, int out_size, void* d_ws, size_t ws_size,
                              hipStream_t stream) {
    (void)in_sizes; (void)n_in; (void)out_size; (void)ws_size;
    const __hip_bfloat16* Ax = (const __hip_bfloat16*)d_in[0];
    const __hip_bfloat16* Awqkv = (const __hip_bfloat16*)d_in[1];
    const __hip_bfloat16* Awo = (const __hip_bfloat16*)d_in[2];
    const __hip_bfloat16* Bx = (const __hip_bfloat16*)d_in[3];
    const __hip_bfloat16* Bckv = (const __hip_bfloat16*)d_in[4];
    const __hip_bfloat16* Bwq = (const __hip_bfloat16*)d_in[5];
    const __hip_bfloat16* Bwuk = (const __hip_bfloat16*)d_in[6];
    const __hip_bfloat16* Bwuv = (const __hip_bfloat16*)d_in[7];
    const __hip_bfloat16* Bwo = (const __hip_bfloat16*)d_in[8];
    char* ws = (char*)d_ws;
    (void)hipFuncSetAttribute((const void*)k1_proj, hipFuncAttributeMaxDynamicSharedMemorySize, G1_LDS);
    (void)hipFuncSetAttribute((const void*)k2_attn, hipFuncAttributeMaxDynamicSharedMemorySize, K2_LDS);
    (void)hipFuncSetAttribute((const void*)k3_oproj, hipFuncAttributeMaxDynamicSharedMemorySize, G3_LDS);
    k1_proj<<<256, 512, G1_LDS, stream>>>(Ax, Awqkv, Bx, Bckv, Bwq, Bwuk, Bwuv, Awo, Bwo, ws);
    k2_attn<<<256, 512, K2_LDS, stream>>>(ws);
    k3_oproj<<<240, 512, G3_LDS, stream>>>(Awo, Bwo, ws, (__hip_bfloat16*)d_out);
}
```

```cpp
#include <hip/hip_runtime.h>
#include <hip/hip_bf16.h>
#include <stdint.h>
#include <type_traits>

typedef short s16x4 __attribute__((ext_vector_type(4)));
typedef short bf16x8 __attribute__((ext_vector_type(8)));
typedef float f32x16 __attribute__((ext_vector_type(16)));
typedef float f32x4 __attribute__((ext_vector_type(4)));
typedef float f32x2 __attribute__((ext_vector_type(2)));
typedef unsigned u32x4 __attribute__((ext_vector_type(4)));
typedef unsigned u32x2 __attribute__((ext_vector_type(2)));
typedef __bf16 bf16x2_t __attribute__((ext_vector_type(2)));
#define LAS __attribute__((address_space(3)))
#define DI __device__ __forceinline__
#define MFMA32(a, b, c) __builtin_amdgcn_mfma_f32_32x32x16_bf16((a), (b), (c), 0, 0, 0)
#define MFMA16(a, b, c) __builtin_amdgcn_mfma_f32_16x16x32_bf16((a), (b), (c), 0, 0, 0)
#define CFENCE() asm volatile("" ::: "memory")
#define BARRIER() do { CFENCE(); __builtin_amdgcn_s_barrier(); CFENCE(); } while (0)
#define WAIT_VM(n) asm volatile("s_waitcnt vmcnt(" #n ") lgkmcnt(0)" ::: "memory")
#define SBAR() __builtin_amdgcn_sched_barrier(0)

DI unsigned pack2(float a, float b) {
    f32x2 v = {a, b};
    bf16x2_t r = __builtin_convertvector(v, bf16x2_t);
    return __builtin_bit_cast(unsigned, r);
}
DI u32x4 pack8(const f32x16& x, int s) {
    u32x4 w;
    w[0] = pack2(x[8 * s + 0], x[8 * s + 1]);
    w[1] = pack2(x[8 * s + 2], x[8 * s + 3]);
    w[2] = pack2(x[8 * s + 4], x[8 * s + 5]);
    w[3] = pack2(x[8 * s + 6], x[8 * s + 7]);
    return w;
}
DI u32x4 pack8s(const f32x16& x, int s, float c) {
    u32x4 w;
    w[0] = pack2(x[8 * s + 0] * c, x[8 * s + 1] * c);
    w[1] = pack2(x[8 * s + 2] * c, x[8 * s + 3] * c);
    w[2] = pack2(x[8 * s + 4] * c, x[8 * s + 5] * c);
    w[3] = pack2(x[8 * s + 6] * c, x[8 * s + 7] * c);
    return w;
}
DI void glds16(const void* g, LAS char* l) {
    __builtin_amdgcn_global_load_lds((const unsigned*)g, (LAS unsigned*)l, 16, 0, 0);
}
DI bf16x8 tr_frag(LAS char* p0, LAS char* p1) {
    s16x4 lo = __builtin_amdgcn_ds_read_tr16_b64_v4i16((LAS s16x4*)p0);
    s16x4 hi = __builtin_amdgcn_ds_read_tr16_b64_v4i16((LAS s16x4*)p1);
    return __builtin_shufflevector(lo, hi, 0, 1, 2, 3, 4, 5, 6, 7);
}

#define STMODE 2
DI void st16(void* p, u32x4 v) {
    asm volatile("global_store_dwordx4 %0, %1, off sc1\n\ts_nop 1" :: "v"(p), "v"(v) : "memory");
}
template <int I, int N, class F> DI void sfor(F&& f) {
    if constexpr (I < N) { f(std::integral_constant<int, I>{}); sfor<I + 1, N>(f); }
}
template <int OFF> DI void lds_rd(bf16x8& d, unsigned addr) {
    asm volatile("ds_read_b128 %0, %1 offset:%2" : "=v"(d) : "v"(addr), "i"(OFF));
}
template <int N> DI void lds_wait(bf16x8& d) { asm volatile("s_waitcnt lgkmcnt(%1)" : "+v"(d) : "i"(N)); }
template <int N> DI void lds_wait2(bf16x8& d, bf16x8& e) { asm volatile("s_waitcnt lgkmcnt(%2)" : "+v"(d), "+v"(e) : "i"(N)); }
#define LGKM0() __builtin_amdgcn_s_waitcnt(0xC07F)
DI void set_m0(unsigned v) { asm volatile("s_mov_b32 m0, %0\n\ts_nop 0" :: "s"(v) : "memory"); }
template <int OFF> DI void dma_s(unsigned voff, const char* sbase) {
    asm volatile("global_load_lds_dwordx4 %0, %1 offset:%2" :: "v"(voff), "s"(sbase), "i"(OFF) : "memory");
}

#define ABL_NOPIECE 0
#define ABL_NOBAR 0
#define ABL_T0 0
#define ABL_K2 0
#define ABL_NODMA 0
#define ABL_NOMFMA 0
#define SEG(i) do { } while (0)
#define STAMP(kid, idx) do { } while (0)
#define STAMPW(kid, idx, w) do { } while (0)
constexpr size_t WS_KVB = 0;
constexpr size_t WS_QB = WS_KVB + (size_t)32 * 1048576;
constexpr size_t WS_KVA = WS_QB + (size_t)16 * 1048576;
constexpr size_t WS_QA = WS_KVA + (size_t)8 * 1048576;
constexpr size_t WS_CTXB = WS_QA + (size_t)4 * 1048576;
constexpr size_t WS_CTXA = WS_CTXB + (size_t)16 * 1048576;
constexpr size_t WS_PART = WS_CTXA + (size_t)4 * 1048576;
constexpr size_t WS_CNT = WS_PART + (size_t)32 * 1048576;
constexpr int NSPLIT = 5;
constexpr size_t SLAB = (size_t)64 * 20 * 4096;

constexpr float CB = 0.08838834764831845f * 1.4426950408889634f;
constexpr float CA = 0.22360679774997896f * 1.4426950408889634f;

#define PREFETCH 1
constexpr int G1_LDS = 2 * 65536 + 2048;
enum { EP_BK = 0, EP_BV = 1, EP_BQ = 2, EP_AQK = 3, EP_AV = 4 };

struct G1Src { unsigned xoff[4], woff[4]; const char* Xb; const char* Wb; unsigned wstep; int nkt; bool half; };
DI void g1_src(G1Src& s, const __hip_bfloat16* X, const __hip_bfloat16* W, int K, int ldw, int m0, int n0, int nlim, int wid, int lane, bool half) {
    s.half = half;
#pragma unroll
    for (int i = 0; i < 4; ++i) {
        const int row = (wid * 4 + i) * 8 + (lane >> 3);
        const int c = (lane & 7) ^ ((row >> 1) & 7);
        s.xoff[i] = (unsigned)(((m0 + row) * K + c * 8) * 2) + 4096 - i * 1024;
        const int k = (wid * 4 + i) * 2 + (lane >> 5);
        const int p = lane & 31, b = (p >> 2) ^ (k & 3);
        int col = n0 + (b * 4 + ((p & 3) ^ ((wid & 1) << 1))) * 8;
        col = col < nlim ? col : nlim - 8;
        s.woff[i] = (unsigned)((k * ldw + col) * 2) - i * 1024;
    }
    s.Xb = (const char*)X; s.Wb = (const char*)W; s.wstep = (unsigned)(64 * ldw * 2); s.nkt = K >> 6;
}
template <int I> DI void g1_piece(const G1Src& s, int kt) {
    if constexpr (I < 4) dma_s<I * 1024 - 4096>(s.xoff[I], s.Xb + (size_t)kt * 128);
    else if (!s.half || (threadIdx.x & 16) == 0) dma_s<(I - 4) * 1024>(s.woff[I - 4], s.Wb + (size_t)kt * s.wstep);
}

template <bool TR, int NT>
DI void g1_loop(f32x4 (&acc)[8][4], bf16x8 (&xs)[2][4], bf16x8 (&wsb)[2][4], const G1Src& cur, const G1Src& nxt, bool has_next, bool first,
                LAS char* lds, int wid, int lane) {
    const int wm = wid >> 2, wn = wid & 3, r16 = lane & 15, g = lane >> 4;
    const unsigned lds0 = (unsigned)(uintptr_t)lds;
    const int nkt = cur.nkt;
    int xb[2];
#pragma unroll
    for (int st = 0; st < 2; ++st) xb[st] = wm * 4 * 8192 + r16 * 128 + (((4 * st + g) ^ (r16 >> 1)) * 16);
    const int q = (lane >> 2) & 3, p4 = lane & 3;
    int wb[4];
#pragma unroll
    for (int nt = 0; nt < 4; ++nt) {
        const int b = NT == 4 ? wn * 2 + (nt >> 1) : wn;
        wb[nt] = g * 8192 + 4096 + q * 512 + ((b ^ q) * 64) + (((nt & 1) ^ (g & 1)) * 32) + p4 * 8;
    }
#pragma unroll
    for (int mt = 0; mt < 8; ++mt)
#pragma unroll
        for (int nt = 0; nt < 4; ++nt) acc[mt][nt] = f32x4{};
#define G1_RX(set, bb, st, mh)                                                                                   \
    _Pragma("unroll") for (int m4 = 0; m4 < 4; ++m4)                                                             \
        xs[set][m4] = *(LAS bf16x8*)((bb) + xb[st] + ((mh) * 2 + (m4 >> 1)) * 8192 + (m4 & 1) * 2048)
#define G1_RW(bb, st)                                                                                            \
    _Pragma("unroll") for (int nt = 0; nt < NTR; ++nt)                                                           \
        wsb[st][nt] = tr_frag((bb) + wb[nt] + (st) * 32768, (bb) + wb[nt] + (st) * 32768 + 2048)
#define G1_MM(set, st, mh, on, kn, p0, pe)                                                                       \
    sfor<0, 4>([&](auto mc) {                                                                                    \
        constexpr int m4 = decltype(mc)::value;                                                                  \
        _Pragma("unroll") for (int nt = 0; nt < NT; ++nt)                                                        \
            acc[(mh) * 4 + m4][nt] = TR ? MFMA16(wsb[st][nt], xs[set][m4], acc[(mh) * 4 + m4][nt])               \
                                        : MFMA16(xs[set][m4], wsb[st][nt], acc[(mh) * 4 + m4][nt]);              \
        if constexpr ((p0) + m4 < (pe)) {                                                                        \
            SBAR();                                                                                              \
            if (on) { if ((kn) < nkt) g1_piece<(p0) + m4>(cur, (kn)); else g1_piece<(p0) + m4>(nxt, (kn) - nkt); } \
            SBAR();                                                                                              \
        }                                                                                                        \
    })
    constexpr int NTR = NT;
    G1_RW(lds, 0);
    if (first) { G1_RX(0, lds, 0, 0); }
#pragma unroll 1
    for (int kt = 0; kt < nkt; ++kt) {
        LAS char* bb = lds + (kt & 1) * 65536;
        LAS char* bn = lds + ((kt + 1) & 1) * 65536;
        const bool on1 = !(first && kt == 0) && (kt + 1 < nkt || has_next);
        const bool on2 = kt + 2 < nkt || has_next;
        LGKM0(); G1_RX(1, bb, 0, 1); SBAR(); G1_MM(0, 0, 0, on1, kt + 1, 4, 8); SBAR();
        LGKM0(); G1_RW(bb, 1); G1_RX(0, bb, 1, 0); SBAR(); G1_MM(1, 0, 1, false, 0, 0, 0); SBAR();
        LGKM0(); G1_RX(1, bb, 1, 1); SBAR(); G1_MM(0, 1, 0, false, 0, 0, 0); SBAR();
        LGKM0(); WAIT_VM(0);
        BARRIER();
        set_m0(lds0 + (kt & 1) * 65536 + wid * 8192 + 4096);
        if (kt + 1 < nkt) G1_RW(bn, 0);
        if (kt + 1 < nkt || has_next) G1_RX(0, bn, 0, 0);
        SBAR(); G1_MM(1, 1, 1, on2, kt + 2, 0, 4); SBAR();
    }
#undef G1_RX
#undef G1_RW
#undef G1_MM
}

DI void st8(void* p, u32x2 v) {
    asm volatile("global_store_dwordx2 %0, %1, off sc1\n\ts_nop 1" :: "v"(p), "v"(v) : "memory");
}

template <int EP>
DI void g1_epi(const f32x4 (&acc)[8][4], int m0, int n0, int nlim, int nsec, char* __restrict__ ws, int wid, int lane) {
    const int wm = wid >> 2, wn = wid & 3, c = lane & 15, g = lane >> 4;
    if constexpr (EP == EP_BK || EP == EP_BQ || EP == EP_BV) {
#pragma unroll
        for (int mt = 0; mt < 8; ++mt) {
            const int tb = (m0 + wm * 128 + mt * 16) >> 5;
            const int tile = tb >> 1, kb = tb & 1, th = mt & 1;
#pragma unroll
            for (int np = 0; np < 2; ++np) {
                const int nb = n0 + wn * 64 + np * 32;
                const int head = nb >> 7;
                const float sc = EP == EP_BQ ? CB : 1.0f;
                const f32x4 a = acc[mt][2 * np], b = acc[mt][2 * np + 1];
                unsigned a0 = pack2(a[0] * sc, a[1] * sc), a1 = pack2(a[2] * sc, a[3] * sc);
                unsigned b0 = pack2(b[0] * sc, b[1] * sc), b1 = pack2(b[2] * sc, b[3] * sc);
                if constexpr (EP == EP_BV) {
                    auto r0 = __builtin_amdgcn_permlane32_swap(a0, b0, false, false);
                    auto r1 = __builtin_amdgcn_permlane32_swap(a1, b1, false, false);
                    const u32x4 w = {r0[0], r1[0], r0[1], r1[1]};
                    const int db = (nb >> 5) & 3;
                    char* base = ws + WS_KVB + (size_t)head * 1048576 + tile * 32768 + 16384 + ((kb * 2 + th) * 4 + db) * 1024;
                    st16(base + ((g >> 1) * 16 + c + 32 * (g & 1)) * 16, w);
                } else {
                    auto r0 = __builtin_amdgcn_permlane16_swap(a0, b0, false, false);
                    auto r1 = __builtin_amdgcn_permlane16_swap(a1, b1, false, false);
                    const u32x4 w = {r0[0], r1[0], r0[1], r1[1]};
                    const int ks = ((nb >> 4) & 7) + (g & 1);
                    char* base = EP == EP_BK ? ws + WS_KVB + (size_t)head * 1048576 + tile * 32768 + (kb * 8 + ks) * 1024
                                             : ws + WS_QB + (size_t)((head * 64 + tb) * 8 + ks) * 1024;
                    st16(base + (th * 16 + c + 32 * (g >> 1)) * 16, w);
                }
            }
        }
        return;
    }
#pragma unroll
    for (int mt = 0; mt < 8; ++mt) {
        const int tb = (m0 + wm * 128 + mt * 16) >> 5;
        const int tile = tb >> 1, kb = tb & 1, th = mt & 1;
#pragma unroll
        for (int nt = 0; nt < 2; ++nt) {
            const int nb = n0 + wn * 32 + nt * 16;
            if (nb >= nlim) continue;
            const f32x4 a = acc[mt][nt];
            if constexpr (EP == EP_BK || EP == EP_BQ || EP == EP_BV) {
            } else if constexpr (EP == EP_AQK) {
                const bool isq = nb < 640;
                const float sc = isq ? CA : 1.0f;
                const int r = th * 16 + c;
                const int n = nb - (isq ? 0 : 640) + 4 * g;
                const int head = n / 20, d0 = n - head * 20;
                char* base = isq ? ws + WS_QA + (size_t)((head * 64 + tb) * 2) * 1024
                                 : ws + WS_KVA + (size_t)head * 262144 + tile * 8192 + kb * 2048;
                char* dst = base + (d0 >> 4) * 1024 + (r + 32 * ((d0 >> 3) & 1)) * 16 + (d0 & 4) * 2;
                u32x2 w;
                w[0] = pack2(a[0] * sc, a[1] * sc);
                w[1] = pack2(a[2] * sc, a[3] * sc);
                *(u32x2*)dst = w;
                if (d0 == 16) {
                    *(u32x2*)(dst + 8) = (u32x2){isq ? 0u : 0x3F803F80u, 0u};
                    *(u32x4*)(base + 1024 + (r + 32) * 16) = (u32x4){0u, 0u, 0u, 0u};
                }
            } else {
                const int n = nb - nsec + c;
                const int head = n / 20, d = n - head * 20;
                char* dst = ws + WS_KVA + (size_t)head * 262144 + tile * 8192 + 4096 + (kb * 2 + th) * 1024 + (d + 32 * (g & 1)) * 16 + (g >> 1) * 8;
                u32x2 w;
                w[0] = pack2(a[0], a[1]);
                w[1] = pack2(a[2], a[3]);
                *(u32x2*)dst = w;
                if (d == 19) *(u32x2*)(dst + 16) = (u32x2){0x3F803F80u, 0x3F803F80u};
            }
        }
    }
}

__global__ __launch_bounds__(512, 2) void k1_proj(const __hip_bfloat16* __restrict__ Ax, const __hip_bfloat16* __restrict__ Awqkv,
                                                  const __hip_bfloat16* __restrict__ Bx, const __hip_bfloat16* __restrict__ Bckv,
                                                  const __hip_bfloat16* __restrict__ Bwq, const __hip_bfloat16* __restrict__ Bwuk,
                                                  const __hip_bfloat16* __restrict__ Bwuv, const __hip_bfloat16* __restrict__ Awo,
                                                  const __hip_bfloat16* __restrict__ Bwo, char* __restrict__ ws) {
    extern __shared__ __attribute__((aligned(16))) char smem[];
    LAS char* lds = (LAS char*)smem;
    const int tid = threadIdx.x, lane = tid & 63, wid = __builtin_amdgcn_readfirstlane(tid >> 6);
    const int bid = blockIdx.x;
    const int m0 = ((bid >> 3) & 7) * 256;
    const int cc = (bid & 7) + 8 * ((bid >> 6) & 1);
    const bool kv = bid < 128;
    const bool has2 = kv || cc < 15;
    G1Src s1, s2;
    if (kv) {
        g1_src(s1, Bckv, Bwuk, 512, 4096, m0, cc * 256, 4096, wid, lane, false);
        g1_src(s2, Bckv, Bwuv, 512, 4096, m0, cc * 256, 4096, wid, lane, false);
    } else {
        g1_src(s1, Bx, Bwq, 640, 4096, m0, cc * 256, 4096, wid, lane, false);
        g1_src(s2, Ax, Awqkv, 640, 1920, m0, has2 ? cc * 128 : 0, 1920, wid, lane, true);
    }

    f32x4 acc[8][4];
    bf16x8 xf[2][4], wf[2][4];
    const unsigned lds0 = (unsigned)(uintptr_t)lds;
    if (bid == 255 && tid < 40) ((unsigned*)(ws + WS_CNT))[tid * 64] = 0u;
    STAMP(0, 0);
    set_m0(lds0 + wid * 8192 + 4096);
    sfor<0, 8>([&](auto ic) { g1_piece<decltype(ic)::value>(s1, 0); });
    set_m0(lds0 + 65536 + wid * 8192 + 4096);
    sfor<0, 8>([&](auto ic) { g1_piece<decltype(ic)::value>(s1, 1); });
    WAIT_VM(8);
    BARRIER();
    STAMP(0, 1);
    {
        set_m0(lds0 + 131072 + wid * 256);
        const unsigned gw = (unsigned)(bid * 8 + wid) * 64 + lane;
#define PF(ptr, nbytes) do { constexpr unsigned R_ = 131072u / ((nbytes) / 128u);                                     \
            const unsigned ln_ = gw / R_;                                                                                \
            const unsigned off_ = (gw % R_ == 0u && ln_ * 128u < (nbytes)) ? ln_ * 128u : 0u;                             \
            asm volatile("global_load_lds_dword %0, %1" :: "v"(off_), "s"((const char*)(ptr)) : "memory"); } while (0)
        PF(Bx, 2048u * 640u * 2u); PF(Bwq, 640u * 4096u * 2u); PF(Ax, 2048u * 640u * 2u); PF(Awqkv, 640u * 1920u * 2u);
        PF(Bwo, 4096u * 640u * 2u); PF(Awo, 640u * 640u * 2u);
        PF(Bckv, 2048u * 512u * 2u); PF(Bwuk, 512u * 4096u * 2u); PF(Bwuv, 512u * 4096u * 2u);
#undef PF
    }
    g1_loop<true, 4>(acc, xf, wf, s1, s2, has2, true, lds, wid, lane);
    if (kv) g1_epi<EP_BK>(acc, m0, cc * 256, 4096, 0, ws, wid, lane);
    else g1_epi<EP_BQ>(acc, m0, cc * 256, 4096, 0, ws, wid, lane);
    STAMP(0, 2);
    if (kv) {
        g1_loop<false, 4>(acc, xf, wf, s2, s2, false, false, lds, wid, lane);
        g1_epi<EP_BV>(acc, m0, cc * 256, 4096, 0, ws, wid, lane);
    } else if (cc < 10) {
        g1_loop<true, 2>(acc, xf, wf, s2, s2, false, false, lds, wid, lane);
        g1_epi<EP_AQK>(acc, m0, cc * 128, 1280, 0, ws, wid, lane);
    } else if (has2) {
        g1_loop<false, 2>(acc, xf, wf, s2, s2, false, false, lds, wid, lane);
        g1_epi<EP_AV>(acc, m0, cc * 128, 1920, 1280, ws, wid, lane);
    }
    STAMP(0, 3);
}

constexpr int SLOT = 40960;
constexpr int NSLOT = 4;
constexpr int K2_LDS = NSLOT * SLOT;
constexpr int NIT = 34;

__global__ __launch_bounds__(512, 2) void k2_attn(char* __restrict__ ws) {
    extern __shared__ __attribute__((aligned(16))) char smem[];
    LAS char* lds = (LAS char*)smem;
    const int tid = threadIdx.x, lane = tid & 63, wid = __builtin_amdgcn_readfirstlane(tid >> 6);
    const int r = lane & 31, h = lane >> 5;
    const int g = blockIdx.x, xcd = g & 7, idx = g >> 3;
    const int head = xcd * 4 + (idx >> 3), x = idx & 7;
    const char* kvb = ws + WS_KVB + (size_t)head * 1048576;
    const char* kva = ws + WS_KVA + (size_t)head * 262144;
    const int n1 = 32 - 2 * x;
    const unsigned lbase = (unsigned)(uintptr_t)lds + lane * 16;

    STAMP(1, 0);
    const unsigned lds0 = (unsigned)(uintptr_t)lds;
    const unsigned voff = lane * 16;
    const int wa = wid & 3;
    const char* sbB = kvb;
    const char* sbA = kva;
#define K2_M0(tt)                                                                           \
    do {                                                                                    \
        const int t3_ = (tt) < NIT ? (tt) : NIT - 1;                                        \
        const int tb3_ = t3_ < n1 ? t3_ : t3_ - n1;                                         \
        const int ta3_ = t3_ < 32 ? t3_ : 31;                                               \
        sbB = kvb + (size_t)tb3_ * 32768 + wid * 4096 + 2048;                               \
        sbA = kva + (size_t)ta3_ * 8192 + wid * 1024 - 2048;                                \
        set_m0(lds0 + ((tt) < NIT ? ((tt) & 3) : 2 + ((tt) & 1)) * SLOT + wid * 5120 + 2048); \
    } while (0)
#define K2_DMA(i) do { SBAR(); if constexpr ((i) < 4) dma_s<(i) * 1024 - 2048>(voff, sbB); else dma_s<2048>(voff, sbA); SBAR(); } while (0)
#define K2_STAGE_ALL(tt) do { K2_M0(tt); K2_DMA(0); K2_DMA(1); K2_DMA(2); K2_DMA(3); K2_DMA(4); } while (0)
#define BOFF(f) (((f) >> 2) * 5120 + ((f) & 3) * 1024)
#define AOFF(a) ((a) * 5120 + 4096)
    K2_STAGE_ALL(0);
    K2_STAGE_ALL(1);

#define K2_SYNC(it) do { WAIT_VM(5); BARRIER(); K2_M0((it) + 3); } while (0)

    if (wid < 4) {
        bf16x8 fr[8];
        K2_SYNC(-1);
        STAMP(1, 1);
        sfor<0, 8>([&](auto ic) { constexpr int j = decltype(ic)::value; lds_rd<BOFF((j & 1) * 8 + (j >> 1))>(fr[j], lbase); });
        K2_DMA(0); K2_DMA(1);
        int it = 0;
        for (int blk = 0; blk < 2; ++blk) {
            const int qb = blk == 0 ? 15 - x : x;
            const int nt = 2 * qb + 2;
            const int q0 = qb * 128 + wid * 32;
            const int tb32 = q0 >> 5;
            bf16x8 qf[8];
            const char* qp = ws + WS_QB + (size_t)((head * 64 + tb32) * 8) * 1024 + lane * 16;
#pragma unroll
            for (int ks = 0; ks < 8; ++ks) qf[ks] = *(const bf16x8*)(qp + ks * 1024);
            f32x16 o[4];
#pragma unroll
            for (int db = 0; db < 4; ++db) o[db] = f32x16{};
            f32x16 negm = f32x16{};
            float l = 0.f;
            for (int t = 0; t < nt; ++t, ++it) {
                const int rel = (q0 - 64 * t) >> 5;
                const unsigned ac = lbase + (it & 3) * SLOT;
                const unsigned an = lbase + ((it + 1) & 3) * SLOT;
                f32x16 s0, s1;
                SEG(0);
                sfor<0, 16>([&](auto ic) {
                    constexpr int j = decltype(ic)::value, ks = j >> 1;
                    lds_wait<7>(fr[j & 7]);
                    if constexpr (j == 0) s0 = MFMA32(fr[0], qf[0], negm);
                    else if constexpr (j == 1) s1 = MFMA32(fr[1], qf[0], negm);
                    else if constexpr ((j & 1) == 0) s0 = MFMA32(fr[j & 7], qf[ks], s0);
                    else s1 = MFMA32(fr[j & 7], qf[ks], s1);
                    constexpr int jn = j + 8;
                    SBAR();
                    if constexpr (jn < 16) lds_rd<BOFF((jn & 1) * 8 + (jn >> 1))>(fr[j & 7], ac);
                    else lds_rd<BOFF(jn)>(fr[j & 7], ac);
                    if constexpr (j == 2) K2_DMA(2);
                    if constexpr (j == 8) K2_DMA(3);
                    if constexpr (j == 14) K2_DMA(4);
                    SBAR();
                });
#define B_MASK()                                                                                        \
    do {                                                                                                \
        if (rel <= 1) {                                                                                 \
            const int lim = 32 * rel + r - 4 * h;                                                       \
            _Pragma("unroll") for (int i = 0; i < 16; ++i) {                                            \
                const int kr = (i & 3) + 8 * (i >> 2);                                                  \
                if (kr > lim) s0[i] = -__builtin_inff();                                                \
                if (kr + 32 > lim) s1[i] = -__builtin_inff();                                           \
            }                                                                                           \
        }                                                                                               \
    } while (0)
                SEG(1);
                B_MASK();
                float ps = 0.f;
                if (t > 0) {
#pragma unroll
                    for (int i = 0; i < 16; ++i) { s0[i] = __builtin_amdgcn_exp2f(s0[i]); ps += s0[i]; }
#pragma unroll
                    for (int i = 0; i < 16; ++i) { s1[i] = __builtin_amdgcn_exp2f(s1[i]); ps += s1[i]; }
                }
                if (t == 0 || __any(!(ps <= 65536.f))) {
                    if (t > 0) {
                        LAS char* sl = lds + (it & 3) * SLOT + lane * 16;
                        s0 = negm; s1 = negm;
#pragma unroll 1
                        for (int ks = 0; ks < 8; ++ks) {
                            bf16x8 qq = qf[0];
#pragma unroll
                            for (int u = 1; u < 8; ++u) qq = (ks == u) ? qf[u] : qq;
                            s0 = MFMA32(*(LAS bf16x8*)(sl + (ks >> 2) * 5120 + (ks & 3) * 1024), qq, s0);
                            s1 = MFMA32(*(LAS bf16x8*)(sl + 10240 + (ks >> 2) * 5120 + (ks & 3) * 1024), qq, s1);
                        }
                        B_MASK();
                    }
                    float mx = s0[0];
#pragma unroll
                    for (int i = 1; i < 16; ++i) mx = fmaxf(mx, s0[i]);
#pragma unroll
                    for (int i = 0; i < 16; ++i) mx = fmaxf(mx, s1[i]);
                    auto rr = __builtin_amdgcn_permlane32_swap(__float_as_uint(mx), __float_as_uint(mx), false, false);
                    mx = fmaxf(__uint_as_float(rr[0]), __uint_as_float(rr[1]));
                    float delta = mx;
                    if (t > 0) {
                        delta = fmaxf(mx, 0.f);
                        const float alpha = __builtin_amdgcn_exp2f(-delta);
                        l *= alpha;
#pragma unroll
                        for (int db = 0; db < 4; ++db)
#pragma unroll
                            for (int i = 0; i < 16; ++i) o[db][i] *= alpha;
                    }
                    const float nm = negm[0] - delta;
#pragma unroll
                    for (int i = 0; i < 16; ++i) negm[i] = nm;
                    ps = 0.f;
#pragma unroll
                    for (int i = 0; i < 16; ++i) { s0[i] = __builtin_amdgcn_exp2f(s0[i] - delta); ps += s0[i]; }
#pragma unroll
                    for (int i = 0; i < 16; ++i) { s1[i] = __builtin_amdgcn_exp2f(s1[i] - delta); ps += s1[i]; }
                }
                l += ps;
                bf16x8 pf[4];
                pf[0] = __builtin_bit_cast(bf16x8, pack8(s0, 0));
                pf[1] = __builtin_bit_cast(bf16x8, pack8(s0, 1));
                pf[2] = __builtin_bit_cast(bf16x8, pack8(s1, 0));
                pf[3] = __builtin_bit_cast(bf16x8, pack8(s1, 1));
                SEG(2);
                if (it + 1 < NIT) { K2_SYNC(it); } else { K2_M0(it + 3); }
                SEG(3);
                sfor<0, 16>([&](auto ic) {
                    constexpr int j = decltype(ic)::value, s4 = j >> 2, db = j & 3;
                    lds_wait<7>(fr[j & 7]);
                    o[db] = MFMA32(fr[j & 7], pf[s4], o[db]);
                    SBAR();
                    if constexpr (j < 8) lds_rd<BOFF(24 + j)>(fr[j & 7], ac);
                    else { constexpr int jn = j - 8; lds_rd<BOFF((jn & 1) * 8 + (jn >> 1))>(fr[j & 7], an); }
                    if constexpr (j == 4) K2_DMA(0);
                    if constexpr (j == 10) K2_DMA(1);
                    SBAR();
                });
                SEG(4);
            }
            auto rr = __builtin_amdgcn_permlane32_swap(__float_as_uint(l), __float_as_uint(l), false, false);
            const float inv = 1.0f / (__uint_as_float(rr[0]) + __uint_as_float(rr[1]));
            char* cp = ws + WS_CTXB + (size_t)(tb32 * 256 + head * 8) * 1024 + lane * 16;
#pragma unroll
            for (int db = 0; db < 4; ++db) {
                st16(cp + (2 * db) * 1024, pack8s(o[db], 0, inv));
                st16(cp + (2 * db + 1) * 1024, pack8s(o[db], 1, inv));
            }
        }
        WAIT_VM(0);
        STAMP(1, 2);
#undef B_MASK
    } else {
        const int tb32 = x * 8 + wa * 2;
        bf16x8 qf[2][2];
        const char* qp = ws + WS_QA + (size_t)((head * 64 + tb32) * 2) * 1024 + lane * 16;
#pragma unroll
        for (int qt = 0; qt < 2; ++qt)
#pragma unroll
            for (int ks = 0; ks < 2; ++ks) qf[qt][ks] = *(const bf16x8*)(qp + (qt * 2 + ks) * 1024);
        f32x16 o[2];
        o[0] = f32x16{}; o[1] = f32x16{};
        float negm[2] = {0.f, 0.f};
        bf16x8 kf[4], vf[4];
        f32x16 s[2][2];
        K2_SYNC(-1);
        lds_rd<AOFF(0)>(kf[0], lbase); lds_rd<AOFF(1)>(kf[1], lbase); lds_rd<AOFF(2)>(kf[2], lbase); lds_rd<AOFF(3)>(kf[3], lbase);
        for (int it = 0; it <= 32; ++it) {
            SEG(0);
            if (it > 0) {
                u32x4 pf[2][2][2];
                unsigned chk = 0;
                sfor<0, 4>([&](auto bc) {
                    constexpr int blk = decltype(bc)::value, kb = blk >> 1, qt = blk & 1;
                    f32x16 e;
#pragma unroll
                    for (int i = 0; i < 8; ++i) e[i] = __builtin_amdgcn_exp2f(s[kb][qt][i]);
                    K2_DMA(blk);
#pragma unroll
                    for (int i = 8; i < 16; ++i) e[i] = __builtin_amdgcn_exp2f(s[kb][qt][i]);
                    if constexpr (blk == 3) K2_DMA(4);
                    pf[kb][qt][0] = pack8(e, 0);
                    pf[kb][qt][1] = pack8(e, 1);
                    chk |= pf[kb][qt][0][0] | pf[kb][qt][0][1] | pf[kb][qt][0][2] | pf[kb][qt][0][3];
                    chk |= pf[kb][qt][1][0] | pf[kb][qt][1][1] | pf[kb][qt][1][2] | pf[kb][qt][1][3];
                });
                const bool first = (it == 1);
                if (first || __any((chk & 0x40004000u) != 0u)) {
#pragma unroll
                    for (int qt = 0; qt < 2; ++qt) {
                        float mx = s[0][qt][0];
#pragma unroll
                        for (int i = 1; i < 16; ++i) mx = fmaxf(mx, s[0][qt][i]);
#pragma unroll
                        for (int i = 0; i < 16; ++i) mx = fmaxf(mx, s[1][qt][i]);
                        auto rr = __builtin_amdgcn_permlane32_swap(__float_as_uint(mx), __float_as_uint(mx), false, false);
                        mx = fmaxf(__uint_as_float(rr[0]), __uint_as_float(rr[1]));
                        float delta = mx + 10.f;
                        if (!first) delta = fmaxf(delta, 0.f);
                        const float tgt = negm[qt] - delta;
                        const unsigned hi = pack2(tgt, 0.f) & 0xFFFFu;
                        const float hif = __uint_as_float(hi << 16);
                        const unsigned lo = pack2(tgt - hif, 0.f) & 0xFFFFu;
                        const float neff = hif + __uint_as_float(lo << 16);
                        delta = negm[qt] - neff;
                        negm[qt] = neff;
                        {
                            u32x4 qq = __builtin_bit_cast(u32x4, qf[qt][1]);
                            qq[2] = h ? 0u : (hi | (lo << 16));
                            qf[qt][1] = __builtin_bit_cast(bf16x8, qq);
                        }
                        if (!first) {
                            const float alpha = __builtin_amdgcn_exp2f(-delta);
#pragma unroll
                            for (int i = 0; i < 16; ++i) o[qt][i] *= alpha;
                        }
#pragma unroll
                        for (int kb = 0; kb < 2; ++kb) {
                            f32x16 e;
#pragma unroll
                            for (int i = 0; i < 16; ++i) e[i] = __builtin_amdgcn_exp2f(s[kb][qt][i] - delta);
                            pf[kb][qt][0] = pack8(e, 0);
                            pf[kb][qt][1] = pack8(e, 1);
                        }
                    }
                }
                SEG(1);
                lds_wait2<4>(vf[0], vf[1]);
                lds_wait2<4>(vf[2], vf[3]);
#pragma unroll
                for (int kb = 0; kb < 2; ++kb)
#pragma unroll
                    for (int s2 = 0; s2 < 2; ++s2)
#pragma unroll
                        for (int qt = 0; qt < 2; ++qt)
                            o[qt] = MFMA32(vf[kb * 2 + s2], __builtin_bit_cast(bf16x8, pf[kb][qt][s2]), o[qt]);
            } else {
                K2_DMA(0); K2_DMA(1); K2_DMA(2); K2_DMA(3); K2_DMA(4);
            }
            SEG(2);
            lds_wait2<0>(kf[0], kf[1]);
            lds_wait2<0>(kf[2], kf[3]);
#pragma unroll
            for (int kb = 0; kb < 2; ++kb)
#pragma unroll
                for (int qt = 0; qt < 2; ++qt) {
                    s[kb][qt] = MFMA32(kf[kb * 2], qf[qt][0], f32x16{});
                    s[kb][qt] = MFMA32(kf[kb * 2 + 1], qf[qt][1], s[kb][qt]);
                }
            SBAR();
            if (it == 32) STAMPW(1, 3, 4);
            SEG(3);
            K2_SYNC(it);
            SEG(4);
            const unsigned ac = lbase + (it & 3) * SLOT;
            const unsigned an = lbase + ((it + 1) & 3) * SLOT;
            lds_rd<AOFF(4)>(vf[0], ac); lds_rd<AOFF(5)>(vf[1], ac); lds_rd<AOFF(6)>(vf[2], ac); lds_rd<AOFF(7)>(vf[3], ac);
            lds_rd<AOFF(0)>(kf[0], an); lds_rd<AOFF(1)>(kf[1], an); lds_rd<AOFF(2)>(kf[2], an); lds_rd<AOFF(3)>(kf[3], an);
        }
        WAIT_VM(0);
#pragma unroll
        for (int qt = 0; qt < 2; ++qt) {
            const unsigned lv = __float_as_uint(o[qt][8]);
            auto rr = __builtin_amdgcn_permlane32_swap(lv, lv, false, false);
            const float lsum = __uint_as_float(rr[1]);
            const float inv = 1.0f / lsum;
            char* cp = ws + WS_CTXA + (size_t)((tb32 + qt) * 40) * 1024;
#pragma unroll
            for (int gq = 0; gq < 3; ++gq) {
                const int d0 = 8 * gq + 4 * h;
                if (d0 < 20) {
                    const int k = head * 20 + d0;
                    u32x2 w;
                    w[0] = pack2(o[qt][4 * gq + 0] * inv, o[qt][4 * gq + 1] * inv);
                    w[1] = pack2(o[qt][4 * gq + 2] * inv, o[qt][4 * gq + 3] * inv);
                    *(u32x2*)(cp + (k >> 4) * 1024 + (r + 32 * ((k >> 3) & 1)) * 16 + (k & 4) * 2) = w;
                }
            }
        }
    }
}

constexpr int G3_STAGE = 49152;
constexpr int G3_LDS = 3 * G3_STAGE;

__global__ __launch_bounds__(512, 2) void k3_oproj(const __hip_bfloat16* __restrict__ Awo, const __hip_bfloat16* __restrict__ Bwo,
                                                   char* __restrict__ ws, __hip_bfloat16* __restrict__ out) {
    extern __shared__ __attribute__((aligned(16))) char smem[];
    LAS char* lds = (LAS char*)smem;
    const int tid = threadIdx.x, lane = tid & 63, wid = __builtin_amdgcn_readfirstlane(tid >> 6);
    const int wm = wid >> 1, wn = wid & 1, h = lane >> 5;
    const int bid = blockIdx.x;
    const bool isB = bid < 200;
    int mt, nt, sp, kt0, nkt, kstot;
    const char* Xc; const char* Wb;
    if (isB) {
        mt = bid & 7; const int rest = bid >> 3; nt = rest % 5; sp = rest / 5;
        kt0 = sp * 13; nkt = sp == 4 ? 12 : 13; kstot = 256;
        Xc = ws + WS_CTXB; Wb = (const char*)Bwo;
    } else {
        const int b2 = bid - 200; mt = b2 & 7; nt = b2 >> 3; sp = 5;
        kt0 = 0; nkt = 10; kstot = 40;
        Xc = ws + WS_CTXA; Wb = (const char*)Awo;
    }
    unsigned woff[2];
#pragma unroll
    for (int i = 0; i < 2; ++i) {
        const int k = (wid * 2 + i) * 4 + (lane >> 4);
        const int p = lane & 15, b = (p >> 2) ^ (k & 3);
        woff[i] = (unsigned)(((kt0 * 64 + k) * 640 + nt * 128 + (b * 4 + (p & 3)) * 8) * 2);
    }
    const unsigned lds0 = (unsigned)(uintptr_t)lds;
    const char* xsb = Xc + (size_t)((mt * 8 + wid) * kstot + kt0 * 4) * 1024 + 4096;
    const unsigned voff = lane * 16;
    woff[1] -= 1024;
    auto piece = [&](int kt, auto ic) {
        constexpr int i = decltype(ic)::value;
        if constexpr (i < 4) dma_s<i * 1024 - 4096>(voff, xsb + (size_t)kt * 4096);
        else dma_s<(i - 4) * 1024>(woff[i - 4], Wb + (size_t)kt * (64u * 640u * 2u));
    };
    const int q = (lane >> 2) & 3, p4 = lane & 3, g16 = (lane >> 4) & 1;
    const int r0 = isB ? 4 * h : 8 * h, r1 = isB ? 8 + 4 * h : 8 * h + 4;
    int wb0[2], wb1[2];
#pragma unroll
    for (int ni = 0; ni < 2; ++ni) {
        const int b = wn * 2 + ni;
        const int c = ((b ^ q) * 64) + g16 * 32 + p4 * 8;
        wb0[ni] = (r0 >> 3) * 6144 + 4096 + ((r0 & 7) + q) * 256 + c;
        wb1[ni] = (r1 >> 3) * 6144 + 4096 + ((r1 & 7) + q) * 256 + c;
    }
    const int xb0 = wm * 2 * 6144 + lane * 16;
    f32x16 acc[2][2];
#pragma unroll
    for (int mi = 0; mi < 2; ++mi)
#pragma unroll
        for (int ni = 0; ni < 2; ++ni) acc[mi][ni] = f32x16{};

    bf16x8 xf[2][4][2], wf[2][4][2];
#define G3_RD(set, bb)                                                                                           \
    do {                                                                                                         \
        _Pragma("unroll") for (int ks = 0; ks < 4; ++ks) {                                                       \
            _Pragma("unroll") for (int ni = 0; ni < 2; ++ni)                                                     \
                wf[set][ks][ni] = tr_frag((bb) + wb0[ni] + ks * 12288, (bb) + wb1[ni] + ks * 12288);             \
            _Pragma("unroll") for (int mi = 0; mi < 2; ++mi)                                                     \
                xf[set][ks][mi] = *(LAS bf16x8*)((bb) + xb0 + mi * 6144 + ks * 1024);                            \
        }                                                                                                        \
    } while (0)
#define G3_STEP(set, kt)                                                                                         \
    do {                                                                                                         \
        if ((kt) + 2 < nkt) { WAIT_VM(6); } else { WAIT_VM(0); }                                                 \
        LGKM0();                                                                                                 \
        BARRIER();                                                                                               \
        const int b0_ = (kt) % 3, b1_ = ((kt) + 1) % 3;                                                          \
        set_m0(lds0 + b0_ * G3_STAGE + wid * 6144 + 4096);                                                       \
        if ((kt) + 1 < nkt) G3_RD((set) ^ 1, lds + b1_ * G3_STAGE);                                              \
        SBAR();                                                                                                  \
        const bool on_ = (kt) + 3 < nkt;                                                                         \
        sfor<0, 8>([&](auto jc) {                                                                                \
            constexpr int j = decltype(jc)::value, ks = j >> 1, mi = j & 1;                                      \
            _Pragma("unroll") for (int ni = 0; ni < 2; ++ni)                                                     \
                acc[mi][ni] = MFMA32(wf[set][ks][ni], xf[set][ks][mi], acc[mi][ni]);                             \
            if constexpr (j < 6) { SBAR(); if (on_) piece((kt) + 3, jc); SBAR(); }                               \
        });                                                                                                      \
        SBAR();                                                                                                  \
    } while (0)
    STAMP(2, 0);
#pragma unroll
    for (int t = 0; t < 3; ++t) {
        set_m0(lds0 + t * G3_STAGE + wid * 6144 + 4096);
        sfor<0, 6>([&](auto ic) { piece(t, ic); });
    }
    WAIT_VM(12);
    BARRIER();
    STAMP(2, 1);
    G3_RD(0, lds);
    int kt = 0;
#pragma unroll 1
    for (; kt + 1 < nkt; kt += 2) { G3_STEP(0, kt); G3_STEP(1, kt + 1); }
    if (kt < nkt) G3_STEP(0, kt);
#undef G3_RD
#undef G3_STEP
    STAMP(2, 2);
    const int r = lane & 31;
    if (!isB) {
#pragma unroll
        for (int mi = 0; mi < 2; ++mi)
#pragma unroll
            for (int ni = 0; ni < 2; ++ni) {
                char* op = (char*)out + (size_t)((mt * 8 + wm * 2 + mi) * 32 + r) * 2560 + ((nt * 4 + wn * 2 + ni) * 32 + 4 * h) * 2;
#pragma unroll
                for (int gq = 0; gq < 4; ++gq) {
                    u32x2 o;
                    o[0] = pack2(acc[mi][ni][4 * gq], acc[mi][ni][4 * gq + 1]);
                    o[1] = pack2(acc[mi][ni][4 * gq + 2], acc[mi][ni][4 * gq + 3]);
                    *(u32x2*)(op + gq * 16) = o;
                }
            }
        STAMP(2, 3);
        return;
    }
#pragma unroll
    for (int mi = 0; mi < 2; ++mi)
#pragma unroll
        for (int ni = 0; ni < 2; ++ni) {
            const int mt32 = mt * 8 + wm * 2 + mi, nt32 = nt * 4 + wn * 2 + ni;
            char* dst = ws + WS_PART + (size_t)sp * SLAB + (size_t)(mt32 * 20 + nt32) * 4096 + lane * 16;
#pragma unroll
            for (int gq = 0; gq < 4; ++gq) {
                f32x4 v = {acc[mi][ni][4 * gq], acc[mi][ni][4 * gq + 1], acc[mi][ni][4 * gq + 2], acc[mi][ni][4 * gq + 3]};
                asm volatile("global_store_dwordx4 %0, %1, off sc1\n\ts_nop 1" :: "v"(dst + gq * 1024), "v"(v) : "memory");
            }
        }
    asm volatile("s_waitcnt vmcnt(0)" ::: "memory");
    BARRIER();
    unsigned* cnt = (unsigned*)(ws + WS_CNT) + (mt * 5 + nt) * 64;
    if (wid == 0) {
        if (lane == 0) __hip_atomic_fetch_add(cnt, 1u, __ATOMIC_RELAXED, __HIP_MEMORY_SCOPE_AGENT);
        for (unsigned spins = 0; spins < (1u << 24); ++spins) {
            const unsigned c = __builtin_amdgcn_readfirstlane(__hip_atomic_load(cnt, __ATOMIC_RELAXED, __HIP_MEMORY_SCOPE_AGENT));
            if (c >= (unsigned)NSPLIT) break;
            __builtin_amdgcn_s_sleep(1);
        }
        __builtin_amdgcn_fence(__ATOMIC_ACQUIRE, "agent");
        asm volatile("s_waitcnt vmcnt(0)" ::: "memory");
    }
    BARRIER();
    const int st = sp + 5 * wid;
    if (st < 32) {
        const int mt32 = mt * 8 + (st >> 2), nt32 = nt * 4 + (st & 3);
        const char* src = ws + WS_PART + (size_t)(mt32 * 20 + nt32) * 4096 + lane * 16;
        f32x4 v[4][NSPLIT];
#pragma unroll
        for (int gq = 0; gq < 4; ++gq)
#pragma unroll
            for (int s = 0; s < NSPLIT; ++s) v[gq][s] = *(const f32x4*)(src + (size_t)s * SLAB + gq * 1024);
        char* op = (char*)out + (size_t)(mt32 * 32 + r) * 2560 + (640 + nt32 * 32 + 4 * h) * 2;
#pragma unroll
        for (int gq = 0; gq < 4; ++gq) {
            const f32x4 t = ((v[gq][0] + v[gq][1]) + (v[gq][2] + v[gq][3])) + v[gq][4];
            u32x2 o;
            o[0] = pack2(t[0], t[1]);
            o[1] = pack2(t[2], t[3]);
            *(u32x2*)(op + gq * 16) = o;
        }
    }
    STAMP(2, 3);
}

extern "C" void kernel_launch(void* const* d_in, const int* in_sizes, int n_in,
                              void* d_out, int out_size, void* d_ws, size_t ws_size,
                              hipStream_t stream) {
    (void)in_sizes; (void)n_in; (void)out_size; (void)ws_size;
    const __hip_bfloat16* Ax = (const __hip_bfloat16*)d_in[0];
    const __hip_bfloat16* Awqkv = (const __hip_bfloat16*)d_in[1];
    const __hip_bfloat16* Awo = (const __hip_bfloat16*)d_in[2];
    const __hip_bfloat16* Bx = (const __hip_bfloat16*)d_in[3];
    const __hip_bfloat16* Bckv = (const __hip_bfloat16*)d_in[4];
    const __hip_bfloat16* Bwq = (const __hip_bfloat16*)d_in[5];
    const __hip_bfloat16* Bwuk = (const __hip_bfloat16*)d_in[6];
    const __hip_bfloat16* Bwuv = (const __hip_bfloat16*)d_in[7];
    const __hip_bfloat16* Bwo = (const __hip_bfloat16*)d_in[8];
    char* ws = (char*)d_ws;
    (void)hipFuncSetAttribute((const void*)k1_proj, hipFuncAttributeMaxDynamicSharedMemorySize, G1_LDS);
    (void)hipFuncSetAttribute((const void*)k2_attn, hipFuncAttributeMaxDynamicSharedMemorySize, K2_LDS);
    (void)hipFuncSetAttribute((const void*)k3_oproj, hipFuncAttributeMaxDynamicSharedMemorySize, G3_LDS);
    k1_proj<<<256, 512, G1_LDS, stream>>>(Ax, Awqkv, Bx, Bckv, Bwq, Bwuk, Bwuv, Awo, Bwo, ws);
    k2_attn<<<256, 512, K2_LDS, stream>>>(ws);
    k3_oproj<<<240, 512, G3_LDS, stream>>>(Awo, Bwo, ws, (__hip_bfloat16*)d_out);
}
```

```cpp
#include <hip/hip_runtime.h>
#include <hip/hip_bf16.h>
#include <stdint.h>
#include <type_traits>

typedef short s16x4 __attribute__((ext_vector_type(4)));
typedef short bf16x8 __attribute__((ext_vector_type(8)));
typedef float f32x16 __attribute__((ext_vector_type(16)));
typedef float f32x4 __attribute__((ext_vector_type(4)));
typedef float f32x2 __attribute__((ext_vector_type(2)));
typedef unsigned u32x4 __attribute__((ext_vector_type(4)));
typedef unsigned u32x2 __attribute__((ext_vector_type(2)));
typedef __bf16 bf16x2_t __attribute__((ext_vector_type(2)));
#define LAS __attribute__((address_space(3)))
#define DI __device__ __forceinline__
#define MFMA32(a, b, c) __builtin_amdgcn_mfma_f32_32x32x16_bf16((a), (b), (c), 0, 0, 0)
#define MFMA16(a, b, c) __builtin_amdgcn_mfma_f32_16x16x32_bf16((a), (b), (c), 0, 0, 0)
#define CFENCE() asm volatile("" ::: "memory")
#define BARRIER() do { CFENCE(); __builtin_amdgcn_s_barrier(); CFENCE(); } while (0)
#define WAIT_VM(n) asm volatile("s_waitcnt vmcnt(" #n ") lgkmcnt(0)" ::: "memory")
#define SBAR() __builtin_amdgcn_sched_barrier(0)

DI unsigned pack2(float a, float b) {
    f32x2 v = {a, b};
    bf16x2_t r = __builtin_convertvector(v, bf16x2_t);
    return __builtin_bit_cast(unsigned, r);
}
DI u32x4 pack8(const f32x16& x, int s) {
    u32x4 w;
    w[0] = pack2(x[8 * s + 0], x[8 * s + 1]);
    w[1] = pack2(x[8 * s + 2], x[8 * s + 3]);
    w[2] = pack2(x[8 * s + 4], x[8 * s + 5]);
    w[3] = pack2(x[8 * s + 6], x[8 * s + 7]);
    return w;
}
DI u32x4 pack8s(const f32x16& x, int s, float c) {
    u32x4 w;
    w[0] = pack2(x[8 * s + 0] * c, x[8 * s + 1] * c);
    w[1] = pack2(x[8 * s + 2] * c, x[8 * s + 3] * c);
    w[2] = pack2(x[8 * s + 4] * c, x[8 * s + 5] * c);
    w[3] = pack2(x[8 * s + 6] * c, x[8 * s + 7] * c);
    return w;
}
DI void glds16(const void* g, LAS char* l) {
    __builtin_amdgcn_global_load_lds((const unsigned*)g, (LAS unsigned*)l, 16, 0, 0);
}
DI bf16x8 tr_frag(LAS char* p0, LAS char* p1) {
    s16x4 lo = __builtin_amdgcn_ds_read_tr16_b64_v4i16((LAS s16x4*)p0);
    s16x4 hi = __builtin_amdgcn_ds_read_tr16_b64_v4i16((LAS s16x4*)p1);
    return __builtin_shufflevector(lo, hi, 0, 1, 2, 3, 4, 5, 6, 7);
}

#define STMODE 2
DI void st16(void* p, u32x4 v) {
    asm volatile("global_store_dwordx4 %0, %1, off sc1\n\ts_nop 1" :: "v"(p), "v"(v) : "memory");
}
template <int I, int N, class F> DI void sfor(F&& f) {
    if constexpr (I < N) { f(std::integral_constant<int, I>{}); sfor<I + 1, N>(f); }
}
template <int OFF> DI void lds_rd(bf16x8& d, unsigned addr) {
    asm volatile("ds_read_b128 %0, %1 offset:%2" : "=v"(d) : "v"(addr), "i"(OFF));
}
template <int N> DI void lds_wait(bf16x8& d) { asm volatile("s_waitcnt lgkmcnt(%1)" : "+v"(d) : "i"(N)); }
template <int N> DI void lds_wait2(bf16x8& d, bf16x8& e) { asm volatile("s_waitcnt lgkmcnt(%2)" : "+v"(d), "+v"(e) : "i"(N)); }
#define LGKM0() __builtin_amdgcn_s_waitcnt(0xC07F)
DI void set_m0(unsigned v) { asm volatile("s_mov_b32 m0, %0\n\ts_nop 0" :: "s"(v) : "memory"); }
template <int OFF> DI void dma_s(unsigned voff, const char* sbase) {
    asm volatile("global_load_lds_dwordx4 %0, %1 offset:%2" :: "v"(voff), "s"(sbase), "i"(OFF) : "memory");
}

#define DYNPRIO 0
#define PRIO(x) do { if (DYNPRIO) __builtin_amdgcn_s_setprio(x); } while (0)
#define ABL_NOPIECE 0
#define ABL_NOBAR 0
#define ABL_T0 0
#define ABL_K2 0
#define ABL_NODMA 0
#define ABL_NOMFMA 0
#define SEG(i) do { } while (0)
#define STAMP(kid, idx) do { } while (0)
#define STAMPW(kid, idx, w) do { } while (0)
constexpr size_t WS_KVB = 0;
constexpr size_t WS_QB = WS_KVB + (size_t)32 * 1048576;
constexpr size_t WS_KVA = WS_QB + (size_t)16 * 1048576;
constexpr size_t WS_QA = WS_KVA + (size_t)8 * 1048576;
constexpr size_t WS_CTXB = WS_QA + (size_t)4 * 1048576;
constexpr size_t WS_CTXA = WS_CTXB + (size_t)16 * 1048576;
constexpr size_t WS_PART = WS_CTXA + (size_t)4 * 1048576;
constexpr size_t WS_CNT = WS_PART + (size_t)32 * 1048576;
constexpr int NSPLIT = 5;
constexpr size_t SLAB = (size_t)64 * 20 * 4096;

constexpr float CB = 0.08838834764831845f * 1.4426950408889634f;
constexpr float CA = 0.22360679774997896f * 1.4426950408889634f;

#define PREFETCH 1
constexpr int G1_LDS = 2 * 65536 + 2048;
enum { EP_BK = 0, EP_BV = 1, EP_BQ = 2, EP_AQK = 3, EP_AV = 4 };

struct G1Src { unsigned xoff[4], woff[4]; const char* Xb; const char* Wb; unsigned wstep; int nkt; bool half; };
DI void g1_src(G1Src& s, const __hip_bfloat16* X, const __hip_bfloat16* W, int K, int ldw, int m0, int n0, int nlim, int wid, int lane, bool half) {
    s.half = half;
#pragma unroll
    for (int i = 0; i < 4; ++i) {
        const int row = (wid * 4 + i) * 8 + (lane >> 3);
        const int c = (lane & 7) ^ ((row >> 1) & 7);
        s.xoff[i] = (unsigned)(((m0 + row) * K + c * 8) * 2) + 4096 - i * 1024;
        const int k = (wid * 4 + i) * 2 + (lane >> 5);
        const int p = lane & 31, b = (p >> 2) ^ (k & 3);
        int col = n0 + (b * 4 + ((p & 3) ^ ((wid & 1) << 1))) * 8;
        col = col < nlim ? col : nlim - 8;
        s.woff[i] = (unsigned)((k * ldw + col) * 2) - i * 1024;
    }
    s.Xb = (const char*)X; s.Wb = (const char*)W; s.wstep = (unsigned)(64 * ldw * 2); s.nkt = K >> 6;
}
template <int I> DI void g1_piece(const G1Src& s, int kt) {
    if constexpr (I < 4) dma_s<I * 1024 - 4096>(s.xoff[I], s.Xb + (size_t)kt * 128);
    else if (!s.half || (threadIdx.x & 16) == 0) dma_s<(I - 4) * 1024>(s.woff[I - 4], s.Wb + (size_t)kt * s.wstep);
}

template <bool TR, int NT>
DI void g1_loop(f32x4 (&acc)[8][4], bf16x8 (&xs)[2][4], bf16x8 (&wsb)[2][4], const G1Src& cur, const G1Src& nxt, bool has_next, bool first,
                LAS char* lds, int wid, int lane) {
    const int wm = wid >> 2, wn = wid & 3, r16 = lane & 15, g = lane >> 4;
    const unsigned lds0 = (unsigned)(uintptr_t)lds;
    const int nkt = cur.nkt;
    int xb[2];
#pragma unroll
    for (int st = 0; st < 2; ++st) xb[st] = wm * 4 * 8192 + r16 * 128 + (((4 * st + g) ^ (r16 >> 1)) * 16);
    const int q = (lane >> 2) & 3, p4 = lane & 3;
    int wb[4];
#pragma unroll
    for (int nt = 0; nt < 4; ++nt) {
        const int b = NT == 4 ? wn * 2 + (nt >> 1) : wn;
        wb[nt] = g * 8192 + 4096 + q * 512 + ((b ^ q) * 64) + (((nt & 1) ^ (g & 1)) * 32) + p4 * 8;
    }
#pragma unroll
    for (int mt = 0; mt < 8; ++mt)
#pragma unroll
        for (int nt = 0; nt < 4; ++nt) acc[mt][nt] = f32x4{};
#define G1_RX(set, bb, st, mh)                                                                                   \
    _Pragma("unroll") for (int m4 = 0; m4 < 4; ++m4)                                                             \
        xs[set][m4] = *(LAS bf16x8*)((bb) + xb[st] + ((mh) * 2 + (m4 >> 1)) * 8192 + (m4 & 1) * 2048)
#define G1_RW(bb, st)                                                                                            \
    _Pragma("unroll") for (int nt = 0; nt < NTR; ++nt)                                                           \
        wsb[st][nt] = tr_frag((bb) + wb[nt] + (st) * 32768, (bb) + wb[nt] + (st) * 32768 + 2048)
#define G1_MM(set, st, mh, on, kn, p0, pe)                                                                       \
    sfor<0, 4>([&](auto mc) {                                                                                    \
        constexpr int m4 = decltype(mc)::value;                                                                  \
        _Pragma("unroll") for (int nt = 0; nt < NT; ++nt)                                                        \
            acc[(mh) * 4 + m4][nt] = TR ? MFMA16(wsb[st][nt], xs[set][m4], acc[(mh) * 4 + m4][nt])               \
                                        : MFMA16(xs[set][m4], wsb[st][nt], acc[(mh) * 4 + m4][nt]);              \
        if constexpr ((p0) + m4 < (pe)) {                                                                        \
            SBAR();                                                                                              \
            if (on) { if ((kn) < nkt) g1_piece<(p0) + m4>(cur, (kn)); else g1_piece<(p0) + m4>(nxt, (kn) - nkt); } \
            SBAR();                                                                                              \
        }                                                                                                        \
    })
    constexpr int NTR = NT;
    G1_RW(lds, 0);
    if (first) { G1_RX(0, lds, 0, 0); }
#pragma unroll 1
    for (int kt = 0; kt < nkt; ++kt) {
        LAS char* bb = lds + (kt & 1) * 65536;
        LAS char* bn = lds + ((kt + 1) & 1) * 65536;
        const bool on1 = !(first && kt == 0) && (kt + 1 < nkt || has_next);
        const bool on2 = kt + 2 < nkt || has_next;
        LGKM0(); G1_RX(1, bb, 0, 1); SBAR(); G1_MM(0, 0, 0, on1, kt + 1, 4, 8); SBAR();
        LGKM0(); G1_RW(bb, 1); G1_RX(0, bb, 1, 0); SBAR(); G1_MM(1, 0, 1, false, 0, 0, 0); SBAR();
        LGKM0(); G1_RX(1, bb, 1, 1); SBAR(); G1_MM(0, 1, 0, false, 0, 0, 0); SBAR();
        LGKM0(); WAIT_VM(0);
        BARRIER();
        set_m0(lds0 + (kt & 1) * 65536 + wid * 8192 + 4096);
        if (kt + 1 < nkt) G1_RW(bn, 0);
        if (kt + 1 < nkt || has_next) G1_RX(0, bn, 0, 0);
        SBAR(); G1_MM(1, 1, 1, on2, kt + 2, 0, 4); SBAR();
    }
#undef G1_RX
#undef G1_RW
#undef G1_MM
}

DI void st8(void* p, u32x2 v) {
    asm volatile("global_store_dwordx2 %0, %1, off sc1\n\ts_nop 1" :: "v"(p), "v"(v) : "memory");
}

template <int EP>
DI void g1_epi(const f32x4 (&acc)[8][4], int m0, int n0, int nlim, int nsec, char* __restrict__ ws, int wid, int lane) {
    const int wm = wid >> 2, wn = wid & 3, c = lane & 15, g = lane >> 4;
    if constexpr (EP == EP_BK || EP == EP_BQ || EP == EP_BV) {
#pragma unroll
        for (int mt = 0; mt < 8; ++mt) {
            const int tb = (m0 + wm * 128 + mt * 16) >> 5;
            const int tile = tb >> 1, kb = tb & 1, th = mt & 1;
#pragma unroll
            for (int np = 0; np < 2; ++np) {
                const int nb = n0 + wn * 64 + np * 32;
                const int head = nb >> 7;
                const float sc = EP == EP_BQ ? CB : 1.0f;
                const f32x4 a = acc[mt][2 * np], b = acc[mt][2 * np + 1];
                unsigned a0 = pack2(a[0] * sc, a[1] * sc), a1 = pack2(a[2] * sc, a[3] * sc);
                unsigned b0 = pack2(b[0] * sc, b[1] * sc), b1 = pack2(b[2] * sc, b[3] * sc);
                if constexpr (EP == EP_BV) {
                    auto r0 = __builtin_amdgcn_permlane32_swap(a0, b0, false, false);
                    auto r1 = __builtin_amdgcn_permlane32_swap(a1, b1, false, false);
                    const u32x4 w = {r0[0], r1[0], r0[1], r1[1]};
                    const int db = (nb >> 5) & 3;
                    char* base = ws + WS_KVB + (size_t)head * 1048576 + tile * 32768 + 16384 + ((kb * 2 + th) * 4 + db) * 1024;
                    st16(base + ((g >> 1) * 16 + c + 32 * (g & 1)) * 16, w);
                } else {
                    auto r0 = __builtin_amdgcn_permlane16_swap(a0, b0, false, false);
                    auto r1 = __builtin_amdgcn_permlane16_swap(a1, b1, false, false);
                    const u32x4 w = {r0[0], r1[0], r0[1], r1[1]};
                    const int ks = ((nb >> 4) & 7) + (g & 1);
                    char* base = EP == EP_BK ? ws + WS_KVB + (size_t)head * 1048576 + tile * 32768 + (kb * 8 + ks) * 1024
                                             : ws + WS_QB + (size_t)((head * 64 + tb) * 8 + ks) * 1024;
                    st16(base + (th * 16 + c + 32 * (g >> 1)) * 16, w);
                }
            }
        }
        return;
    }
#pragma unroll
    for (int mt = 0; mt < 8; ++mt) {
        const int tb = (m0 + wm * 128 + mt * 16) >> 5;
        const int tile = tb >> 1, kb = tb & 1, th = mt & 1;
#pragma unroll
        for (int nt = 0; nt < 2; ++nt) {
            const int nb = n0 + wn * 32 + nt * 16;
            if (nb >= nlim) continue;
            const f32x4 a = acc[mt][nt];
            if constexpr (EP == EP_BK || EP == EP_BQ || EP == EP_BV) {
            } else if constexpr (EP == EP_AQK) {
                const bool isq = nb < 640;
                const float sc = isq ? CA : 1.0f;
                const int r = th * 16 + c;
                const int n = nb - (isq ? 0 : 640) + 4 * g;
                const int head = n / 20, d0 = n - head * 20;
                char* base = isq ? ws + WS_QA + (size_t)((head * 64 + tb) * 2) * 1024
                                 : ws + WS_KVA + (size_t)head * 262144 + tile * 8192 + kb * 2048;
                char* dst = base + (d0 >> 4) * 1024 + (r + 32 * ((d0 >> 3) & 1)) * 16 + (d0 & 4) * 2;
                u32x2 w;
                w[0] = pack2(a[0] * sc, a[1] * sc);
                w[1] = pack2(a[2] * sc, a[3] * sc);
                *(u32x2*)dst = w;
                if (d0 == 16) {
                    *(u32x2*)(dst + 8) = (u32x2){isq ? 0u : 0x3F803F80u, 0u};
                    *(u32x4*)(base + 1024 + (r + 32) * 16) = (u32x4){0u, 0u, 0u, 0u};
                }
            } else {
                const int n = nb - nsec + c;
                const int head = n / 20, d = n - head * 20;
                char* dst = ws + WS_KVA + (size_t)head * 262144 + tile * 8192 + 4096 + (kb * 2 + th) * 1024 + (d + 32 * (g & 1)) * 16 + (g >> 1) * 8;
                u32x2 w;
                w[0] = pack2(a[0], a[1]);
                w[1] = pack2(a[2], a[3]);
                *(u32x2*)dst = w;
                if (d == 19) *(u32x2*)(dst + 16) = (u32x2){0x3F803F80u, 0x3F803F80u};
            }
        }
    }
}

__global__ __launch_bounds__(512, 2) void k1_proj(const __hip_bfloat16* __restrict__ Ax, const __hip_bfloat16* __restrict__ Awqkv,
                                                  const __hip_bfloat16* __restrict__ Bx, const __hip_bfloat16* __restrict__ Bckv,
                                                  const __hip_bfloat16* __restrict__ Bwq, const __hip_bfloat16* __restrict__ Bwuk,
                                                  const __hip_bfloat16* __restrict__ Bwuv, const __hip_bfloat16* __restrict__ Awo,
                                                  const __hip_bfloat16* __restrict__ Bwo, char* __restrict__ ws) {
    extern __shared__ __attribute__((aligned(16))) char smem[];
    LAS char* lds = (LAS char*)smem;
    const int tid = threadIdx.x, lane = tid & 63, wid = __builtin_amdgcn_readfirstlane(tid >> 6);
    const int bid = blockIdx.x;
    const int m0 = ((bid >> 3) & 7) * 256;
    const int cc = (bid & 7) + 8 * ((bid >> 6) & 1);
    const bool kv = bid < 128;
    const bool has2 = kv || cc < 15;
    G1Src s1, s2;
    if (kv) {
        g1_src(s1, Bckv, Bwuk, 512, 4096, m0, cc * 256, 4096, wid, lane, false);
        g1_src(s2, Bckv, Bwuv, 512, 4096, m0, cc * 256, 4096, wid, lane, false);
    } else {
        g1_src(s1, Bx, Bwq, 640, 4096, m0, cc * 256, 4096, wid, lane, false);
        g1_src(s2, Ax, Awqkv, 640, 1920, m0, has2 ? cc * 128 : 0, 1920, wid, lane, true);
    }

    f32x4 acc[8][4];
    bf16x8 xf[2][4], wf[2][4];
    const unsigned lds0 = (unsigned)(uintptr_t)lds;
    if (bid == 255 && tid < 40) ((unsigned*)(ws + WS_CNT))[tid * 64] = 0u;
    STAMP(0, 0);
    set_m0(lds0 + wid * 8192 + 4096);
    sfor<0, 8>([&](auto ic) { g1_piece<decltype(ic)::value>(s1, 0); });
    set_m0(lds0 + 65536 + wid * 8192 + 4096);
    sfor<0, 8>([&](auto ic) { g1_piece<decltype(ic)::value>(s1, 1); });
    WAIT_VM(8);
    BARRIER();
    STAMP(0, 1);
    {
        set_m0(lds0 + 131072 + wid * 256);
        const unsigned gw = (unsigned)(bid * 8 + wid) * 64 + lane;
#define PF(ptr, nbytes) do { constexpr unsigned R_ = 131072u / ((nbytes) / 128u);                                     \
            const unsigned ln_ = gw / R_;                                                                                \
            const unsigned off_ = (gw % R_ == 0u && ln_ * 128u < (nbytes)) ? ln_ * 128u : 0u;                             \
            asm volatile("global_load_lds_dword %0, %1" :: "v"(off_), "s"((const char*)(ptr)) : "memory"); } while (0)
        PF(Bx, 2048u * 640u * 2u); PF(Bwq, 640u * 4096u * 2u); PF(Ax, 2048u * 640u * 2u); PF(Awqkv, 640u * 1920u * 2u);
        PF(Bwo, 4096u * 640u * 2u); PF(Awo, 640u * 640u * 2u);
        PF(Bckv, 2048u * 512u * 2u); PF(Bwuk, 512u * 4096u * 2u); PF(Bwuv, 512u * 4096u * 2u);
#undef PF
    }
    g1_loop<true, 4>(acc, xf, wf, s1, s2, has2, true, lds, wid, lane);
    if (kv) g1_epi<EP_BK>(acc, m0, cc * 256, 4096, 0, ws, wid, lane);
    else g1_epi<EP_BQ>(acc, m0, cc * 256, 4096, 0, ws, wid, lane);
    STAMP(0, 2);
    if (kv) {
        g1_loop<false, 4>(acc, xf, wf, s2, s2, false, false, lds, wid, lane);
        g1_epi<EP_BV>(acc, m0, cc * 256, 4096, 0, ws, wid, lane);
    } else if (cc < 10) {
        g1_loop<true, 2>(acc, xf, wf, s2, s2, false, false, lds, wid, lane);
        g1_epi<EP_AQK>(acc, m0, cc * 128, 1280, 0, ws, wid, lane);
    } else if (has2) {
        g1_loop<false, 2>(acc, xf, wf, s2, s2, false, false, lds, wid, lane);
        g1_epi<EP_AV>(acc, m0, cc * 128, 1920, 1280, ws, wid, lane);
    }
    STAMP(0, 3);
}

constexpr int SLOT = 40960;
constexpr int NSLOT = 4;
constexpr int K2_LDS = NSLOT * SLOT;
constexpr int NIT = 34;

__global__ __launch_bounds__(512, 2) void k2_attn(char* __restrict__ ws) {
    extern __shared__ __attribute__((aligned(16))) char smem[];
    LAS char* lds = (LAS char*)smem;
    const int tid = threadIdx.x, lane = tid & 63, wid = __builtin_amdgcn_readfirstlane(tid >> 6);
    const int r = lane & 31, h = lane >> 5;
    const int g = blockIdx.x, xcd = g & 7, idx = g >> 3;
    const int head = xcd * 4 + (idx >> 3), x = idx & 7;
    const char* kvb = ws + WS_KVB + (size_t)head * 1048576;
    const char* kva = ws + WS_KVA + (size_t)head * 262144;
    const int n1 = 32 - 2 * x;
    const unsigned lbase = (unsigned)(uintptr_t)lds + lane * 16;

    STAMP(1, 0);
    const unsigned lds0 = (unsigned)(uintptr_t)lds;
    const unsigned voff = lane * 16;
    const int wa = wid & 3;
    const char* sbB = kvb;
    const char* sbA = kva;
#define K2_M0(tt)                                                                           \
    do {                                                                                    \
        const int t3_ = (tt) < NIT ? (tt) : NIT - 1;                                        \
        const int tb3_ = t3_ < n1 ? t3_ : t3_ - n1;                                         \
        const int ta3_ = t3_ < 32 ? t3_ : 31;                                               \
        sbB = kvb + (size_t)tb3_ * 32768 + wid * 4096 + 2048;                               \
        sbA = kva + (size_t)ta3_ * 8192 + wid * 1024 - 2048;                                \
        set_m0(lds0 + ((tt) < NIT ? ((tt) & 3) : 2 + ((tt) & 1)) * SLOT + wid * 5120 + 2048); \
    } while (0)
#define K2_DMA(i) do { SBAR(); if constexpr ((i) < 4) dma_s<(i) * 1024 - 2048>(voff, sbB); else dma_s<2048>(voff, sbA); SBAR(); } while (0)
#define K2_STAGE_ALL(tt) do { K2_M0(tt); K2_DMA(0); K2_DMA(1); K2_DMA(2); K2_DMA(3); K2_DMA(4); } while (0)
#define BOFF(f) (((f) >> 2) * 5120 + ((f) & 3) * 1024)
#define AOFF(a) ((a) * 5120 + 4096)
    K2_STAGE_ALL(0);
    K2_STAGE_ALL(1);

#define K2_SYNC(it) do { WAIT_VM(5); BARRIER(); K2_M0((it) + 3); } while (0)

    if (wid < 4) {
        bf16x8 fr[8];
        K2_SYNC(-1);
        STAMP(1, 1);
        sfor<0, 8>([&](auto ic) { constexpr int j = decltype(ic)::value; lds_rd<BOFF((j & 1) * 8 + (j >> 1))>(fr[j], lbase); });
        K2_DMA(0); K2_DMA(1);
        PRIO(1);
        int it = 0;
        for (int blk = 0; blk < 2; ++blk) {
            const int qb = blk == 0 ? 15 - x : x;
            const int nt = 2 * qb + 2;
            const int q0 = qb * 128 + wid * 32;
            const int tb32 = q0 >> 5;
            bf16x8 qf[8];
            const char* qp = ws + WS_QB + (size_t)((head * 64 + tb32) * 8) * 1024 + lane * 16;
#pragma unroll
            for (int ks = 0; ks < 8; ++ks) qf[ks] = *(const bf16x8*)(qp + ks * 1024);
            f32x16 o[4];
#pragma unroll
            for (int db = 0; db < 4; ++db) o[db] = f32x16{};
            f32x16 negm = f32x16{};
            float l = 0.f;
            for (int t = 0; t < nt; ++t, ++it) {
                const int rel = (q0 - 64 * t) >> 5;
                const unsigned ac = lbase + (it & 3) * SLOT;
                const unsigned an = lbase + ((it + 1) & 3) * SLOT;
                f32x16 s0, s1;
                SEG(0);
                sfor<0, 16>([&](auto ic) {
                    constexpr int j = decltype(ic)::value, ks = j >> 1;
                    lds_wait<7>(fr[j & 7]);
                    if constexpr (j == 0) s0 = MFMA32(fr[0], qf[0], negm);
                    else if constexpr (j == 1) s1 = MFMA32(fr[1], qf[0], negm);
                    else if constexpr ((j & 1) == 0) s0 = MFMA32(fr[j & 7], qf[ks], s0);
                    else s1 = MFMA32(fr[j & 7], qf[ks], s1);
                    constexpr int jn = j + 8;
                    SBAR();
                    if constexpr (jn < 16) lds_rd<BOFF((jn & 1) * 8 + (jn >> 1))>(fr[j & 7], ac);
                    else lds_rd<BOFF(jn)>(fr[j & 7], ac);
                    if constexpr (j == 2) K2_DMA(2);
                    if constexpr (j == 8) K2_DMA(3);
                    if constexpr (j == 14) K2_DMA(4);
                    SBAR();
                });
#define B_MASK()                                                                                        \
    do {                                                                                                \
        if (rel <= 1) {                                                                                 \
            const int lim = 32 * rel + r - 4 * h;                                                       \
            _Pragma("unroll") for (int i = 0; i < 16; ++i) {                                            \
                const int kr = (i & 3) + 8 * (i >> 2);                                                  \
                if (kr > lim) s0[i] = -__builtin_inff();                                                \
                if (kr + 32 > lim) s1[i] = -__builtin_inff();                                           \
            }                                                                                           \
        }                                                                                               \
    } while (0)
                SEG(1);
                PRIO(0);
                B_MASK();
                float ps = 0.f;
                if (t > 0) {
#pragma unroll
                    for (int i = 0; i < 16; ++i) { s0[i] = __builtin_amdgcn_exp2f(s0[i]); ps += s0[i]; }
#pragma unroll
                    for (int i = 0; i < 16; ++i) { s1[i] = __builtin_amdgcn_exp2f(s1[i]); ps += s1[i]; }
                }
                if (t == 0 || __any(!(ps <= 65536.f))) {
                    if (t > 0) {
                        LAS char* sl = lds + (it & 3) * SLOT + lane * 16;
                        s0 = negm; s1 = negm;
#pragma unroll 1
                        for (int ks = 0; ks < 8; ++ks) {
                            bf16x8 qq = qf[0];
#pragma unroll
                            for (int u = 1; u < 8; ++u) qq = (ks == u) ? qf[u] : qq;
                            s0 = MFMA32(*(LAS bf16x8*)(sl + (ks >> 2) * 5120 + (ks & 3) * 1024), qq, s0);
                            s1 = MFMA32(*(LAS bf16x8*)(sl + 10240 + (ks >> 2) * 5120 + (ks & 3) * 1024), qq, s1);
                        }
                        B_MASK();
                    }
                    float mx = s0[0];
#pragma unroll
                    for (int i = 1; i < 16; ++i) mx = fmaxf(mx, s0[i]);
#pragma unroll
                    for (int i = 0; i < 16; ++i) mx = fmaxf(mx, s1[i]);
                    auto rr = __builtin_amdgcn_permlane32_swap(__float_as_uint(mx), __float_as_uint(mx), false, false);
                    mx = fmaxf(__uint_as_float(rr[0]), __uint_as_float(rr[1]));
                    float delta = mx;
                    if (t > 0) {
                        delta = fmaxf(mx, 0.f);
                        const float alpha = __builtin_amdgcn_exp2f(-delta);
                        l *= alpha;
#pragma unroll
                        for (int db = 0; db < 4; ++db)
#pragma unroll
                            for (int i = 0; i < 16; ++i) o[db][i] *= alpha;
                    }
                    const float nm = negm[0] - delta;
#pragma unroll
                    for (int i = 0; i < 16; ++i) negm[i] = nm;
                    ps = 0.f;
#pragma unroll
                    for (int i = 0; i < 16; ++i) { s0[i] = __builtin_amdgcn_exp2f(s0[i] - delta); ps += s0[i]; }
#pragma unroll
                    for (int i = 0; i < 16; ++i) { s1[i] = __builtin_amdgcn_exp2f(s1[i] - delta); ps += s1[i]; }
                }
                l += ps;
                bf16x8 pf[4];
                pf[0] = __builtin_bit_cast(bf16x8, pack8(s0, 0));
                pf[1] = __builtin_bit_cast(bf16x8, pack8(s0, 1));
                pf[2] = __builtin_bit_cast(bf16x8, pack8(s1, 0));
                pf[3] = __builtin_bit_cast(bf16x8, pack8(s1, 1));
                SEG(2);
                if (it + 1 < NIT) { K2_SYNC(it); } else { K2_M0(it + 3); }
                SEG(3);
                PRIO(1);
                sfor<0, 16>([&](auto ic) {
                    constexpr int j = decltype(ic)::value, s4 = j >> 2, db = j & 3;
                    lds_wait<7>(fr[j & 7]);
                    o[db] = MFMA32(fr[j & 7], pf[s4], o[db]);
                    SBAR();
                    if constexpr (j < 8) lds_rd<BOFF(24 + j)>(fr[j & 7], ac);
                    else { constexpr int jn = j - 8; lds_rd<BOFF((jn & 1) * 8 + (jn >> 1))>(fr[j & 7], an); }
                    if constexpr (j == 4) K2_DMA(0);
                    if constexpr (j == 10) K2_DMA(1);
                    SBAR();
                });
                SEG(4);
            }
            auto rr = __builtin_amdgcn_permlane32_swap(__float_as_uint(l), __float_as_uint(l), false, false);
            const float inv = 1.0f / (__uint_as_float(rr[0]) + __uint_as_float(rr[1]));
            char* cp = ws + WS_CTXB + (size_t)(tb32 * 256 + head * 8) * 1024 + lane * 16;
#pragma unroll
            for (int db = 0; db < 4; ++db) {
                st16(cp + (2 * db) * 1024, pack8s(o[db], 0, inv));
                st16(cp + (2 * db + 1) * 1024, pack8s(o[db], 1, inv));
            }
        }
        WAIT_VM(0);
        STAMP(1, 2);
#undef B_MASK
    } else {
        const int tb32 = x * 8 + wa * 2;
        bf16x8 qf[2][2];
        const char* qp = ws + WS_QA + (size_t)((head * 64 + tb32) * 2) * 1024 + lane * 16;
#pragma unroll
        for (int qt = 0; qt < 2; ++qt)
#pragma unroll
            for (int ks = 0; ks < 2; ++ks) qf[qt][ks] = *(const bf16x8*)(qp + (qt * 2 + ks) * 1024);
        f32x16 o[2];
        o[0] = f32x16{}; o[1] = f32x16{};
        float negm[2] = {0.f, 0.f};
        bf16x8 kf[4], vf[4];
        f32x16 s[2][2];
        K2_SYNC(-1);
        lds_rd<AOFF(0)>(kf[0], lbase); lds_rd<AOFF(1)>(kf[1], lbase); lds_rd<AOFF(2)>(kf[2], lbase); lds_rd<AOFF(3)>(kf[3], lbase);
        for (int it = 0; it <= 32; ++it) {
            SEG(0);
            if (it > 0) {
                u32x4 pf[2][2][2];
                unsigned chk = 0;
                sfor<0, 4>([&](auto bc) {
                    constexpr int blk = decltype(bc)::value, kb = blk >> 1, qt = blk & 1;
                    f32x16 e;
#pragma unroll
                    for (int i = 0; i < 8; ++i) e[i] = __builtin_amdgcn_exp2f(s[kb][qt][i]);
                    K2_DMA(blk);
#pragma unroll
                    for (int i = 8; i < 16; ++i) e[i] = __builtin_amdgcn_exp2f(s[kb][qt][i]);
                    if constexpr (blk == 3) K2_DMA(4);
                    pf[kb][qt][0] = pack8(e, 0);
                    pf[kb][qt][1] = pack8(e, 1);
                    chk |= pf[kb][qt][0][0] | pf[kb][qt][0][1] | pf[kb][qt][0][2] | pf[kb][qt][0][3];
                    chk |= pf[kb][qt][1][0] | pf[kb][qt][1][1] | pf[kb][qt][1][2] | pf[kb][qt][1][3];
                });
                const bool first = (it == 1);
                if (first || __any((chk & 0x40004000u) != 0u)) {
#pragma unroll
                    for (int qt = 0; qt < 2; ++qt) {
                        float mx = s[0][qt][0];
#pragma unroll
                        for (int i = 1; i < 16; ++i) mx = fmaxf(mx, s[0][qt][i]);
#pragma unroll
                        for (int i = 0; i < 16; ++i) mx = fmaxf(mx, s[1][qt][i]);
                        auto rr = __builtin_amdgcn_permlane32_swap(__float_as_uint(mx), __float_as_uint(mx), false, false);
                        mx = fmaxf(__uint_as_float(rr[0]), __uint_as_float(rr[1]));
                        float delta = mx + 10.f;
                        if (!first) delta = fmaxf(delta, 0.f);
                        const float tgt = negm[qt] - delta;
                        const unsigned hi = pack2(tgt, 0.f) & 0xFFFFu;
                        const float hif = __uint_as_float(hi << 16);
                        const unsigned lo = pack2(tgt - hif, 0.f) & 0xFFFFu;
                        const float neff = hif + __uint_as_float(lo << 16);
                        delta = negm[qt] - neff;
                        negm[qt] = neff;
                        {
                            u32x4 qq = __builtin_bit_cast(u32x4, qf[qt][1]);
                            qq[2] = h ? 0u : (hi | (lo << 16));
                            qf[qt][1] = __builtin_bit_cast(bf16x8, qq);
                        }
                        if (!first) {
                            const float alpha = __builtin_amdgcn_exp2f(-delta);
#pragma unroll
                            for (int i = 0; i < 16; ++i) o[qt][i] *= alpha;
                        }
#pragma unroll
                        for (int kb = 0; kb < 2; ++kb) {
                            f32x16 e;
#pragma unroll
                            for (int i = 0; i < 16; ++i) e[i] = __builtin_amdgcn_exp2f(s[kb][qt][i] - delta);
                            pf[kb][qt][0] = pack8(e, 0);
                            pf[kb][qt][1] = pack8(e, 1);
                        }
                    }
                }
                SEG(1);
                PRIO(1);
                lds_wait2<4>(vf[0], vf[1]);
                lds_wait2<4>(vf[2], vf[3]);
#pragma unroll
                for (int kb = 0; kb < 2; ++kb)
#pragma unroll
                    for (int s2 = 0; s2 < 2; ++s2)
#pragma unroll
                        for (int qt = 0; qt < 2; ++qt)
                            o[qt] = MFMA32(vf[kb * 2 + s2], __builtin_bit_cast(bf16x8, pf[kb][qt][s2]), o[qt]);
            } else {
                K2_DMA(0); K2_DMA(1); K2_DMA(2); K2_DMA(3); K2_DMA(4);
            }
            SEG(2);
            lds_wait2<0>(kf[0], kf[1]);
            lds_wait2<0>(kf[2], kf[3]);
#pragma unroll
            for (int kb = 0; kb < 2; ++kb)
#pragma unroll
                for (int qt = 0; qt < 2; ++qt) {
                    s[kb][qt] = MFMA32(kf[kb * 2], qf[qt][0], f32x16{});
                    s[kb][qt] = MFMA32(kf[kb * 2 + 1], qf[qt][1], s[kb][qt]);
                }
            SBAR();
            if (it == 32) STAMPW(1, 3, 4);
            SEG(3);
            PRIO(0);
            K2_SYNC(it);
            SEG(4);
            const unsigned ac = lbase + (it & 3) * SLOT;
            const unsigned an = lbase + ((it + 1) & 3) * SLOT;
            lds_rd<AOFF(4)>(vf[0], ac); lds_rd<AOFF(5)>(vf[1], ac); lds_rd<AOFF(6)>(vf[2], ac); lds_rd<AOFF(7)>(vf[3], ac);
            lds_rd<AOFF(0)>(kf[0], an); lds_rd<AOFF(1)>(kf[1], an); lds_rd<AOFF(2)>(kf[2], an); lds_rd<AOFF(3)>(kf[3], an);
        }
        WAIT_VM(0);
#pragma unroll
        for (int qt = 0; qt < 2; ++qt) {
            const unsigned lv = __float_as_uint(o[qt][8]);
            auto rr = __builtin_amdgcn_permlane32_swap(lv, lv, false, false);
            const float lsum = __uint_as_float(rr[1]);
            const float inv = 1.0f / lsum;
            char* cp = ws + WS_CTXA + (size_t)((tb32 + qt) * 40) * 1024;
#pragma unroll
            for (int gq = 0; gq < 3; ++gq) {
                const int d0 = 8 * gq + 4 * h;
                if (d0 < 20) {
                    const int k = head * 20 + d0;
                    u32x2 w;
                    w[0] = pack2(o[qt][4 * gq + 0] * inv, o[qt][4 * gq + 1] * inv);
                    w[1] = pack2(o[qt][4 * gq + 2] * inv, o[qt][4 * gq + 3] * inv);
                    *(u32x2*)(cp + (k >> 4) * 1024 + (r + 32 * ((k >> 3) & 1)) * 16 + (k & 4) * 2) = w;
                }
            }
        }
    }
}

constexpr int G3_STAGE = 49152;
constexpr int G3_LDS = 3 * G3_STAGE;

__global__ __launch_bounds__(512, 2) void k3_oproj(const __hip_bfloat16* __restrict__ Awo, const __hip_bfloat16* __restrict__ Bwo,
                                                   char* __restrict__ ws, __hip_bfloat16* __restrict__ out) {
    extern __shared__ __attribute__((aligned(16))) char smem[];
    LAS char* lds = (LAS char*)smem;
    const int tid = threadIdx.x, lane = tid & 63, wid = __builtin_amdgcn_readfirstlane(tid >> 6);
    const int wm = wid >> 1, wn = wid & 1, h = lane >> 5;
    const int bid = blockIdx.x;
    const bool isB = bid < 200;
    int mt, nt, sp, kt0, nkt, kstot;
    const char* Xc; const char* Wb;
    if (isB) {
        mt = bid & 7; const int rest = bid >> 3; nt = rest % 5; sp = rest / 5;
        kt0 = sp * 13; nkt = sp == 4 ? 12 : 13; kstot = 256;
        Xc = ws + WS_CTXB; Wb = (const char*)Bwo;
    } else {
        const int b2 = bid - 200; mt = b2 & 7; nt = b2 >> 3; sp = 5;
        kt0 = 0; nkt = 10; kstot = 40;
        Xc = ws + WS_CTXA; Wb = (const char*)Awo;
    }
    unsigned woff[2];
#pragma unroll
    for (int i = 0; i < 2; ++i) {
        const int k = (wid * 2 + i) * 4 + (lane >> 4);
        const int p = lane & 15, b = (p >> 2) ^ (k & 3);
        woff[i] = (unsigned)(((kt0 * 64 + k) * 640 + nt * 128 + (b * 4 + (p & 3)) * 8) * 2);
    }
    const unsigned lds0 = (unsigned)(uintptr_t)lds;
    const char* xsb = Xc + (size_t)((mt * 8 + wid) * kstot + kt0 * 4) * 1024 + 4096;
    const unsigned voff = lane * 16;
    woff[1] -= 1024;
    auto piece = [&](int kt, auto ic) {
        constexpr int i = decltype(ic)::value;
        if constexpr (i < 4) dma_s<i * 1024 - 4096>(voff, xsb + (size_t)kt * 4096);
        else dma_s<(i - 4) * 1024>(woff[i - 4], Wb + (size_t)kt * (64u * 640u * 2u));
    };
    const int q = (lane >> 2) & 3, p4 = lane & 3, g16 = (lane >> 4) & 1;
    const int r0 = isB ? 4 * h : 8 * h, r1 = isB ? 8 + 4 * h : 8 * h + 4;
    int wb0[2], wb1[2];
#pragma unroll
    for (int ni = 0; ni < 2; ++ni) {
        const int b = wn * 2 + ni;
        const int c = ((b ^ q) * 64) + g16 * 32 + p4 * 8;
        wb0[ni] = (r0 >> 3) * 6144 + 4096 + ((r0 & 7) + q) * 256 + c;
        wb1[ni] = (r1 >> 3) * 6144 + 4096 + ((r1 & 7) + q) * 256 + c;
    }
    const int xb0 = wm * 2 * 6144 + lane * 16;
    f32x16 acc[2][2];
#pragma unroll
    for (int mi = 0; mi < 2; ++mi)
#pragma unroll
        for (int ni = 0; ni < 2; ++ni) acc[mi][ni] = f32x16{};

    bf16x8 xf[2][4][2], wf[2][4][2];
#define G3_RD(set, bb)                                                                                           \
    do {                                                                                                         \
        _Pragma("unroll") for (int ks = 0; ks < 4; ++ks) {                                                       \
            _Pragma("unroll") for (int ni = 0; ni < 2; ++ni)                                                     \
                wf[set][ks][ni] = tr_frag((bb) + wb0[ni] + ks * 12288, (bb) + wb1[ni] + ks * 12288);             \
            _Pragma("unroll") for (int mi = 0; mi < 2; ++mi)                                                     \
                xf[set][ks][mi] = *(LAS bf16x8*)((bb) + xb0 + mi * 6144 + ks * 1024);                            \
        }                                                                                                        \
    } while (0)
#define G3_STEP(set, kt)                                                                                         \
    do {                                                                                                         \
        if ((kt) + 2 < nkt) { WAIT_VM(6); } else { WAIT_VM(0); }                                                 \
        LGKM0();                                                                                                 \
        BARRIER();                                                                                               \
        const int b0_ = (kt) % 3, b1_ = ((kt) + 1) % 3;                                                          \
        set_m0(lds0 + b0_ * G3_STAGE + wid * 6144 + 4096);                                                       \
        if ((kt) + 1 < nkt) G3_RD((set) ^ 1, lds + b1_ * G3_STAGE);                                              \
        SBAR();                                                                                                  \
        const bool on_ = (kt) + 3 < nkt;                                                                         \
        sfor<0, 8>([&](auto jc) {                                                                                \
            constexpr int j = decltype(jc)::value, ks = j >> 1, mi = j & 1;                                      \
            _Pragma("unroll") for (int ni = 0; ni < 2; ++ni)                                                     \
                acc[mi][ni] = MFMA32(wf[set][ks][ni], xf[set][ks][mi], acc[mi][ni]);                             \
            if constexpr (j < 6) { SBAR(); if (on_) piece((kt) + 3, jc); SBAR(); }                               \
        });                                                                                                      \
        SBAR();                                                                                                  \
    } while (0)
    STAMP(2, 0);
#pragma unroll
    for (int t = 0; t < 3; ++t) {
        set_m0(lds0 + t * G3_STAGE + wid * 6144 + 4096);
        sfor<0, 6>([&](auto ic) { piece(t, ic); });
    }
    WAIT_VM(12);
    BARRIER();
    STAMP(2, 1);
    G3_RD(0, lds);
    int kt = 0;
#pragma unroll 1
    for (; kt + 1 < nkt; kt += 2) { G3_STEP(0, kt); G3_STEP(1, kt + 1); }
    if (kt < nkt) G3_STEP(0, kt);
#undef G3_RD
#undef G3_STEP
    STAMP(2, 2);
    const int r = lane & 31;
    if (!isB) {
#pragma unroll
        for (int mi = 0; mi < 2; ++mi)
#pragma unroll
            for (int ni = 0; ni < 2; ++ni) {
                char* op = (char*)out + (size_t)((mt * 8 + wm * 2 + mi) * 32 + r) * 2560 + ((nt * 4 + wn * 2 + ni) * 32 + 4 * h) * 2;
#pragma unroll
                for (int gq = 0; gq < 4; ++gq) {
                    u32x2 o;
                    o[0] = pack2(acc[mi][ni][4 * gq], acc[mi][ni][4 * gq + 1]);
                    o[1] = pack2(acc[mi][ni][4 * gq + 2], acc[mi][ni][4 * gq + 3]);
                    *(u32x2*)(op + gq * 16) = o;
                }
            }
        STAMP(2, 3);
        return;
    }
#pragma unroll
    for (int mi = 0; mi < 2; ++mi)
#pragma unroll
        for (int ni = 0; ni < 2; ++ni) {
            const int st = (wm * 2 + mi) * 4 + wn * 2 + ni;
            if (st % 5 == sp) continue;
            const int mt32 = mt * 8 + wm * 2 + mi, nt32 = nt * 4 + wn * 2 + ni;
            char* dst = ws + WS_PART + (size_t)sp * SLAB + (size_t)(mt32 * 20 + nt32) * 4096 + lane * 16;
#pragma unroll
            for (int gq = 0; gq < 4; ++gq) {
                f32x4 v = {acc[mi][ni][4 * gq], acc[mi][ni][4 * gq + 1], acc[mi][ni][4 * gq + 2], acc[mi][ni][4 * gq + 3]};
                asm volatile("global_store_dwordx4 %0, %1, off sc1\n\ts_nop 1" :: "v"(dst + gq * 1024), "v"(v) : "memory");
            }
        }
    asm volatile("s_waitcnt vmcnt(0)" ::: "memory");
    BARRIER();
    unsigned* cnt = (unsigned*)(ws + WS_CNT) + (mt * 5 + nt) * 64;
    if (wid == 0) {
        if (lane == 0) __hip_atomic_fetch_add(cnt, 1u, __ATOMIC_RELAXED, __HIP_MEMORY_SCOPE_AGENT);
        for (unsigned spins = 0; spins < (1u << 24); ++spins) {
            const unsigned c = __builtin_amdgcn_readfirstlane(__hip_atomic_load(cnt, __ATOMIC_RELAXED, __HIP_MEMORY_SCOPE_AGENT));
            if (c >= (unsigned)NSPLIT) break;
            __builtin_amdgcn_s_sleep(1);
        }
    }
    BARRIER();
    const auto rsrc = __builtin_amdgcn_make_buffer_rsrc((void*)(ws + WS_PART), 0, (int)(NSPLIT * SLAB), 0x00020000);
#pragma unroll
    for (int mi = 0; mi < 2; ++mi)
#pragma unroll
        for (int ni = 0; ni < 2; ++ni) {
            const int st = (wm * 2 + mi) * 4 + wn * 2 + ni;
            if (st % 5 != sp) continue;
            const int mt32 = mt * 8 + wm * 2 + mi, nt32 = nt * 4 + wn * 2 + ni;
            const int off = (mt32 * 20 + nt32) * 4096 + lane * 16;
            u32x4 v[4][4];
#pragma unroll
            for (int s = 0; s < 4; ++s) {
                const int so = (s < sp ? s : s + 1) * (int)SLAB;
#pragma unroll
                for (int gq = 0; gq < 4; ++gq) v[s][gq] = __builtin_amdgcn_raw_buffer_load_b128(rsrc, off + gq * 1024, so, 16);
            }
            char* op = (char*)out + (size_t)(mt32 * 32 + r) * 2560 + (640 + nt32 * 32 + 4 * h) * 2;
#pragma unroll
            for (int gq = 0; gq < 4; ++gq) {
                f32x4 t = {acc[mi][ni][4 * gq], acc[mi][ni][4 * gq + 1], acc[mi][ni][4 * gq + 2], acc[mi][ni][4 * gq + 3]};
                t += (__builtin_bit_cast(f32x4, v[0][gq]) + __builtin_bit_cast(f32x4, v[1][gq])) +
                     (__builtin_bit_cast(f32x4, v[2][gq]) + __builtin_bit_cast(f32x4, v[3][gq]));
                u32x2 o;
                o[0] = pack2(t[0], t[1]);
                o[1] = pack2(t[2], t[3]);
                *(u32x2*)(op + gq * 16) = o;
            }
        }
    STAMP(2, 3);
}

extern "C" void kernel_launch(void* const* d_in, const int* in_sizes, int n_in,
                              void* d_out, int out_size, void* d_ws, size_t ws_size,
                              hipStream_t stream) {
    (void)in_sizes; (void)n_in; (void)out_size; (void)ws_size;
    const __hip_bfloat16* Ax = (const __hip_bfloat16*)d_in[0];
    const __hip_bfloat16* Awqkv = (const __hip_bfloat16*)d_in[1];
    const __hip_bfloat16* Awo = (const __hip_bfloat16*)d_in[2];
    const __hip_bfloat16* Bx = (const __hip_bfloat16*)d_in[3];
    const __hip_bfloat16* Bckv = (const __hip_bfloat16*)d_in[4];
    const __hip_bfloat16* Bwq = (const __hip_bfloat16*)d_in[5];
    const __hip_bfloat16* Bwuk = (const __hip_bfloat16*)d_in[6];
    const __hip_bfloat16* Bwuv = (const __hip_bfloat16*)d_in[7];
    const __hip_bfloat16* Bwo = (const __hip_bfloat16*)d_in[8];
    char* ws = (char*)d_ws;
    (void)hipFuncSetAttribute((const void*)k1_proj, hipFuncAttributeMaxDynamicSharedMemorySize, G1_LDS);
    (void)hipFuncSetAttribute((const void*)k2_attn, hipFuncAttributeMaxDynamicSharedMemorySize, K2_LDS);
    (void)hipFuncSetAttribute((const void*)k3_oproj, hipFuncAttributeMaxDynamicSharedMemorySize, G3_LDS);
    k1_proj<<<256, 512, G1_LDS, stream>>>(Ax, Awqkv, Bx, Bckv, Bwq, Bwuk, Bwuv, Awo, Bwo, ws);
    k2_attn<<<256, 512, K2_LDS, stream>>>(ws);
    k3_oproj<<<240, 512, G3_LDS, stream>>>(Awo, Bwo, ws, (__hip_bfloat16*)d_out);
}
```

```cpp
#include <hip/hip_runtime.h>
#include <hip/hip_bf16.h>
#include <stdint.h>
#include <type_traits>

typedef short s16x4 __attribute__((ext_vector_type(4)));
typedef short bf16x8 __attribute__((ext_vector_type(8)));
typedef float f32x16 __attribute__((ext_vector_type(16)));
typedef float f32x4 __attribute__((ext_vector_type(4)));
typedef float f32x2 __attribute__((ext_vector_type(2)));
typedef unsigned u32x4 __attribute__((ext_vector_type(4)));
typedef unsigned u32x2 __attribute__((ext_vector_type(2)));
typedef __bf16 bf16x2_t __attribute__((ext_vector_type(2)));
#define LAS __attribute__((address_space(3)))
#define DI __device__ __forceinline__
#define MFMA32(a, b, c) __builtin_amdgcn_mfma_f32_32x32x16_bf16((a), (b), (c), 0, 0, 0)
#define MFMA16(a, b, c) __builtin_amdgcn_mfma_f32_16x16x32_bf16((a), (b), (c), 0, 0, 0)
#define CFENCE() asm volatile("" ::: "memory")
#define BARRIER() do { CFENCE(); __builtin_amdgcn_s_barrier(); CFENCE(); } while (0)
#define WAIT_VM(n) asm volatile("s_waitcnt vmcnt(" #n ") lgkmcnt(0)" ::: "memory")
#define SBAR() __builtin_amdgcn_sched_barrier(0)

DI unsigned pack2(float a, float b) {
    f32x2 v = {a, b};
    bf16x2_t r = __builtin_convertvector(v, bf16x2_t);
    return __builtin_bit_cast(unsigned, r);
}
DI u32x4 pack8(const f32x16& x, int s) {
    u32x4 w;
    w[0] = pack2(x[8 * s + 0], x[8 * s + 1]);
    w[1] = pack2(x[8 * s + 2], x[8 * s + 3]);
    w[2] = pack2(x[8 * s + 4], x[8 * s + 5]);
    w[3] = pack2(x[8 * s + 6], x[8 * s + 7]);
    return w;
}
DI u32x4 pack8s(const f32x16& x, int s, float c) {
    u32x4 w;
    w[0] = pack2(x[8 * s + 0] * c, x[8 * s + 1] * c);
    w[1] = pack2(x[8 * s + 2] * c, x[8 * s + 3] * c);
    w[2] = pack2(x[8 * s + 4] * c, x[8 * s + 5] * c);
    w[3] = pack2(x[8 * s + 6] * c, x[8 * s + 7] * c);
    return w;
}
DI void glds16(const void* g, LAS char* l) {
    __builtin_amdgcn_global_load_lds((const unsigned*)g, (LAS unsigned*)l, 16, 0, 0);
}
DI bf16x8 tr_frag(LAS char* p0, LAS char* p1) {
    s16x4 lo = __builtin_amdgcn_ds_read_tr16_b64_v4i16((LAS s16x4*)p0);
    s16x4 hi = __builtin_amdgcn_ds_read_tr16_b64_v4i16((LAS s16x4*)p1);
    return __builtin_shufflevector(lo, hi, 0, 1, 2, 3, 4, 5, 6, 7);
}

#define STMODE 2
DI void st16(void* p, u32x4 v) {
    asm volatile("global_store_dwordx4 %0, %1, off sc1\n\ts_nop 1" :: "v"(p), "v"(v) : "memory");
}
template <int I, int N, class F> DI void sfor(F&& f) {
    if constexpr (I < N) { f(std::integral_constant<int, I>{}); sfor<I + 1, N>(f); }
}
template <int OFF> DI void lds_rd(bf16x8& d, unsigned addr) {
    asm volatile("ds_read_b128 %0, %1 offset:%2" : "=v"(d) : "v"(addr), "i"(OFF));
}
template <int N> DI void lds_wait(bf16x8& d) { asm volatile("s_waitcnt lgkmcnt(%1)" : "+v"(d) : "i"(N)); }
template <int N> DI void lds_wait2(bf16x8& d, bf16x8& e) { asm volatile("s_waitcnt lgkmcnt(%2)" : "+v"(d), "+v"(e) : "i"(N)); }
#define LGKM0() __builtin_amdgcn_s_waitcnt(0xC07F)
DI void set_m0(unsigned v) { asm volatile("s_mov_b32 m0, %0\n\ts_nop 0" :: "s"(v) : "memory"); }
template <int OFF> DI void dma_s(unsigned voff, const char* sbase) {
    asm volatile("global_load_lds_dwordx4 %0, %1 offset:%2" :: "v"(voff), "s"(sbase), "i"(OFF) : "memory");
}

#define DYNPRIO 0
#define PRIO(x) do { if (DYNPRIO) __builtin_amdgcn_s_setprio(x); } while (0)
#define ABL_NOPIECE 0
#define ABL_NOBAR 0
#define ABL_T0 0
#define ABL_K2 0
#define ABL_NODMA 0
#define ABL_NOMFMA 0
#define SEG(i) do { } while (0)
#define STAMP(kid, idx) do { } while (0)
#define STAMPW(kid, idx, w) do { } while (0)
constexpr size_t WS_KVB = 0;
constexpr size_t WS_QB = WS_KVB + (size_t)32 * 1048576;
constexpr size_t WS_KVA = WS_QB + (size_t)16 * 1048576;
constexpr size_t WS_QA = WS_KVA + (size_t)8 * 1048576;
constexpr size_t WS_CTXB = WS_QA + (size_t)4 * 1048576;
constexpr size_t WS_CTXA = WS_CTXB + (size_t)16 * 1048576;
constexpr size_t WS_PART = WS_CTXA + (size_t)4 * 1048576;
constexpr size_t WS_CNT = WS_PART + (size_t)32 * 1048576;
constexpr int NSPLIT = 5;
constexpr size_t SLAB = (size_t)64 * 20 * 4096;

constexpr float CB = 0.08838834764831845f * 1.4426950408889634f;
constexpr float CA = 0.22360679774997896f * 1.4426950408889634f;

#define PREFETCH 1
constexpr int G1_LDS = 2 * 65536 + 2048;
enum { EP_BK = 0, EP_BV = 1, EP_BQ = 2, EP_AQK = 3, EP_AV = 4 };

struct G1Src { unsigned xoff[4], woff[4]; const char* Xb; const char* Wb; unsigned wstep; int nkt; bool half; };
DI void g1_src(G1Src& s, const __hip_bfloat16* X, const __hip_bfloat16* W, int K, int ldw, int m0, int n0, int nlim, int wid, int lane, bool half) {
    s.half = half;
#pragma unroll
    for (int i = 0; i < 4; ++i) {
        const int row = (wid * 4 + i) * 8 + (lane >> 3);
        const int c = (lane & 7) ^ ((row >> 1) & 7);
        s.xoff[i] = (unsigned)(((m0 + row) * K + c * 8) * 2) + 4096 - i * 1024;
        const int k = (wid * 4 + i) * 2 + (lane >> 5);
        const int p = lane & 31, b = (p >> 2) ^ (k & 3);
        int col = n0 + (b * 4 + ((p & 3) ^ ((wid & 1) << 1))) * 8;
        col = col < nlim ? col : nlim - 8;
        s.woff[i] = (unsigned)((k * ldw + col) * 2) - i * 1024;
    }
    s.Xb = (const char*)X; s.Wb = (const char*)W; s.wstep = (unsigned)(64 * ldw * 2); s.nkt = K >> 6;
}
template <int I> DI void g1_piece(const G1Src& s, int kt) {
    if constexpr (I < 4) dma_s<I * 1024 - 4096>(s.xoff[I], s.Xb + (size_t)kt * 128);
    else if (!s.half || (threadIdx.x & 16) == 0) dma_s<(I - 4) * 1024>(s.woff[I - 4], s.Wb + (size_t)kt * s.wstep);
}

template <bool TR, int NT>
DI void g1_loop(f32x4 (&acc)[8][4], bf16x8 (&xs)[2][4], bf16x8 (&wsb)[2][4], const G1Src& cur, const G1Src& nxt, bool has_next, bool first,
                LAS char* lds, int wid, int lane) {
    const int wm = wid >> 2, wn = wid & 3, r16 = lane & 15, g = lane >> 4;
    const unsigned lds0 = (unsigned)(uintptr_t)lds;
    const int nkt = cur.nkt;
    int xb[2];
#pragma unroll
    for (int st = 0; st < 2; ++st) xb[st] = wm * 4 * 8192 + r16 * 128 + (((4 * st + g) ^ (r16 >> 1)) * 16);
    const int q = (lane >> 2) & 3, p4 = lane & 3;
    int wb[4];
#pragma unroll
    for (int nt = 0; nt < 4; ++nt) {
        const int b = NT == 4 ? wn * 2 + (nt >> 1) : wn;
        wb[nt] = g * 8192 + 4096 + q * 512 + ((b ^ q) * 64) + (((nt & 1) ^ (g & 1)) * 32) + p4 * 8;
    }
#pragma unroll
    for (int mt = 0; mt < 8; ++mt)
#pragma unroll
        for (int nt = 0; nt < 4; ++nt) acc[mt][nt] = f32x4{};
#define G1_RX(set, bb, st, mh)                                                                                   \
    _Pragma("unroll") for (int m4 = 0; m4 < 4; ++m4)                                                             \
        xs[set][m4] = *(LAS bf16x8*)((bb) + xb[st] + ((mh) * 2 + (m4 >> 1)) * 8192 + (m4 & 1) * 2048)
#define G1_RW(bb, st)                                                                                            \
    _Pragma("unroll") for (int nt = 0; nt < NTR; ++nt)                                                           \
        wsb[st][nt] = tr_frag((bb) + wb[nt] + (st) * 32768, (bb) + wb[nt] + (st) * 32768 + 2048)
#define G1_MM(set, st, mh, on, kn, p0, pe)                                                                       \
    sfor<0, 4>([&](auto mc) {                                                                                    \
        constexpr int m4 = decltype(mc)::value;                                                                  \
        _Pragma("unroll") for (int nt = 0; nt < NT; ++nt)                                                        \
            acc[(mh) * 4 + m4][nt] = TR ? MFMA16(wsb[st][nt], xs[set][m4], acc[(mh) * 4 + m4][nt])               \
                                        : MFMA16(xs[set][m4], wsb[st][nt], acc[(mh) * 4 + m4][nt]);              \
        if constexpr ((pe) > (p0)) {                                                                             \
            SBAR();                                                                                              \
            if (on) {                                                                                            \
                if ((kn) < nkt) { g1_piece<2 * m4>(cur, (kn)); g1_piece<2 * m4 + 1>(cur, (kn)); }                \
                else { g1_piece<2 * m4>(nxt, (kn) - nkt); g1_piece<2 * m4 + 1>(nxt, (kn) - nkt); }               \
            }                                                                                                    \
            SBAR();                                                                                              \
        }                                                                                                        \
    })
    constexpr int NTR = NT;
    G1_RW(lds, 0);
    if (first) { G1_RX(0, lds, 0, 0); }
#pragma unroll 1
    for (int kt = 0; kt < nkt; ++kt) {
        LAS char* bb = lds + (kt & 1) * 65536;
        LAS char* bn = lds + ((kt + 1) & 1) * 65536;
        const bool on2 = kt + 2 < nkt || has_next;
        LGKM0(); G1_RX(1, bb, 0, 1); SBAR(); G1_MM(0, 0, 0, false, 0, 0, 0); SBAR();
        LGKM0(); G1_RW(bb, 1); G1_RX(0, bb, 1, 0); SBAR(); G1_MM(1, 0, 1, false, 0, 0, 0); SBAR();
        LGKM0(); G1_RX(1, bb, 1, 1); SBAR(); G1_MM(0, 1, 0, false, 0, 0, 0); SBAR();
        LGKM0();
        if (PREFETCH && first && kt == 0) { WAIT_VM(5); } else { WAIT_VM(0); }
        BARRIER();
        set_m0(lds0 + (kt & 1) * 65536 + wid * 8192 + 4096);
        if (kt + 1 < nkt) G1_RW(bn, 0);
        if (kt + 1 < nkt || has_next) G1_RX(0, bn, 0, 0);
        SBAR(); G1_MM(1, 1, 1, on2, kt + 2, 0, 8); SBAR();
    }
#undef G1_RX
#undef G1_RW
#undef G1_MM
}

DI void st8(void* p, u32x2 v) {
    asm volatile("global_store_dwordx2 %0, %1, off sc1\n\ts_nop 1" :: "v"(p), "v"(v) : "memory");
}

template <int EP>
DI void g1_epi(const f32x4 (&acc)[8][4], int m0, int n0, int nlim, int nsec, char* __restrict__ ws, int wid, int lane) {
    const int wm = wid >> 2, wn = wid & 3, c = lane & 15, g = lane >> 4;
    if constexpr (EP == EP_BK || EP == EP_BQ || EP == EP_BV) {
#pragma unroll
        for (int mt = 0; mt < 8; ++mt) {
            const int tb = (m0 + wm * 128 + mt * 16) >> 5;
            const int tile = tb >> 1, kb = tb & 1, th = mt & 1;
#pragma unroll
            for (int np = 0; np < 2; ++np) {
                const int nb = n0 + wn * 64 + np * 32;
                const int head = nb >> 7;
                const float sc = EP == EP_BQ ? CB : 1.0f;
                const f32x4 a = acc[mt][2 * np], b = acc[mt][2 * np + 1];
                unsigned a0 = pack2(a[0] * sc, a[1] * sc), a1 = pack2(a[2] * sc, a[3] * sc);
                unsigned b0 = pack2(b[0] * sc, b[1] * sc), b1 = pack2(b[2] * sc, b[3] * sc);
                if constexpr (EP == EP_BV) {
                    auto r0 = __builtin_amdgcn_permlane32_swap(a0, b0, false, false);
                    auto r1 = __builtin_amdgcn_permlane32_swap(a1, b1, false, false);
                    const u32x4 w = {r0[0], r1[0], r0[1], r1[1]};
                    const int db = (nb >> 5) & 3;
                    char* base = ws + WS_KVB + (size_t)head * 1048576 + tile * 32768 + 16384 + ((kb * 2 + th) * 4 + db) * 1024;
                    st16(base + ((g >> 1) * 16 + c + 32 * (g & 1)) * 16, w);
                } else {
                    auto r0 = __builtin_amdgcn_permlane16_swap(a0, b0, false, false);
                    auto r1 = __builtin_amdgcn_permlane16_swap(a1, b1, false, false);
                    const u32x4 w = {r0[0], r1[0], r0[1], r1[1]};
                    const int ks = ((nb >> 4) & 7) + (g & 1);
                    char* base = EP == EP_BK ? ws + WS_KVB + (size_t)head * 1048576 + tile * 32768 + (kb * 8 + ks) * 1024
                                             : ws + WS_QB + (size_t)((head * 64 + tb) * 8 + ks) * 1024;
                    st16(base + (th * 16 + c + 32 * (g >> 1)) * 16, w);
                }
            }
        }
        return;
    }
#pragma unroll
    for (int mt = 0; mt < 8; ++mt) {
        const int tb = (m0 + wm * 128 + mt * 16) >> 5;
        const int tile = tb >> 1, kb = tb & 1, th = mt & 1;
#pragma unroll
        for (int nt = 0; nt < 2; ++nt) {
            const int nb = n0 + wn * 32 + nt * 16;
            if (nb >= nlim) continue;
            const f32x4 a = acc[mt][nt];
            if constexpr (EP == EP_BK || EP == EP_BQ || EP == EP_BV) {
            } else if constexpr (EP == EP_AQK) {
                const bool isq = nb < 640;
                const float sc = isq ? CA : 1.0f;
                const int r = th * 16 + c;
                const int n = nb - (isq ? 0 : 640) + 4 * g;
                const int head = n / 20, d0 = n - head * 20;
                char* base = isq ? ws + WS_QA + (size_t)((head * 64 + tb) * 2) * 1024
                                 : ws + WS_KVA + (size_t)head * 262144 + tile * 8192 + kb * 2048;
                char* dst = base + (d0 >> 4) * 1024 + (r + 32 * ((d0 >> 3) & 1)) * 16 + (d0 & 4) * 2;
                u32x2 w;
                w[0] = pack2(a[0] * sc, a[1] * sc);
                w[1] = pack2(a[2] * sc, a[3] * sc);
                *(u32x2*)dst = w;
                if (d0 == 16) {
                    *(u32x2*)(dst + 8) = (u32x2){isq ? 0u : 0x3F803F80u, 0u};
                    *(u32x4*)(base + 1024 + (r + 32) * 16) = (u32x4){0u, 0u, 0u, 0u};
                }
            } else {
                const int n = nb - nsec + c;
                const int head = n / 20, d = n - head * 20;
                char* dst = ws + WS_KVA + (size_t)head * 262144 + tile * 8192 + 4096 + (kb * 2 + th) * 1024 + (d + 32 * (g & 1)) * 16 + (g >> 1) * 8;
                u32x2 w;
                w[0] = pack2(a[0], a[1]);
                w[1] = pack2(a[2], a[3]);
                *(u32x2*)dst = w;
                if (d == 19) *(u32x2*)(dst + 16) = (u32x2){0x3F803F80u, 0x3F803F80u};
            }
        }
    }
}

__global__ __launch_bounds__(512, 2) void k1_proj(const __hip_bfloat16* __restrict__ Ax, const __hip_bfloat16* __restrict__ Awqkv,
                                                  const __hip_bfloat16* __restrict__ Bx, const __hip_bfloat16* __restrict__ Bckv,
                                                  const __hip_bfloat16* __restrict__ Bwq, const __hip_bfloat16* __restrict__ Bwuk,
                                                  const __hip_bfloat16* __restrict__ Bwuv, const __hip_bfloat16* __restrict__ Awo,
                                                  const __hip_bfloat16* __restrict__ Bwo, char* __restrict__ ws) {
    extern __shared__ __attribute__((aligned(16))) char smem[];
    LAS char* lds = (LAS char*)smem;
    const int tid = threadIdx.x, lane = tid & 63, wid = __builtin_amdgcn_readfirstlane(tid >> 6);
    const int bid = blockIdx.x;
    const int m0 = ((bid >> 3) & 7) * 256;
    const int cc = (bid & 7) + 8 * ((bid >> 6) & 1);
    const bool kv = bid < 128;
    const bool has2 = kv || cc < 15;
    G1Src s1, s2;
    if (kv) {
        g1_src(s1, Bckv, Bwuk, 512, 4096, m0, cc * 256, 4096, wid, lane, false);
        g1_src(s2, Bckv, Bwuv, 512, 4096, m0, cc * 256, 4096, wid, lane, false);
    } else {
        g1_src(s1, Bx, Bwq, 640, 4096, m0, cc * 256, 4096, wid, lane, false);
        g1_src(s2, Ax, Awqkv, 640, 1920, m0, has2 ? cc * 128 : 0, 1920, wid, lane, true);
    }

    f32x4 acc[8][4];
    bf16x8 xf[2][4], wf[2][4];
    const unsigned lds0 = (unsigned)(uintptr_t)lds;
    if (bid == 255 && tid < 40) ((unsigned*)(ws + WS_CNT))[tid * 64] = 0u;
    STAMP(0, 0);
    set_m0(lds0 + wid * 8192 + 4096);
    sfor<0, 8>([&](auto ic) { g1_piece<decltype(ic)::value>(s1, 0); });
    set_m0(lds0 + 65536 + wid * 8192 + 4096);
    sfor<0, 8>([&](auto ic) { g1_piece<decltype(ic)::value>(s1, 1); });
    WAIT_VM(8);
    BARRIER();
    STAMP(0, 1);
    {
        set_m0(lds0 + 131072 + wid * 256);
        const unsigned gw = (unsigned)(bid * 8 + wid) * 64 + lane;
#define PF(ptr, nbytes) do { constexpr unsigned R_ = 131072u / ((nbytes) / 128u);                                     \
            const unsigned ln_ = gw / R_;                                                                                \
            const unsigned off_ = (gw % R_ == 0u && ln_ * 128u < (nbytes)) ? ln_ * 128u : 0u;                             \
            asm volatile("global_load_lds_dword %0, %1" :: "v"(off_), "s"((const char*)(ptr)) : "memory"); } while (0)
        PF(Bwuv, 512u * 4096u * 2u); PF(Ax, 2048u * 640u * 2u); PF(Awqkv, 640u * 1920u * 2u);
        PF(Bwo, 4096u * 640u * 2u); PF(Awo, 640u * 640u * 2u);
#undef PF
    }
    g1_loop<true, 4>(acc, xf, wf, s1, s2, has2, true, lds, wid, lane);
    if (kv) g1_epi<EP_BK>(acc, m0, cc * 256, 4096, 0, ws, wid, lane);
    else g1_epi<EP_BQ>(acc, m0, cc * 256, 4096, 0, ws, wid, lane);
    STAMP(0, 2);
    if (kv) {
        g1_loop<false, 4>(acc, xf, wf, s2, s2, false, false, lds, wid, lane);
        g1_epi<EP_BV>(acc, m0, cc * 256, 4096, 0, ws, wid, lane);
    } else if (cc < 10) {
        g1_loop<true, 2>(acc, xf, wf, s2, s2, false, false, lds, wid, lane);
        g1_epi<EP_AQK>(acc, m0, cc * 128, 1280, 0, ws, wid, lane);
    } else if (has2) {
        g1_loop<false, 2>(acc, xf, wf, s2, s2, false, false, lds, wid, lane);
        g1_epi<EP_AV>(acc, m0, cc * 128, 1920, 1280, ws, wid, lane);
    }
    STAMP(0, 3);
}

constexpr int SLOT = 40960;
constexpr int NSLOT = 4;
constexpr int K2_LDS = NSLOT * SLOT;
constexpr int NIT = 34;

__global__ __launch_bounds__(512, 2) void k2_attn(char* __restrict__ ws) {
    extern __shared__ __attribute__((aligned(16))) char smem[];
    LAS char* lds = (LAS char*)smem;
    const int tid = threadIdx.x, lane = tid & 63, wid = __builtin_amdgcn_readfirstlane(tid >> 6);
    const int r = lane & 31, h = lane >> 5;
    const int g = blockIdx.x, xcd = g & 7, idx = g >> 3;
    const int head = xcd * 4 + (idx >> 3), x = idx & 7;
    const char* kvb = ws + WS_KVB + (size_t)head * 1048576;
    const char* kva = ws + WS_KVA + (size_t)head * 262144;
    const int n1 = 32 - 2 * x;
    const unsigned lbase = (unsigned)(uintptr_t)lds + lane * 16;

    STAMP(1, 0);
    const unsigned lds0 = (unsigned)(uintptr_t)lds;
    const unsigned voff = lane * 16;
    const int wa = wid & 3;
    const char* sbB = kvb;
    const char* sbA = kva;
#define K2_M0(tt)                                                                           \
    do {                                                                                    \
        const int t3_ = (tt) < NIT ? (tt) : NIT - 1;                                        \
        const int tb3_ = t3_ < n1 ? t3_ : t3_ - n1;                                         \
        const int ta3_ = t3_ < 32 ? t3_ : 31;                                               \
        sbB = kvb + (size_t)tb3_ * 32768 + wid * 4096 + 2048;                               \
        sbA = kva + (size_t)ta3_ * 8192 + wid * 1024 - 2048;                                \
        set_m0(lds0 + ((tt) < NIT ? ((tt) & 3) : 2 + ((tt) & 1)) * SLOT + wid * 5120 + 2048); \
    } while (0)
#define K2_DMA(i) do { SBAR(); if constexpr ((i) < 4) dma_s<(i) * 1024 - 2048>(voff, sbB); else dma_s<2048>(voff, sbA); SBAR(); } while (0)
#define K2_STAGE_ALL(tt) do { K2_M0(tt); K2_DMA(0); K2_DMA(1); K2_DMA(2); K2_DMA(3); K2_DMA(4); } while (0)
#define BOFF(f) (((f) >> 2) * 5120 + ((f) & 3) * 1024)
#define AOFF(a) ((a) * 5120 + 4096)
    K2_STAGE_ALL(0);
    K2_STAGE_ALL(1);

#define K2_SYNC(it) do { WAIT_VM(5); BARRIER(); K2_M0((it) + 3); } while (0)

    if (wid < 4) {
        bf16x8 fr[8];
        K2_SYNC(-1);
        STAMP(1, 1);
        sfor<0, 8>([&](auto ic) { constexpr int j = decltype(ic)::value; lds_rd<BOFF((j & 1) * 8 + (j >> 1))>(fr[j], lbase); });
        K2_DMA(0); K2_DMA(1);
        PRIO(1);
        int it = 0;
        for (int blk = 0; blk < 2; ++blk) {
            const int qb = blk == 0 ? 15 - x : x;
            const int nt = 2 * qb + 2;
            const int q0 = qb * 128 + wid * 32;
            const int tb32 = q0 >> 5;
            bf16x8 qf[8];
            const char* qp = ws + WS_QB + (size_t)((head * 64 + tb32) * 8) * 1024 + lane * 16;
#pragma unroll
            for (int ks = 0; ks < 8; ++ks) qf[ks] = *(const bf16x8*)(qp + ks * 1024);
            f32x16 o[4];
#pragma unroll
            for (int db = 0; db < 4; ++db) o[db] = f32x16{};
            f32x16 negm = f32x16{};
            float l = 0.f;
            for (int t = 0; t < nt; ++t, ++it) {
                const int rel = (q0 - 64 * t) >> 5;
                const unsigned ac = lbase + (it & 3) * SLOT;
                const unsigned an = lbase + ((it + 1) & 3) * SLOT;
                f32x16 s0, s1;
                SEG(0);
                sfor<0, 16>([&](auto ic) {
                    constexpr int j = decltype(ic)::value, ks = j >> 1;
                    lds_wait<7>(fr[j & 7]);
                    if constexpr (j == 0) s0 = MFMA32(fr[0], qf[0], negm);
                    else if constexpr (j == 1) s1 = MFMA32(fr[1], qf[0], negm);
                    else if constexpr ((j & 1) == 0) s0 = MFMA32(fr[j & 7], qf[ks], s0);
                    else s1 = MFMA32(fr[j & 7], qf[ks], s1);
                    constexpr int jn = j + 8;
                    SBAR();
                    if constexpr (jn < 16) lds_rd<BOFF((jn & 1) * 8 + (jn >> 1))>(fr[j & 7], ac);
                    else lds_rd<BOFF(jn)>(fr[j & 7], ac);
                    if constexpr (j == 2) K2_DMA(2);
                    if constexpr (j == 8) K2_DMA(3);
                    if constexpr (j == 14) K2_DMA(4);
                    SBAR();
                });
#define B_MASK()                                                                                        \
    do {                                                                                                \
        if (rel <= 1) {                                                                                 \
            const int lim = 32 * rel + r - 4 * h;                                                       \
            _Pragma("unroll") for (int i = 0; i < 16; ++i) {                                            \
                const int kr = (i & 3) + 8 * (i >> 2);                                                  \
                if (kr > lim) s0[i] = -__builtin_inff();                                                \
                if (kr + 32 > lim) s1[i] = -__builtin_inff();                                           \
            }                                                                                           \
        }                                                                                               \
    } while (0)
                SEG(1);
                PRIO(0);
                B_MASK();
                float ps = 0.f;
                if (t > 0) {
#pragma unroll
                    for (int i = 0; i < 16; ++i) { s0[i] = __builtin_amdgcn_exp2f(s0[i]); ps += s0[i]; }
#pragma unroll
                    for (int i = 0; i < 16; ++i) { s1[i] = __builtin_amdgcn_exp2f(s1[i]); ps += s1[i]; }
                }
                if (t == 0 || __any(!(ps <= 65536.f))) {
                    if (t > 0) {
                        LAS char* sl = lds + (it & 3) * SLOT + lane * 16;
                        s0 = negm; s1 = negm;
#pragma unroll 1
                        for (int ks = 0; ks < 8; ++ks) {
                            bf16x8 qq = qf[0];
#pragma unroll
                            for (int u = 1; u < 8; ++u) qq = (ks == u) ? qf[u] : qq;
                            s0 = MFMA32(*(LAS bf16x8*)(sl + (ks >> 2) * 5120 + (ks & 3) * 1024), qq, s0);
                            s1 = MFMA32(*(LAS bf16x8*)(sl + 10240 + (ks >> 2) * 5120 + (ks & 3) * 1024), qq, s1);
                        }
                        B_MASK();
                    }
                    float mx = s0[0];
#pragma unroll
                    for (int i = 1; i < 16; ++i) mx = fmaxf(mx, s0[i]);
#pragma unroll
                    for (int i = 0; i < 16; ++i) mx = fmaxf(mx, s1[i]);
                    auto rr = __builtin_amdgcn_permlane32_swap(__float_as_uint(mx), __float_as_uint(mx), false, false);
                    mx = fmaxf(__uint_as_float(rr[0]), __uint_as_float(rr[1]));
                    float delta = mx;
                    if (t > 0) {
                        delta = fmaxf(mx, 0.f);
                        const float alpha = __builtin_amdgcn_exp2f(-delta);
                        l *= alpha;
#pragma unroll
                        for (int db = 0; db < 4; ++db)
#pragma unroll
                            for (int i = 0; i < 16; ++i) o[db][i] *= alpha;
                    }
                    const float nm = negm[0] - delta;
#pragma unroll
                    for (int i = 0; i < 16; ++i) negm[i] = nm;
                    ps = 0.f;
#pragma unroll
                    for (int i = 0; i < 16; ++i) { s0[i] = __builtin_amdgcn_exp2f(s0[i] - delta); ps += s0[i]; }
#pragma unroll
                    for (int i = 0; i < 16; ++i) { s1[i] = __builtin_amdgcn_exp2f(s1[i] - delta); ps += s1[i]; }
                }
                l += ps;
                bf16x8 pf[4];
                pf[0] = __builtin_bit_cast(bf16x8, pack8(s0, 0));
                pf[1] = __builtin_bit_cast(bf16x8, pack8(s0, 1));
                pf[2] = __builtin_bit_cast(bf16x8, pack8(s1, 0));
                pf[3] = __builtin_bit_cast(bf16x8, pack8(s1, 1));
                SEG(2);
                if (it + 1 < NIT) { K2_SYNC(it); } else { K2_M0(it + 3); }
                SEG(3);
                PRIO(1);
                sfor<0, 16>([&](auto ic) {
                    constexpr int j = decltype(ic)::value, s4 = j >> 2, db = j & 3;
                    lds_wait<7>(fr[j & 7]);
                    o[db] = MFMA32(fr[j & 7], pf[s4], o[db]);
                    SBAR();
                    if constexpr (j < 8) lds_rd<BOFF(24 + j)>(fr[j & 7], ac);
                    else { constexpr int jn = j - 8; lds_rd<BOFF((jn & 1) * 8 + (jn >> 1))>(fr[j & 7], an); }
                    if constexpr (j == 4) K2_DMA(0);
                    if constexpr (j == 10) K2_DMA(1);
                    SBAR();
                });
                SEG(4);
            }
            auto rr = __builtin_amdgcn_permlane32_swap(__float_as_uint(l), __float_as_uint(l), false, false);
            const float inv = 1.0f / (__uint_as_float(rr[0]) + __uint_as_float(rr[1]));
            char* cp = ws + WS_CTXB + (size_t)(tb32 * 256 + head * 8) * 1024 + lane * 16;
#pragma unroll
            for (int db = 0; db < 4; ++db) {
                st16(cp + (2 * db) * 1024, pack8s(o[db], 0, inv));
                st16(cp + (2 * db + 1) * 1024, pack8s(o[db], 1, inv));
            }
        }
        WAIT_VM(0);
        STAMP(1, 2);
#undef B_MASK
    } else {
        const int tb32 = x * 8 + wa * 2;
        bf16x8 qf[2][2];
        const char* qp = ws + WS_QA + (size_t)((head * 64 + tb32) * 2) * 1024 + lane * 16;
#pragma unroll
        for (int qt = 0; qt < 2; ++qt)
#pragma unroll
            for (int ks = 0; ks < 2; ++ks) qf[qt][ks] = *(const bf16x8*)(qp + (qt * 2 + ks) * 1024);
        f32x16 o[2];
        o[0] = f32x16{}; o[1] = f32x16{};
        float negm[2] = {0.f, 0.f};
        bf16x8 kf[4], vf[4];
        f32x16 s[2][2];
        K2_SYNC(-1);
        lds_rd<AOFF(0)>(kf[0], lbase); lds_rd<AOFF(1)>(kf[1], lbase); lds_rd<AOFF(2)>(kf[2], lbase); lds_rd<AOFF(3)>(kf[3], lbase);
        for (int it = 0; it <= 32; ++it) {
            SEG(0);
            if (it > 0) {
                u32x4 pf[2][2][2];
                unsigned chk = 0;
                sfor<0, 4>([&](auto bc) {
                    constexpr int blk = decltype(bc)::value, kb = blk >> 1, qt = blk & 1;
                    f32x16 e;
#pragma unroll
                    for (int i = 0; i < 8; ++i) e[i] = __builtin_amdgcn_exp2f(s[kb][qt][i]);
                    K2_DMA(blk);
#pragma unroll
                    for (int i = 8; i < 16; ++i) e[i] = __builtin_amdgcn_exp2f(s[kb][qt][i]);
                    if constexpr (blk == 3) K2_DMA(4);
                    pf[kb][qt][0] = pack8(e, 0);
                    pf[kb][qt][1] = pack8(e, 1);
                    chk |= pf[kb][qt][0][0] | pf[kb][qt][0][1] | pf[kb][qt][0][2] | pf[kb][qt][0][3];
                    chk |= pf[kb][qt][1][0] | pf[kb][qt][1][1] | pf[kb][qt][1][2] | pf[kb][qt][1][3];
                });
                const bool first = (it == 1);
                if (first || __any((chk & 0x40004000u) != 0u)) {
#pragma unroll
                    for (int qt = 0; qt < 2; ++qt) {
                        float mx = s[0][qt][0];
#pragma unroll
                        for (int i = 1; i < 16; ++i) mx = fmaxf(mx, s[0][qt][i]);
#pragma unroll
                        for (int i = 0; i < 16; ++i) mx = fmaxf(mx, s[1][qt][i]);
                        auto rr = __builtin_amdgcn_permlane32_swap(__float_as_uint(mx), __float_as_uint(mx), false, false);
                        mx = fmaxf(__uint_as_float(rr[0]), __uint_as_float(rr[1]));
                        float delta = mx + 10.f;
                        if (!first) delta = fmaxf(delta, 0.f);
                        const float tgt = negm[qt] - delta;
                        const unsigned hi = pack2(tgt, 0.f) & 0xFFFFu;
                        const float hif = __uint_as_float(hi << 16);
                        const unsigned lo = pack2(tgt - hif, 0.f) & 0xFFFFu;
                        const float neff = hif + __uint_as_float(lo << 16);
                        delta = negm[qt] - neff;
                        negm[qt] = neff;
                        {
                            u32x4 qq = __builtin_bit_cast(u32x4, qf[qt][1]);
                            qq[2] = h ? 0u : (hi | (lo << 16));
                            qf[qt][1] = __builtin_bit_cast(bf16x8, qq);
                        }
                        if (!first) {
                            const float alpha = __builtin_amdgcn_exp2f(-delta);
#pragma unroll
                            for (int i = 0; i < 16; ++i) o[qt][i] *= alpha;
                        }
#pragma unroll
                        for (int kb = 0; kb < 2; ++kb) {
                            f32x16 e;
#pragma unroll
                            for (int i = 0; i < 16; ++i) e[i] = __builtin_amdgcn_exp2f(s[kb][qt][i] - delta);
                            pf[kb][qt][0] = pack8(e, 0);
                            pf[kb][qt][1] = pack8(e, 1);
                        }
                    }
                }
                SEG(1);
                PRIO(1);
                lds_wait2<4>(vf[0], vf[1]);
                lds_wait2<4>(vf[2], vf[3]);
#pragma unroll
                for (int kb = 0; kb < 2; ++kb)
#pragma unroll
                    for (int s2 = 0; s2 < 2; ++s2)
#pragma unroll
                        for (int qt = 0; qt < 2; ++qt)
                            o[qt] = MFMA32(vf[kb * 2 + s2], __builtin_bit_cast(bf16x8, pf[kb][qt][s2]), o[qt]);
            } else {
                K2_DMA(0); K2_DMA(1); K2_DMA(2); K2_DMA(3); K2_DMA(4);
            }
            SEG(2);
            lds_wait2<0>(kf[0], kf[1]);
            lds_wait2<0>(kf[2], kf[3]);
#pragma unroll
            for (int kb = 0; kb < 2; ++kb)
#pragma unroll
                for (int qt = 0; qt < 2; ++qt) {
                    s[kb][qt] = MFMA32(kf[kb * 2], qf[qt][0], f32x16{});
                    s[kb][qt] = MFMA32(kf[kb * 2 + 1], qf[qt][1], s[kb][qt]);
                }
            SBAR();
            if (it == 32) STAMPW(1, 3, 4);
            SEG(3);
            PRIO(0);
            K2_SYNC(it);
            SEG(4);
            const unsigned ac = lbase + (it & 3) * SLOT;
            const unsigned an = lbase + ((it + 1) & 3) * SLOT;
            lds_rd<AOFF(4)>(vf[0], ac); lds_rd<AOFF(5)>(vf[1], ac); lds_rd<AOFF(6)>(vf[2], ac); lds_rd<AOFF(7)>(vf[3], ac);
            lds_rd<AOFF(0)>(kf[0], an); lds_rd<AOFF(1)>(kf[1], an); lds_rd<AOFF(2)>(kf[2], an); lds_rd<AOFF(3)>(kf[3], an);
        }
        WAIT_VM(0);
#pragma unroll
        for (int qt = 0; qt < 2; ++qt) {
            const unsigned lv = __float_as_uint(o[qt][8]);
            auto rr = __builtin_amdgcn_permlane32_swap(lv, lv, false, false);
            const float lsum = __uint_as_float(rr[1]);
            const float inv = 1.0f / lsum;
            char* cp = ws + WS_CTXA + (size_t)((tb32 + qt) * 40) * 1024;
#pragma unroll
            for (int gq = 0; gq < 3; ++gq) {
                const int d0 = 8 * gq + 4 * h;
                if (d0 < 20) {
                    const int k = head * 20 + d0;
                    u32x2 w;
                    w[0] = pack2(o[qt][4 * gq + 0] * inv, o[qt][4 * gq + 1] * inv);
                    w[1] = pack2(o[qt][4 * gq + 2] * inv, o[qt][4 * gq + 3] * inv);
                    *(u32x2*)(cp + (k >> 4) * 1024 + (r + 32 * ((k >> 3) & 1)) * 16 + (k & 4) * 2) = w;
                }
            }
        }
    }
}

constexpr int G3_STAGE = 49152;
constexpr int G3_LDS = 3 * G3_STAGE;

__global__ __launch_bounds__(512, 2) void k3_oproj(const __hip_bfloat16* __restrict__ Awo, const __hip_bfloat16* __restrict__ Bwo,
                                                   char* __restrict__ ws, __hip_bfloat16* __restrict__ out) {
    extern __shared__ __attribute__((aligned(16))) char smem[];
    LAS char* lds = (LAS char*)smem;
    const int tid = threadIdx.x, lane = tid & 63, wid = __builtin_amdgcn_readfirstlane(tid >> 6);
    const int wm = wid >> 1, wn = wid & 1, h = lane >> 5;
    const int bid = blockIdx.x;
    const bool isB = bid < 200;
    int mt, nt, sp, kt0, nkt, kstot;
    const char* Xc; const char* Wb;
    if (isB) {
        mt = bid & 7; const int rest = bid >> 3; nt = rest % 5; sp = rest / 5;
        kt0 = sp * 13; nkt = sp == 4 ? 12 : 13; kstot = 256;
        Xc = ws + WS_CTXB; Wb = (const char*)Bwo;
    } else {
        const int b2 = bid - 200; mt = b2 & 7; nt = b2 >> 3; sp = 5;
        kt0 = 0; nkt = 10; kstot = 40;
        Xc = ws + WS_CTXA; Wb = (const char*)Awo;
    }
    unsigned woff[2];
#pragma unroll
    for (int i = 0; i < 2; ++i) {
        const int k = (wid * 2 + i) * 4 + (lane >> 4);
        const int p = lane & 15, b = (p >> 2) ^ (k & 3);
        woff[i] = (unsigned)(((kt0 * 64 + k) * 640 + nt * 128 + (b * 4 + (p & 3)) * 8) * 2);
    }
    const unsigned lds0 = (unsigned)(uintptr_t)lds;
    const char* xsb = Xc + (size_t)((mt * 8 + wid) * kstot + kt0 * 4) * 1024 + 4096;
    const unsigned voff = lane * 16;
    woff[1] -= 1024;
    auto piece = [&](int kt, auto ic) {
        constexpr int i = decltype(ic)::value;
        if constexpr (i < 4) dma_s<i * 1024 - 4096>(voff, xsb + (size_t)kt * 4096);
        else dma_s<(i - 4) * 1024>(woff[i - 4], Wb + (size_t)kt * (64u * 640u * 2u));
    };
    const int q = (lane >> 2) & 3, p4 = lane & 3, g16 = (lane >> 4) & 1;
    const int r0 = isB ? 4 * h : 8 * h, r1 = isB ? 8 + 4 * h : 8 * h + 4;
    int wb0[2], wb1[2];
#pragma unroll
    for (int ni = 0; ni < 2; ++ni) {
        const int b = wn * 2 + ni;
        const int c = ((b ^ q) * 64) + g16 * 32 + p4 * 8;
        wb0[ni] = (r0 >> 3) * 6144 + 4096 + ((r0 & 7) + q) * 256 + c;
        wb1[ni] = (r1 >> 3) * 6144 + 4096 + ((r1 & 7) + q) * 256 + c;
    }
    const int xb0 = wm * 2 * 6144 + lane * 16;
    f32x16 acc[2][2];
#pragma unroll
    for (int mi = 0; mi < 2; ++mi)
#pragma unroll
        for (int ni = 0; ni < 2; ++ni) acc[mi][ni] = f32x16{};

    bf16x8 xf[2][4][2], wf[2][4][2];
#define G3_RD(set, bb)                                                                                           \
    do {                                                                                                         \
        _Pragma("unroll") for (int ks = 0; ks < 4; ++ks) {                                                       \
            _Pragma("unroll") for (int ni = 0; ni < 2; ++ni)                                                     \
                wf[set][ks][ni] = tr_frag((bb) + wb0[ni] + ks * 12288, (bb) + wb1[ni] + ks * 12288);             \
            _Pragma("unroll") for (int mi = 0; mi < 2; ++mi)                                                     \
                xf[set][ks][mi] = *(LAS bf16x8*)((bb) + xb0 + mi * 6144 + ks * 1024);                            \
        }                                                                                                        \
    } while (0)
#define G3_STEP(set, kt)                                                                                         \
    do {                                                                                                         \
        if ((kt) + 2 < nkt) { WAIT_VM(6); } else { WAIT_VM(0); }                                                 \
        LGKM0();                                                                                                 \
        BARRIER();                                                                                               \
        const int b0_ = (kt) % 3, b1_ = ((kt) + 1) % 3;                                                          \
        set_m0(lds0 + b0_ * G3_STAGE + wid * 6144 + 4096);                                                       \
        if ((kt) + 1 < nkt) G3_RD((set) ^ 1, lds + b1_ * G3_STAGE);                                              \
        SBAR();                                                                                                  \
        const bool on_ = (kt) + 3 < nkt;                                                                         \
        sfor<0, 8>([&](auto jc) {                                                                                \
            constexpr int j = decltype(jc)::value, ks = j >> 1, mi = j & 1;                                      \
            _Pragma("unroll") for (int ni = 0; ni < 2; ++ni)                                                     \
                acc[mi][ni] = MFMA32(wf[set][ks][ni], xf[set][ks][mi], acc[mi][ni]);                             \
            if constexpr (j < 6) { SBAR(); if (on_) piece((kt) + 3, jc); SBAR(); }                               \
        });                                                                                                      \
        SBAR();                                                                                                  \
    } while (0)
    STAMP(2, 0);
#pragma unroll
    for (int t = 0; t < 3; ++t) {
        set_m0(lds0 + t * G3_STAGE + wid * 6144 + 4096);
        sfor<0, 6>([&](auto ic) { piece(t, ic); });
    }
    WAIT_VM(12);
    BARRIER();
    STAMP(2, 1);
    G3_RD(0, lds);
    int kt = 0;
#pragma unroll 1
    for (; kt + 1 < nkt; kt += 2) { G3_STEP(0, kt); G3_STEP(1, kt + 1); }
    if (kt < nkt) G3_STEP(0, kt);
#undef G3_RD
#undef G3_STEP
    STAMP(2, 2);
    const int r = lane & 31;
    if (!isB) {
#pragma unroll
        for (int mi = 0; mi < 2; ++mi)
#pragma unroll
            for (int ni = 0; ni < 2; ++ni) {
                char* op = (char*)out + (size_t)((mt * 8 + wm * 2 + mi) * 32 + r) * 2560 + ((nt * 4 + wn * 2 + ni) * 32 + 4 * h) * 2;
#pragma unroll
                for (int gq = 0; gq < 4; ++gq) {
                    u32x2 o;
                    o[0] = pack2(acc[mi][ni][4 * gq], acc[mi][ni][4 * gq + 1]);
                    o[1] = pack2(acc[mi][ni][4 * gq + 2], acc[mi][ni][4 * gq + 3]);
                    *(u32x2*)(op + gq * 16) = o;
                }
            }
        STAMP(2, 3);
        return;
    }
#pragma unroll
    for (int mi = 0; mi < 2; ++mi)
#pragma unroll
        for (int ni = 0; ni < 2; ++ni) {
            const int st = (wm * 2 + mi) * 4 + wn * 2 + ni;
            if (st % 5 == sp) continue;
            const int mt32 = mt * 8 + wm * 2 + mi, nt32 = nt * 4 + wn * 2 + ni;
            char* dst = ws + WS_PART + (size_t)sp * SLAB + (size_t)(mt32 * 20 + nt32) * 4096 + lane * 16;
#pragma unroll
            for (int gq = 0; gq < 4; ++gq) {
                f32x4 v = {acc[mi][ni][4 * gq], acc[mi][ni][4 * gq + 1], acc[mi][ni][4 * gq + 2], acc[mi][ni][4 * gq + 3]};
                asm volatile("global_store_dwordx4 %0, %1, off sc1\n\ts_nop 1" :: "v"(dst + gq * 1024), "v"(v) : "memory");
            }
        }
    asm volatile("s_waitcnt vmcnt(0)" ::: "memory");
    BARRIER();
    unsigned* cnt = (unsigned*)(ws + WS_CNT) + (mt * 5 + nt) * 64;
    if (wid == 0) {
        if (lane == 0) __hip_atomic_fetch_add(cnt, 1u, __ATOMIC_RELAXED, __HIP_MEMORY_SCOPE_AGENT);
        for (unsigned spins = 0; spins < (1u << 24); ++spins) {
            const unsigned c = __builtin_amdgcn_readfirstlane(__hip_atomic_load(cnt, __ATOMIC_RELAXED, __HIP_MEMORY_SCOPE_AGENT));
            if (c >= (unsigned)NSPLIT) break;
            __builtin_amdgcn_s_sleep(1);
        }
    }
    BARRIER();
    const auto rsrc = __builtin_amdgcn_make_buffer_rsrc((void*)(ws + WS_PART), 0, (int)(NSPLIT * SLAB), 0x00020000);
#pragma unroll
    for (int mi = 0; mi < 2; ++mi)
#pragma unroll
        for (int ni = 0; ni < 2; ++ni) {
            const int st = (wm * 2 + mi) * 4 + wn * 2 + ni;
            if (st % 5 != sp) continue;
            const int mt32 = mt * 8 + wm * 2 + mi, nt32 = nt * 4 + wn * 2 + ni;
            const int off = (mt32 * 20 + nt32) * 4096 + lane * 16;
            u32x4 v[4][4];
#pragma unroll
            for (int s = 0; s < 4; ++s) {
                const int so = (s < sp ? s : s + 1) * (int)SLAB;
#pragma unroll
                for (int gq = 0; gq < 4; ++gq) v[s][gq] = __builtin_amdgcn_raw_buffer_load_b128(rsrc, off + gq * 1024, so, 16);
            }
            char* op = (char*)out + (size_t)(mt32 * 32 + r) * 2560 + (640 + nt32 * 32 + 4 * h) * 2;
#pragma unroll
            for (int gq = 0; gq < 4; ++gq) {
                f32x4 t = {acc[mi][ni][4 * gq], acc[mi][ni][4 * gq + 1], acc[mi][ni][4 * gq + 2], acc[mi][ni][4 * gq + 3]};
                t += (__builtin_bit_cast(f32x4, v[0][gq]) + __builtin_bit_cast(f32x4, v[1][gq])) +
                     (__builtin_bit_cast(f32x4, v[2][gq]) + __builtin_bit_cast(f32x4, v[3][gq]));
                u32x2 o;
                o[0] = pack2(t[0], t[1]);
                o[1] = pack2(t[2], t[3]);
                *(u32x2*)(op + gq * 16) = o;
            }
        }
    STAMP(2, 3);
}

extern "C" void kernel_launch(void* const* d_in, const int* in_sizes, int n_in,
                              void* d_out, int out_size, void* d_ws, size_t ws_size,
                              hipStream_t stream) {
    (void)in_sizes; (void)n_in; (void)out_size; (void)ws_size;
    const __hip_bfloat16* Ax = (const __hip_bfloat16*)d_in[0];
    const __hip_bfloat16* Awqkv = (const __hip_bfloat16*)d_in[1];
    const __hip_bfloat16* Awo = (const __hip_bfloat16*)d_in[2];
    const __hip_bfloat16* Bx = (const __hip_bfloat16*)d_in[3];
    const __hip_bfloat16* Bckv = (const __hip_bfloat16*)d_in[4];
    const __hip_bfloat16* Bwq = (const __hip_bfloat16*)d_in[5];
    const __hip_bfloat16* Bwuk = (const __hip_bfloat16*)d_in[6];
    const __hip_bfloat16* Bwuv = (const __hip_bfloat16*)d_in[7];
    const __hip_bfloat16* Bwo = (const __hip_bfloat16*)d_in[8];
    char* ws = (char*)d_ws;
    (void)hipFuncSetAttribute((const void*)k1_proj, hipFuncAttributeMaxDynamicSharedMemorySize, G1_LDS);
    (void)hipFuncSetAttribute((const void*)k2_attn, hipFuncAttributeMaxDynamicSharedMemorySize, K2_LDS);
    (void)hipFuncSetAttribute((const void*)k3_oproj, hipFuncAttributeMaxDynamicSharedMemorySize, G3_LDS);
    k1_proj<<<256, 512, G1_LDS, stream>>>(Ax, Awqkv, Bx, Bckv, Bwq, Bwuk, Bwuv, Awo, Bwo, ws);
    k2_attn<<<256, 512, K2_LDS, stream>>>(ws);
    k3_oproj<<<240, 512, G3_LDS, stream>>>(Awo, Bwo, ws, (__hip_bfloat16*)d_out);
}
```

```cpp
#include <hip/hip_runtime.h>
#include <hip/hip_bf16.h>
#include <stdint.h>
#include <type_traits>

typedef short s16x4 __attribute__((ext_vector_type(4)));
typedef short bf16x8 __attribute__((ext_vector_type(8)));
typedef float f32x16 __attribute__((ext_vector_type(16)));
typedef float f32x4 __attribute__((ext_vector_type(4)));
typedef float f32x2 __attribute__((ext_vector_type(2)));
typedef unsigned u32x4 __attribute__((ext_vector_type(4)));
typedef unsigned u32x2 __attribute__((ext_vector_type(2)));
typedef __bf16 bf16x2_t __attribute__((ext_vector_type(2)));
#define LAS __attribute__((address_space(3)))
#define DI __device__ __forceinline__
#define MFMA32(a, b, c) __builtin_amdgcn_mfma_f32_32x32x16_bf16((a), (b), (c), 0, 0, 0)
#define MFMA16(a, b, c) __builtin_amdgcn_mfma_f32_16x16x32_bf16((a), (b), (c), 0, 0, 0)
#define CFENCE() asm volatile("" ::: "memory")
#define BARRIER() do { CFENCE(); __builtin_amdgcn_s_barrier(); CFENCE(); } while (0)
#define WAIT_VM(n) asm volatile("s_waitcnt vmcnt(" #n ") lgkmcnt(0)" ::: "memory")
#define SBAR() __builtin_amdgcn_sched_barrier(0)

DI unsigned pack2(float a, float b) {
    f32x2 v = {a, b};
    bf16x2_t r = __builtin_convertvector(v, bf16x2_t);
    return __builtin_bit_cast(unsigned, r);
}
DI u32x4 pack8(const f32x16& x, int s) {
    u32x4 w;
    w[0] = pack2(x[8 * s + 0], x[8 * s + 1]);
    w[1] = pack2(x[8 * s + 2], x[8 * s + 3]);
    w[2] = pack2(x[8 * s + 4], x[8 * s + 5]);
    w[3] = pack2(x[8 * s + 6], x[8 * s + 7]);
    return w;
}
DI u32x4 pack8s(const f32x16& x, int s, float c) {
    u32x4 w;
    w[0] = pack2(x[8 * s + 0] * c, x[8 * s + 1] * c);
    w[1] = pack2(x[8 * s + 2] * c, x[8 * s + 3] * c);
    w[2] = pack2(x[8 * s + 4] * c, x[8 * s + 5] * c);
    w[3] = pack2(x[8 * s + 6] * c, x[8 * s + 7] * c);
    return w;
}
DI void glds16(const void* g, LAS char* l) {
    __builtin_amdgcn_global_load_lds((const unsigned*)g, (LAS unsigned*)l, 16, 0, 0);
}
DI bf16x8 tr_frag(LAS char* p0, LAS char* p1) {
    s16x4 lo = __builtin_amdgcn_ds_read_tr16_b64_v4i16((LAS s16x4*)p0);
    s16x4 hi = __builtin_amdgcn_ds_read_tr16_b64_v4i16((LAS s16x4*)p1);
    return __builtin_shufflevector(lo, hi, 0, 1, 2, 3, 4, 5, 6, 7);
}

#define STMODE 2
DI void st16(void* p, u32x4 v) {
    asm volatile("global_store_dwordx4 %0, %1, off sc1\n\ts_nop 1" :: "v"(p), "v"(v) : "memory");
}
template <int I, int N, class F> DI void sfor(F&& f) {
    if constexpr (I < N) { f(std::integral_constant<int, I>{}); sfor<I + 1, N>(f); }
}
template <int OFF> DI void lds_rd(bf16x8& d, unsigned addr) {
    asm volatile("ds_read_b128 %0, %1 offset:%2" : "=v"(d) : "v"(addr), "i"(OFF));
}
template <int N> DI void lds_wait(bf16x8& d) { asm volatile("s_waitcnt lgkmcnt(%1)" : "+v"(d) : "i"(N)); }
template <int N> DI void lds_wait2(bf16x8& d, bf16x8& e) { asm volatile("s_waitcnt lgkmcnt(%2)" : "+v"(d), "+v"(e) : "i"(N)); }
#define LGKM0() __builtin_amdgcn_s_waitcnt(0xC07F)
DI void set_m0(unsigned v) { asm volatile("s_mov_b32 m0, %0\n\ts_nop 0" :: "s"(v) : "memory"); }
template <int OFF> DI void dma_s(unsigned voff, const char* sbase) {
    asm volatile("global_load_lds_dwordx4 %0, %1 offset:%2" :: "v"(voff), "s"(sbase), "i"(OFF) : "memory");
}

#define DYNPRIO 0
#define PRIO(x) do { if (DYNPRIO) __builtin_amdgcn_s_setprio(x); } while (0)
#define ABL_NOPIECE 0
#define ABL_NOBAR 0
#define ABL_T0 0
#define ABL_K2 0
#define ABL_NODMA 0
#define ABL_NOMFMA 0
#define SEG(i) do { } while (0)
#define STAMP(kid, idx) do { } while (0)
#define STAMPW(kid, idx, w) do { } while (0)
constexpr size_t WS_KVB = 0;
constexpr size_t WS_QB = WS_KVB + (size_t)32 * 1048576;
constexpr size_t WS_KVA = WS_QB + (size_t)16 * 1048576;
constexpr size_t WS_QA = WS_KVA + (size_t)8 * 1048576;
constexpr size_t WS_CTXB = WS_QA + (size_t)4 * 1048576;
constexpr size_t WS_CTXA = WS_CTXB + (size_t)16 * 1048576;
constexpr size_t WS_PART = WS_CTXA + (size_t)4 * 1048576;
constexpr size_t WS_CNT = WS_PART + (size_t)32 * 1048576;
constexpr int NSPLIT = 5;
constexpr size_t SLAB = (size_t)64 * 20 * 4096;

constexpr float CB = 0.08838834764831845f * 1.4426950408889634f;
constexpr float CA = 0.22360679774997896f * 1.4426950408889634f;

#define PREFETCH 1
constexpr int G1_LDS = 2 * 65536 + 2048;
enum { EP_BK = 0, EP_BV = 1, EP_BQ = 2, EP_AQK = 3, EP_AV = 4 };

struct G1Src { unsigned xoff[4], woff[4]; const char* Xb; const char* Wb; unsigned wstep; int nkt; bool half; };
DI void g1_src(G1Src& s, const __hip_bfloat16* X, const __hip_bfloat16* W, int K, int ldw, int m0, int n0, int nlim, int wid, int lane, bool half) {
    s.half = half;
#pragma unroll
    for (int i = 0; i < 4; ++i) {
        const int row = (wid * 4 + i) * 8 + (lane >> 3);
        const int c = (lane & 7) ^ ((row >> 1) & 7);
        s.xoff[i] = (unsigned)(((m0 + row) * K + c * 8) * 2) + 4096 - i * 1024;
        const int k = (wid * 4 + i) * 2 + (lane >> 5);
        const int p = lane & 31, b = (p >> 2) ^ (k & 3);
        int col = n0 + (b * 4 + ((p & 3) ^ ((wid & 1) << 1))) * 8;
        col = col < nlim ? col : nlim - 8;
        s.woff[i] = (unsigned)((k * ldw + col) * 2) - i * 1024;
    }
    s.Xb = (const char*)X; s.Wb = (const char*)W; s.wstep = (unsigned)(64 * ldw * 2); s.nkt = K >> 6;
}
template <int I> DI void g1_piece(const G1Src& s, int kt) {
    if constexpr (I < 4) dma_s<I * 1024 - 4096>(s.xoff[I], s.Xb + (size_t)kt * 128);
    else if (!s.half || (threadIdx.x & 16) == 0) dma_s<(I - 4) * 1024>(s.woff[I - 4], s.Wb + (size_t)kt * s.wstep);
}

template <bool TR, int NT>
DI void g1_loop(f32x4 (&acc)[8][4], bf16x8 (&xs)[2][4], bf16x8 (&wsb)[2][4], const G1Src& cur, const G1Src& nxt, bool has_next, bool first,
                LAS char* lds, int wid, int lane) {
    const int wm = wid >> 2, wn = wid & 3, r16 = lane & 15, g = lane >> 4;
    const unsigned lds0 = (unsigned)(uintptr_t)lds;
    const int nkt = cur.nkt;
    int xb[2];
#pragma unroll
    for (int st = 0; st < 2; ++st) xb[st] = wm * 4 * 8192 + r16 * 128 + (((4 * st + g) ^ (r16 >> 1)) * 16);
    const int q = (lane >> 2) & 3, p4 = lane & 3;
    int wb[4];
#pragma unroll
    for (int nt = 0; nt < 4; ++nt) {
        const int b = NT == 4 ? wn * 2 + (nt >> 1) : wn;
        wb[nt] = g * 8192 + 4096 + q * 512 + ((b ^ q) * 64) + (((nt & 1) ^ (g & 1)) * 32) + p4 * 8;
    }
#pragma unroll
    for (int mt = 0; mt < 8; ++mt)
#pragma unroll
        for (int nt = 0; nt < 4; ++nt) acc[mt][nt] = f32x4{};
#define G1_RX(set, bb, st, mh)                                                                                   \
    _Pragma("unroll") for (int m4 = 0; m4 < 4; ++m4)                                                             \
        xs[set][m4] = *(LAS bf16x8*)((bb) + xb[st] + ((mh) * 2 + (m4 >> 1)) * 8192 + (m4 & 1) * 2048)
#define G1_RW(bb, st)                                                                                            \
    _Pragma("unroll") for (int nt = 0; nt < NTR; ++nt)                                                           \
        wsb[st][nt] = tr_frag((bb) + wb[nt] + (st) * 32768, (bb) + wb[nt] + (st) * 32768 + 2048)
#define G1_MM(set, st, mh, on, kn, p0, pe)                                                                       \
    sfor<0, 4>([&](auto mc) {                                                                                    \
        constexpr int m4 = decltype(mc)::value;                                                                  \
        _Pragma("unroll") for (int nt = 0; nt < NT; ++nt)                                                        \
            acc[(mh) * 4 + m4][nt] = TR ? MFMA16(wsb[st][nt], xs[set][m4], acc[(mh) * 4 + m4][nt])               \
                                        : MFMA16(xs[set][m4], wsb[st][nt], acc[(mh) * 4 + m4][nt]);              \
        if constexpr ((pe) > (p0)) {                                                                             \
            SBAR();                                                                                              \
            if (on) {                                                                                            \
                if ((kn) < nkt) { g1_piece<2 * m4>(cur, (kn)); g1_piece<2 * m4 + 1>(cur, (kn)); }                \
                else { g1_piece<2 * m4>(nxt, (kn) - nkt); g1_piece<2 * m4 + 1>(nxt, (kn) - nkt); }               \
            }                                                                                                    \
            SBAR();                                                                                              \
        }                                                                                                        \
    })
    constexpr int NTR = NT;
    G1_RW(lds, 0);
    if (first) { G1_RX(0, lds, 0, 0); }
#pragma unroll 1
    for (int kt = 0; kt < nkt; ++kt) {
        LAS char* bb = lds + (kt & 1) * 65536;
        LAS char* bn = lds + ((kt + 1) & 1) * 65536;
        const bool on2 = kt + 2 < nkt || has_next;
        LGKM0(); G1_RX(1, bb, 0, 1); SBAR(); G1_MM(0, 0, 0, false, 0, 0, 0); SBAR();
        LGKM0(); G1_RW(bb, 1); G1_RX(0, bb, 1, 0); SBAR(); G1_MM(1, 0, 1, false, 0, 0, 0); SBAR();
        LGKM0(); G1_RX(1, bb, 1, 1); SBAR(); G1_MM(0, 1, 0, false, 0, 0, 0); SBAR();
        LGKM0();
        if (PREFETCH && first && kt == 0) { WAIT_VM(5); } else { WAIT_VM(0); }
        BARRIER();
        set_m0(lds0 + (kt & 1) * 65536 + wid * 8192 + 4096);
        if (kt + 1 < nkt) G1_RW(bn, 0);
        if (kt + 1 < nkt || has_next) G1_RX(0, bn, 0, 0);
        SBAR(); G1_MM(1, 1, 1, on2, kt + 2, 0, 8); SBAR();
    }
#undef G1_RX
#undef G1_RW
#undef G1_MM
}

DI void st8(void* p, u32x2 v) {
    asm volatile("global_store_dwordx2 %0, %1, off sc1\n\ts_nop 1" :: "v"(p), "v"(v) : "memory");
}

template <int EP>
DI void g1_epi(const f32x4 (&acc)[8][4], int m0, int n0, int nlim, int nsec, char* __restrict__ ws, int wid, int lane) {
    const int wm = wid >> 2, wn = wid & 3, c = lane & 15, g = lane >> 4;
    if constexpr (EP == EP_BK || EP == EP_BQ || EP == EP_BV) {
#pragma unroll
        for (int mt = 0; mt < 8; ++mt) {
            const int tb = (m0 + wm * 128 + mt * 16) >> 5;
            const int tile = tb >> 1, kb = tb & 1, th = mt & 1;
#pragma unroll
            for (int np = 0; np < 2; ++np) {
                const int nb = n0 + wn * 64 + np * 32;
                const int head = nb >> 7;
                const float sc = EP == EP_BQ ? CB : 1.0f;
                const f32x4 a = acc[mt][2 * np], b = acc[mt][2 * np + 1];
                unsigned a0 = pack2(a[0] * sc, a[1] * sc), a1 = pack2(a[2] * sc, a[3] * sc);
                unsigned b0 = pack2(b[0] * sc, b[1] * sc), b1 = pack2(b[2] * sc, b[3] * sc);
                if constexpr (EP == EP_BV) {
                    auto r0 = __builtin_amdgcn_permlane32_swap(a0, b0, false, false);
                    auto r1 = __builtin_amdgcn_permlane32_swap(a1, b1, false, false);
                    const u32x4 w = {r0[0], r1[0], r0[1], r1[1]};
                    const int db = (nb >> 5) & 3;
                    char* base = ws + WS_KVB + (size_t)head * 1048576 + tile * 32768 + 16384 + ((kb * 2 + th) * 4 + db) * 1024;
                    st16(base + ((g >> 1) * 16 + c + 32 * (g & 1)) * 16, w);
                } else {
                    auto r0 = __builtin_amdgcn_permlane16_swap(a0, b0, false, false);
                    auto r1 = __builtin_amdgcn_permlane16_swap(a1, b1, false, false);
                    const u32x4 w = {r0[0], r1[0], r0[1], r1[1]};
                    const int ks = ((nb >> 4) & 7) + (g & 1);
                    char* base = EP == EP_BK ? ws + WS_KVB + (size_t)head * 1048576 + tile * 32768 + (kb * 8 + ks) * 1024
                                             : ws + WS_QB + (size_t)((head * 64 + tb) * 8 + ks) * 1024;
                    st16(base + (th * 16 + c + 32 * (g >> 1)) * 16, w);
                }
            }
        }
        return;
    }
#pragma unroll
    for (int mt = 0; mt < 8; ++mt) {
        const int tb = (m0 + wm * 128 + mt * 16) >> 5;
        const int tile = tb >> 1, kb = tb & 1, th = mt & 1;
#pragma unroll
        for (int nt = 0; nt < 4; ++nt) {
            const int nb = n0 + wn * 64 + nt * 16;
            if (nb >= nlim) continue;
            const f32x4 a = acc[mt][nt];
            if constexpr (EP == EP_BK || EP == EP_BQ || EP == EP_BV) {
            } else if constexpr (EP == EP_AQK) {
                const bool isq = nb < 640;
                const float sc = isq ? CA : 1.0f;
                const int r = th * 16 + c;
                const int n = nb - (isq ? 0 : 640) + 4 * g;
                const int head = n / 20, d0 = n - head * 20;
                char* base = isq ? ws + WS_QA + (size_t)((head * 64 + tb) * 2) * 1024
                                 : ws + WS_KVA + (size_t)head * 262144 + tile * 8192 + kb * 2048;
                char* dst = base + (d0 >> 4) * 1024 + (r + 32 * ((d0 >> 3) & 1)) * 16 + (d0 & 4) * 2;
                u32x2 w;
                w[0] = pack2(a[0] * sc, a[1] * sc);
                w[1] = pack2(a[2] * sc, a[3] * sc);
                *(u32x2*)dst = w;
                if (d0 == 16) {
                    *(u32x2*)(dst + 8) = (u32x2){isq ? 0u : 0x3F803F80u, 0u};
                    *(u32x4*)(base + 1024 + (r + 32) * 16) = (u32x4){0u, 0u, 0u, 0u};
                }
            } else {
                const int n = nb - nsec + c;
                const int head = n / 20, d = n - head * 20;
                char* dst = ws + WS_KVA + (size_t)head * 262144 + tile * 8192 + 4096 + (kb * 2 + th) * 1024 + (d + 32 * (g & 1)) * 16 + (g >> 1) * 8;
                u32x2 w;
                w[0] = pack2(a[0], a[1]);
                w[1] = pack2(a[2], a[3]);
                *(u32x2*)dst = w;
                if (d == 19) *(u32x2*)(dst + 16) = (u32x2){0x3F803F80u, 0x3F803F80u};
            }
        }
    }
}

__global__ __launch_bounds__(512, 2) void k1_proj(const __hip_bfloat16* __restrict__ Ax, const __hip_bfloat16* __restrict__ Awqkv,
                                                  const __hip_bfloat16* __restrict__ Bx, const __hip_bfloat16* __restrict__ Bckv,
                                                  const __hip_bfloat16* __restrict__ Bwq, const __hip_bfloat16* __restrict__ Bwuk,
                                                  const __hip_bfloat16* __restrict__ Bwuv, const __hip_bfloat16* __restrict__ Awo,
                                                  const __hip_bfloat16* __restrict__ Bwo, char* __restrict__ ws) {
    extern __shared__ __attribute__((aligned(16))) char smem[];
    LAS char* lds = (LAS char*)smem;
    const int tid = threadIdx.x, lane = tid & 63, wid = __builtin_amdgcn_readfirstlane(tid >> 6);
    const int bid = blockIdx.x;
    const int m0 = ((bid >> 3) & 7) * 256;
    const int grp = bid >> 6, c8 = bid & 7;
    const int cc = c8 + 8 * (grp & 1);
    const bool has2 = grp < 3;
    G1Src s1, s2;
    if (grp < 2) {
        g1_src(s1, Bckv, Bwuk, 512, 4096, m0, cc * 256, 4096, wid, lane, false);
        g1_src(s2, Bx, Bwq, 640, 4096, m0, cc * 256, 4096, wid, lane, false);
    } else if (grp == 2) {
        g1_src(s1, Bckv, Bwuv, 512, 4096, m0, c8 * 256, 4096, wid, lane, false);
        g1_src(s2, Bckv, Bwuv, 512, 4096, m0, (c8 + 8) * 256, 4096, wid, lane, false);
    } else {
        g1_src(s1, Ax, Awqkv, 640, 1920, m0, c8 * 256, c8 < 5 ? 1280 : 1920, wid, lane, false);
        s2 = s1;
    }

    f32x4 acc[8][4];
    bf16x8 xf[2][4], wf[2][4];
    const unsigned lds0 = (unsigned)(uintptr_t)lds;
    if (bid == 255 && tid < 40) ((unsigned*)(ws + WS_CNT))[tid * 64] = 0u;
    STAMP(0, 0);
    set_m0(lds0 + wid * 8192 + 4096);
    sfor<0, 8>([&](auto ic) { g1_piece<decltype(ic)::value>(s1, 0); });
    set_m0(lds0 + 65536 + wid * 8192 + 4096);
    sfor<0, 8>([&](auto ic) { g1_piece<decltype(ic)::value>(s1, 1); });
    WAIT_VM(8);
    BARRIER();
    STAMP(0, 1);
    {
        set_m0(lds0 + 131072 + wid * 256);
        const unsigned gw = (unsigned)(bid * 8 + wid) * 64 + lane;
#define PF(ptr, nbytes) do { constexpr unsigned R_ = 131072u / ((nbytes) / 128u);                                     \
            const unsigned ln_ = gw / R_;                                                                                \
            const unsigned off_ = (gw % R_ == 0u && ln_ * 128u < (nbytes)) ? ln_ * 128u : 0u;                             \
            asm volatile("global_load_lds_dword %0, %1" :: "v"(off_), "s"((const char*)(ptr)) : "memory"); } while (0)
        PF(Bx, 2048u * 640u * 2u); PF(Bwq, 640u * 4096u * 2u); PF(Bwuv, 512u * 4096u * 2u);
        PF(Bwo, 4096u * 640u * 2u); PF(Awo, 640u * 640u * 2u);
#undef PF
    }
    if (grp < 2 || (grp == 3 && c8 < 5)) {
        g1_loop<true, 4>(acc, xf, wf, s1, s2, has2, true, lds, wid, lane);
        if (grp < 2) g1_epi<EP_BK>(acc, m0, cc * 256, 4096, 0, ws, wid, lane);
        else g1_epi<EP_AQK>(acc, m0, c8 * 256, 1280, 0, ws, wid, lane);
    } else {
        g1_loop<false, 4>(acc, xf, wf, s1, s2, has2, true, lds, wid, lane);
        if (grp == 2) g1_epi<EP_BV>(acc, m0, c8 * 256, 4096, 0, ws, wid, lane);
        else g1_epi<EP_AV>(acc, m0, c8 * 256, 1920, 1280, ws, wid, lane);
    }
    STAMP(0, 2);
    if (grp < 2) {
        g1_loop<true, 4>(acc, xf, wf, s2, s2, false, false, lds, wid, lane);
        g1_epi<EP_BQ>(acc, m0, cc * 256, 4096, 0, ws, wid, lane);
    } else if (grp == 2) {
        g1_loop<false, 4>(acc, xf, wf, s2, s2, false, false, lds, wid, lane);
        g1_epi<EP_BV>(acc, m0, (c8 + 8) * 256, 4096, 0, ws, wid, lane);
    }
    STAMP(0, 3);
}

constexpr int SLOT = 40960;
constexpr int NSLOT = 4;
constexpr int K2_LDS = NSLOT * SLOT;
constexpr int NIT = 34;

__global__ __launch_bounds__(512, 2) void k2_attn(char* __restrict__ ws) {
    extern __shared__ __attribute__((aligned(16))) char smem[];
    LAS char* lds = (LAS char*)smem;
    const int tid = threadIdx.x, lane = tid & 63, wid = __builtin_amdgcn_readfirstlane(tid >> 6);
    const int r = lane & 31, h = lane >> 5;
    const int g = blockIdx.x, xcd = g & 7, idx = g >> 3;
    const int head = xcd * 4 + (idx >> 3), x = idx & 7;
    const char* kvb = ws + WS_KVB + (size_t)head * 1048576;
    const char* kva = ws + WS_KVA + (size_t)head * 262144;
    const int n1 = 32 - 2 * x;
    const unsigned lbase = (unsigned)(uintptr_t)lds + lane * 16;

    STAMP(1, 0);
    const unsigned lds0 = (unsigned)(uintptr_t)lds;
    const unsigned voff = lane * 16;
    const int wa = wid & 3;
    const char* sbB = kvb;
    const char* sbA = kva;
#define K2_M0(tt)                                                                           \
    do {                                                                                    \
        const int t3_ = (tt) < NIT ? (tt) : NIT - 1;                                        \
        const int tb3_ = t3_ < n1 ? t3_ : t3_ - n1;                                         \
        const int ta3_ = t3_ < 32 ? t3_ : 31;                                               \
        sbB = kvb + (size_t)tb3_ * 32768 + wid * 4096 + 2048;                               \
        sbA = kva + (size_t)ta3_ * 8192 + wid * 1024 - 2048;                                \
        set_m0(lds0 + ((tt) < NIT ? ((tt) & 3) : 2 + ((tt) & 1)) * SLOT + wid * 5120 + 2048); \
    } while (0)
#define K2_DMA(i) do { SBAR(); if constexpr ((i) < 4) dma_s<(i) * 1024 - 2048>(voff, sbB); else dma_s<2048>(voff, sbA); SBAR(); } while (0)
#define K2_STAGE_ALL(tt) do { K2_M0(tt); K2_DMA(0); K2_DMA(1); K2_DMA(2); K2_DMA(3); K2_DMA(4); } while (0)
#define BOFF(f) (((f) >> 2) * 5120 + ((f) & 3) * 1024)
#define AOFF(a) ((a) * 5120 + 4096)
    K2_STAGE_ALL(0);
    K2_STAGE_ALL(1);

#define K2_SYNC(it) do { WAIT_VM(5); BARRIER(); K2_M0((it) + 3); } while (0)

    if (wid < 4) {
        bf16x8 fr[8];
        K2_SYNC(-1);
        STAMP(1, 1);
        sfor<0, 8>([&](auto ic) { constexpr int j = decltype(ic)::value; lds_rd<BOFF((j & 1) * 8 + (j >> 1))>(fr[j], lbase); });
        K2_DMA(0); K2_DMA(1);
        PRIO(1);
        int it = 0;
        for (int blk = 0; blk < 2; ++blk) {
            const int qb = blk == 0 ? 15 - x : x;
            const int nt = 2 * qb + 2;
            const int q0 = qb * 128 + wid * 32;
            const int tb32 = q0 >> 5;
            bf16x8 qf[8];
            const char* qp = ws + WS_QB + (size_t)((head * 64 + tb32) * 8) * 1024 + lane * 16;
#pragma unroll
            for (int ks = 0; ks < 8; ++ks) qf[ks] = *(const bf16x8*)(qp + ks * 1024);
            f32x16 o[4];
#pragma unroll
            for (int db = 0; db < 4; ++db) o[db] = f32x16{};
            f32x16 negm = f32x16{};
            float l = 0.f;
            for (int t = 0; t < nt; ++t, ++it) {
                const int rel = (q0 - 64 * t) >> 5;
                const unsigned ac = lbase + (it & 3) * SLOT;
                const unsigned an = lbase + ((it + 1) & 3) * SLOT;
                f32x16 s0, s1;
                SEG(0);
                sfor<0, 16>([&](auto ic) {
                    constexpr int j = decltype(ic)::value, ks = j >> 1;
                    lds_wait<7>(fr[j & 7]);
                    if constexpr (j == 0) s0 = MFMA32(fr[0], qf[0], negm);
                    else if constexpr (j == 1) s1 = MFMA32(fr[1], qf[0], negm);
                    else if constexpr ((j & 1) == 0) s0 = MFMA32(fr[j & 7], qf[ks], s0);
                    else s1 = MFMA32(fr[j & 7], qf[ks], s1);
                    constexpr int jn = j + 8;
                    SBAR();
                    if constexpr (jn < 16) lds_rd<BOFF((jn & 1) * 8 + (jn >> 1))>(fr[j & 7], ac);
                    else lds_rd<BOFF(jn)>(fr[j & 7], ac);
                    if constexpr (j == 2) K2_DMA(2);
                    if constexpr (j == 8) K2_DMA(3);
                    if constexpr (j == 14) K2_DMA(4);
                    SBAR();
                });
#define B_MASK()                                                                                        \
    do {                                                                                                \
        if (rel <= 1) {                                                                                 \
            const int lim = 32 * rel + r - 4 * h;                                                       \
            _Pragma("unroll") for (int i = 0; i < 16; ++i) {                                            \
                const int kr = (i & 3) + 8 * (i >> 2);                                                  \
                if (kr > lim) s0[i] = -__builtin_inff();                                                \
                if (kr + 32 > lim) s1[i] = -__builtin_inff();                                           \
            }                                                                                           \
        }                                                                                               \
    } while (0)
                SEG(1);
                PRIO(0);
                B_MASK();
                float ps = 0.f;
                if (t > 0) {
#pragma unroll
                    for (int i = 0; i < 16; ++i) { s0[i] = __builtin_amdgcn_exp2f(s0[i]); ps += s0[i]; }
#pragma unroll
                    for (int i = 0; i < 16; ++i) { s1[i] = __builtin_amdgcn_exp2f(s1[i]); ps += s1[i]; }
                }
                if (t == 0 || __any(!(ps <= 65536.f))) {
                    if (t > 0) {
                        LAS char* sl = lds + (it & 3) * SLOT + lane * 16;
                        s0 = negm; s1 = negm;
#pragma unroll 1
                        for (int ks = 0; ks < 8; ++ks) {
                            bf16x8 qq = qf[0];
#pragma unroll
                            for (int u = 1; u < 8; ++u) qq = (ks == u) ? qf[u] : qq;
                            s0 = MFMA32(*(LAS bf16x8*)(sl + (ks >> 2) * 5120 + (ks & 3) * 1024), qq, s0);
                            s1 = MFMA32(*(LAS bf16x8*)(sl + 10240 + (ks >> 2) * 5120 + (ks & 3) * 1024), qq, s1);
                        }
                        B_MASK();
                    }
                    float mx = s0[0];
#pragma unroll
                    for (int i = 1; i < 16; ++i) mx = fmaxf(mx, s0[i]);
#pragma unroll
                    for (int i = 0; i < 16; ++i) mx = fmaxf(mx, s1[i]);
                    auto rr = __builtin_amdgcn_permlane32_swap(__float_as_uint(mx), __float_as_uint(mx), false, false);
                    mx = fmaxf(__uint_as_float(rr[0]), __uint_as_float(rr[1]));
                    float delta = mx;
                    if (t > 0) {
                        delta = fmaxf(mx, 0.f);
                        const float alpha = __builtin_amdgcn_exp2f(-delta);
                        l *= alpha;
#pragma unroll
                        for (int db = 0; db < 4; ++db)
#pragma unroll
                            for (int i = 0; i < 16; ++i) o[db][i] *= alpha;
                    }
                    const float nm = negm[0] - delta;
#pragma unroll
                    for (int i = 0; i < 16; ++i) negm[i] = nm;
                    ps = 0.f;
#pragma unroll
                    for (int i = 0; i < 16; ++i) { s0[i] = __builtin_amdgcn_exp2f(s0[i] - delta); ps += s0[i]; }
#pragma unroll
                    for (int i = 0; i < 16; ++i) { s1[i] = __builtin_amdgcn_exp2f(s1[i] - delta); ps += s1[i]; }
                }
                l += ps;
                bf16x8 pf[4];
                pf[0] = __builtin_bit_cast(bf16x8, pack8(s0, 0));
                pf[1] = __builtin_bit_cast(bf16x8, pack8(s0, 1));
                pf[2] = __builtin_bit_cast(bf16x8, pack8(s1, 0));
                pf[3] = __builtin_bit_cast(bf16x8, pack8(s1, 1));
                SEG(2);
                if (it + 1 < NIT) { K2_SYNC(it); } else { K2_M0(it + 3); }
                SEG(3);
                PRIO(1);
                sfor<0, 16>([&](auto ic) {
                    constexpr int j = decltype(ic)::value, s4 = j >> 2, db = j & 3;
                    lds_wait<7>(fr[j & 7]);
                    o[db] = MFMA32(fr[j & 7], pf[s4], o[db]);
                    SBAR();
                    if constexpr (j < 8) lds_rd<BOFF(24 + j)>(fr[j & 7], ac);
                    else { constexpr int jn = j - 8; lds_rd<BOFF((jn & 1) * 8 + (jn >> 1))>(fr[j & 7], an); }
                    if constexpr (j == 4) K2_DMA(0);
                    if constexpr (j == 10) K2_DMA(1);
                    SBAR();
                });
                SEG(4);
            }
            auto rr = __builtin_amdgcn_permlane32_swap(__float_as_uint(l), __float_as_uint(l), false, false);
            const float inv = 1.0f / (__uint_as_float(rr[0]) + __uint_as_float(rr[1]));
            char* cp = ws + WS_CTXB + (size_t)(tb32 * 256 + head * 8) * 1024 + lane * 16;
#pragma unroll
            for (int db = 0; db < 4; ++db) {
                st16(cp + (2 * db) * 1024, pack8s(o[db], 0, inv));
                st16(cp + (2 * db + 1) * 1024, pack8s(o[db], 1, inv));
            }
        }
        WAIT_VM(0);
        STAMP(1, 2);
#undef B_MASK
    } else {
        const int tb32 = x * 8 + wa * 2;
        bf16x8 qf[2][2];
        const char* qp = ws + WS_QA + (size_t)((head * 64 + tb32) * 2) * 1024 + lane * 16;
#pragma unroll
        for (int qt = 0; qt < 2; ++qt)
#pragma unroll
            for (int ks = 0; ks < 2; ++ks) qf[qt][ks] = *(const bf16x8*)(qp + (qt * 2 + ks) * 1024);
        f32x16 o[2];
        o[0] = f32x16{}; o[1] = f32x16{};
        float negm[2] = {0.f, 0.f};
        bf16x8 kf[4], vf[4];
        f32x16 s[2][2];
        K2_SYNC(-1);
        lds_rd<AOFF(0)>(kf[0], lbase); lds_rd<AOFF(1)>(kf[1], lbase); lds_rd<AOFF(2)>(kf[2], lbase); lds_rd<AOFF(3)>(kf[3], lbase);
        for (int it = 0; it <= 32; ++it) {
            SEG(0);
            if (it > 0) {
                u32x4 pf[2][2][2];
                unsigned chk = 0;
                sfor<0, 4>([&](auto bc) {
                    constexpr int blk = decltype(bc)::value, kb = blk >> 1, qt = blk & 1;
                    f32x16 e;
#pragma unroll
                    for (int i = 0; i < 8; ++i) e[i] = __builtin_amdgcn_exp2f(s[kb][qt][i]);
                    K2_DMA(blk);
#pragma unroll
                    for (int i = 8; i < 16; ++i) e[i] = __builtin_amdgcn_exp2f(s[kb][qt][i]);
                    if constexpr (blk == 3) K2_DMA(4);
                    pf[kb][qt][0] = pack8(e, 0);
                    pf[kb][qt][1] = pack8(e, 1);
                    chk |= pf[kb][qt][0][0] | pf[kb][qt][0][1] | pf[kb][qt][0][2] | pf[kb][qt][0][3];
                    chk |= pf[kb][qt][1][0] | pf[kb][qt][1][1] | pf[kb][qt][1][2] | pf[kb][qt][1][3];
                });
                const bool first = (it == 1);
                if (first || __any((chk & 0x40004000u) != 0u)) {
#pragma unroll
                    for (int qt = 0; qt < 2; ++qt) {
                        float mx = s[0][qt][0];
#pragma unroll
                        for (int i = 1; i < 16; ++i) mx = fmaxf(mx, s[0][qt][i]);
#pragma unroll
                        for (int i = 0; i < 16; ++i) mx = fmaxf(mx, s[1][qt][i]);
                        auto rr = __builtin_amdgcn_permlane32_swap(__float_as_uint(mx), __float_as_uint(mx), false, false);
                        mx = fmaxf(__uint_as_float(rr[0]), __uint_as_float(rr[1]));
                        float delta = mx + 10.f;
                        if (!first) delta = fmaxf(delta, 0.f);
                        const float tgt = negm[qt] - delta;
                        const unsigned hi = pack2(tgt, 0.f) & 0xFFFFu;
                        const float hif = __uint_as_float(hi << 16);
                        const unsigned lo = pack2(tgt - hif, 0.f) & 0xFFFFu;
                        const float neff = hif + __uint_as_float(lo << 16);
                        delta = negm[qt] - neff;
                        negm[qt] = neff;
                        {
                            u32x4 qq = __builtin_bit_cast(u32x4, qf[qt][1]);
                            qq[2] = h ? 0u : (hi | (lo << 16));
                            qf[qt][1] = __builtin_bit_cast(bf16x8, qq);
                        }
                        if (!first) {
                            const float alpha = __builtin_amdgcn_exp2f(-delta);
#pragma unroll
                            for (int i = 0; i < 16; ++i) o[qt][i] *= alpha;
                        }
#pragma unroll
                        for (int kb = 0; kb < 2; ++kb) {
                            f32x16 e;
#pragma unroll
                            for (int i = 0; i < 16; ++i) e[i] = __builtin_amdgcn_exp2f(s[kb][qt][i] - delta);
                            pf[kb][qt][0] = pack8(e, 0);
                            pf[kb][qt][1] = pack8(e, 1);
                        }
                    }
                }
                SEG(1);
                PRIO(1);
                lds_wait2<4>(vf[0], vf[1]);
                lds_wait2<4>(vf[2], vf[3]);
#pragma unroll
                for (int kb = 0; kb < 2; ++kb)
#pragma unroll
                    for (int s2 = 0; s2 < 2; ++s2)
#pragma unroll
                        for (int qt = 0; qt < 2; ++qt)
                            o[qt] = MFMA32(vf[kb * 2 + s2], __builtin_bit_cast(bf16x8, pf[kb][qt][s2]), o[qt]);
            } else {
                K2_DMA(0); K2_DMA(1); K2_DMA(2); K2_DMA(3); K2_DMA(4);
            }
            SEG(2);
            lds_wait2<0>(kf[0], kf[1]);
            lds_wait2<0>(kf[2], kf[3]);
#pragma unroll
            for (int kb = 0; kb < 2; ++kb)
#pragma unroll
                for (int qt = 0; qt < 2; ++qt) {
                    s[kb][qt] = MFMA32(kf[kb * 2], qf[qt][0], f32x16{});
                    s[kb][qt] = MFMA32(kf[kb * 2 + 1], qf[qt][1], s[kb][qt]);
                }
            SBAR();
            if (it == 32) STAMPW(1, 3, 4);
            SEG(3);
            PRIO(0);
            K2_SYNC(it);
            SEG(4);
            const unsigned ac = lbase + (it & 3) * SLOT;
            const unsigned an = lbase + ((it + 1) & 3) * SLOT;
            lds_rd<AOFF(4)>(vf[0], ac); lds_rd<AOFF(5)>(vf[1], ac); lds_rd<AOFF(6)>(vf[2], ac); lds_rd<AOFF(7)>(vf[3], ac);
            lds_rd<AOFF(0)>(kf[0], an); lds_rd<AOFF(1)>(kf[1], an); lds_rd<AOFF(2)>(kf[2], an); lds_rd<AOFF(3)>(kf[3], an);
        }
        WAIT_VM(0);
#pragma unroll
        for (int qt = 0; qt < 2; ++qt) {
            const unsigned lv = __float_as_uint(o[qt][8]);
            auto rr = __builtin_amdgcn_permlane32_swap(lv, lv, false, false);
            const float lsum = __uint_as_float(rr[1]);
            const float inv = 1.0f / lsum;
            char* cp = ws + WS_CTXA + (size_t)((tb32 + qt) * 40) * 1024;
#pragma unroll
            for (int gq = 0; gq < 3; ++gq) {
                const int d0 = 8 * gq + 4 * h;
                if (d0 < 20) {
                    const int k = head * 20 + d0;
                    u32x2 w;
                    w[0] = pack2(o[qt][4 * gq + 0] * inv, o[qt][4 * gq + 1] * inv);
                    w[1] = pack2(o[qt][4 * gq + 2] * inv, o[qt][4 * gq + 3] * inv);
                    *(u32x2*)(cp + (k >> 4) * 1024 + (r + 32 * ((k >> 3) & 1)) * 16 + (k & 4) * 2) = w;
                }
            }
        }
    }
}

constexpr int G3_STAGE = 49152;
constexpr int G3_LDS = 3 * G3_STAGE;

__global__ __launch_bounds__(512, 2) void k3_oproj(const __hip_bfloat16* __restrict__ Awo, const __hip_bfloat16* __restrict__ Bwo,
                                                   char* __restrict__ ws, __hip_bfloat16* __restrict__ out) {
    extern __shared__ __attribute__((aligned(16))) char smem[];
    LAS char* lds = (LAS char*)smem;
    const int tid = threadIdx.x, lane = tid & 63, wid = __builtin_amdgcn_readfirstlane(tid >> 6);
    const int wm = wid >> 1, wn = wid & 1, h = lane >> 5;
    const int bid = blockIdx.x;
    const bool isB = bid < 200;
    int mt, nt, sp, kt0, nkt, kstot;
    const char* Xc; const char* Wb;
    if (isB) {
        mt = bid & 7; const int rest = bid >> 3; nt = rest % 5; sp = rest / 5;
        kt0 = sp * 13; nkt = sp == 4 ? 12 : 13; kstot = 256;
        Xc = ws + WS_CTXB; Wb = (const char*)Bwo;
    } else {
        const int b2 = bid - 200; mt = b2 & 7; nt = b2 >> 3; sp = 5;
        kt0 = 0; nkt = 10; kstot = 40;
        Xc = ws + WS_CTXA; Wb = (const char*)Awo;
    }
    unsigned woff[2];
#pragma unroll
    for (int i = 0; i < 2; ++i) {
        const int k = (wid * 2 + i) * 4 + (lane >> 4);
        const int p = lane & 15, b = (p >> 2) ^ (k & 3);
        woff[i] = (unsigned)(((kt0 * 64 + k) * 640 + nt * 128 + (b * 4 + (p & 3)) * 8) * 2);
    }
    const unsigned lds0 = (unsigned)(uintptr_t)lds;
    const char* xsb = Xc + (size_t)((mt * 8 + wid) * kstot + kt0 * 4) * 1024 + 4096;
    const unsigned voff = lane * 16;
    woff[1] -= 1024;
    auto piece = [&](int kt, auto ic) {
        constexpr int i = decltype(ic)::value;
        if constexpr (i < 4) dma_s<i * 1024 - 4096>(voff, xsb + (size_t)kt * 4096);
        else dma_s<(i - 4) * 1024>(woff[i - 4], Wb + (size_t)kt * (64u * 640u * 2u));
    };
    const int q = (lane >> 2) & 3, p4 = lane & 3, g16 = (lane >> 4) & 1;
    const int r0 = isB ? 4 * h : 8 * h, r1 = isB ? 8 + 4 * h : 8 * h + 4;
    int wb0[2], wb1[2];
#pragma unroll
    for (int ni = 0; ni < 2; ++ni) {
        const int b = wn * 2 + ni;
        const int c = ((b ^ q) * 64) + g16 * 32 + p4 * 8;
        wb0[ni] = (r0 >> 3) * 6144 + 4096 + ((r0 & 7) + q) * 256 + c;
        wb1[ni] = (r1 >> 3) * 6144 + 4096 + ((r1 & 7) + q) * 256 + c;
    }
    const int xb0 = wm * 2 * 6144 + lane * 16;
    f32x16 acc[2][2];
#pragma unroll
    for (int mi = 0; mi < 2; ++mi)
#pragma unroll
        for (int ni = 0; ni < 2; ++ni) acc[mi][ni] = f32x16{};

    bf16x8 xf[2][4][2], wf[2][4][2];
#define G3_RD(set, bb)                                                                                           \
    do {                                                                                                         \
        _Pragma("unroll") for (int ks = 0; ks < 4; ++ks) {                                                       \
            _Pragma("unroll") for (int ni = 0; ni < 2; ++ni)                                                     \
                wf[set][ks][ni] = tr_frag((bb) + wb0[ni] + ks * 12288, (bb) + wb1[ni] + ks * 12288);             \
            _Pragma("unroll") for (int mi = 0; mi < 2; ++mi)                                                     \
                xf[set][ks][mi] = *(LAS bf16x8*)((bb) + xb0 + mi * 6144 + ks * 1024);                            \
        }                                                                                                        \
    } while (0)
#define G3_STEP(set, kt)                                                                                         \
    do {                                                                                                         \
        if ((kt) + 2 < nkt) { WAIT_VM(6); } else { WAIT_VM(0); }                                                 \
        LGKM0();                                                                                                 \
        BARRIER();                                                                                               \
        const int b0_ = (kt) % 3, b1_ = ((kt) + 1) % 3;                                                          \
        set_m0(lds0 + b0_ * G3_STAGE + wid * 6144 + 4096);                                                       \
        if ((kt) + 1 < nkt) G3_RD((set) ^ 1, lds + b1_ * G3_STAGE);                                              \
        SBAR();                                                                                                  \
        const bool on_ = (kt) + 3 < nkt;                                                                         \
        sfor<0, 8>([&](auto jc) {                                                                                \
            constexpr int j = decltype(jc)::value, ks = j >> 1, mi = j & 1;                                      \
            _Pragma("unroll") for (int ni = 0; ni < 2; ++ni)                                                     \
                acc[mi][ni] = MFMA32(wf[set][ks][ni], xf[set][ks][mi], acc[mi][ni]);                             \
            if constexpr (j < 6) { SBAR(); if (on_) piece((kt) + 3, jc); SBAR(); }                               \
        });                                                                                                      \
        SBAR();                                                                                                  \
    } while (0)
    STAMP(2, 0);
#pragma unroll
    for (int t = 0; t < 3; ++t) {
        set_m0(lds0 + t * G3_STAGE + wid * 6144 + 4096);
        sfor<0, 6>([&](auto ic) { piece(t, ic); });
    }
    WAIT_VM(12);
    BARRIER();
    STAMP(2, 1);
    G3_RD(0, lds);
    int kt = 0;
#pragma unroll 1
    for (; kt + 1 < nkt; kt += 2) { G3_STEP(0, kt); G3_STEP(1, kt + 1); }
    if (kt < nkt) G3_STEP(0, kt);
#undef G3_RD
#undef G3_STEP
    STAMP(2, 2);
    const int r = lane & 31;
    if (!isB) {
#pragma unroll
        for (int mi = 0; mi < 2; ++mi)
#pragma unroll
            for (int ni = 0; ni < 2; ++ni) {
                char* op = (char*)out + (size_t)((mt * 8 + wm * 2 + mi) * 32 + r) * 2560 + ((nt * 4 + wn * 2 + ni) * 32 + 4 * h) * 2;
#pragma unroll
                for (int gq = 0; gq < 4; ++gq) {
                    u32x2 o;
                    o[0] = pack2(acc[mi][ni][4 * gq], acc[mi][ni][4 * gq + 1]);
                    o[1] = pack2(acc[mi][ni][4 * gq + 2], acc[mi][ni][4 * gq + 3]);
                    *(u32x2*)(op + gq * 16) = o;
                }
            }
        STAMP(2, 3);
        return;
    }
#pragma unroll
    for (int mi = 0; mi < 2; ++mi)
#pragma unroll
        for (int ni = 0; ni < 2; ++ni) {
            const int st = (wm * 2 + mi) * 4 + wn * 2 + ni;
            if (st % 5 == sp) continue;
            const int mt32 = mt * 8 + wm * 2 + mi, nt32 = nt * 4 + wn * 2 + ni;
            char* dst = ws + WS_PART + (size_t)sp * SLAB + (size_t)(mt32 * 20 + nt32) * 4096 + lane * 16;
#pragma unroll
            for (int gq = 0; gq < 4; ++gq) {
                f32x4 v = {acc[mi][ni][4 * gq], acc[mi][ni][4 * gq + 1], acc[mi][ni][4 * gq + 2], acc[mi][ni][4 * gq + 3]};
                asm volatile("global_store_dwordx4 %0, %1, off sc1\n\ts_nop 1" :: "v"(dst + gq * 1024), "v"(v) : "memory");
            }
        }
    asm volatile("s_waitcnt vmcnt(0)" ::: "memory");
    BARRIER();
    unsigned* cnt = (unsigned*)(ws + WS_CNT) + (mt * 5 + nt) * 64;
    if (wid == 0) {
        if (lane == 0) __hip_atomic_fetch_add(cnt, 1u, __ATOMIC_RELAXED, __HIP_MEMORY_SCOPE_AGENT);
        for (unsigned spins = 0; spins < (1u << 24); ++spins) {
            const unsigned c = __builtin_amdgcn_readfirstlane(__hip_atomic_load(cnt, __ATOMIC_RELAXED, __HIP_MEMORY_SCOPE_AGENT));
            if (c >= (unsigned)NSPLIT) break;
            __builtin_amdgcn_s_sleep(1);
        }
    }
    BARRIER();
    const auto rsrc = __builtin_amdgcn_make_buffer_rsrc((void*)(ws + WS_PART), 0, (int)(NSPLIT * SLAB), 0x00020000);
#pragma unroll
    for (int mi = 0; mi < 2; ++mi)
#pragma unroll
        for (int ni = 0; ni < 2; ++ni) {
            const int st = (wm * 2 + mi) * 4 + wn * 2 + ni;
            if (st % 5 != sp) continue;
            const int mt32 = mt * 8 + wm * 2 + mi, nt32 = nt * 4 + wn * 2 + ni;
            const int off = (mt32 * 20 + nt32) * 4096 + lane * 16;
            u32x4 v[4][4];
#pragma unroll
            for (int s = 0; s < 4; ++s) {
                const int so = (s < sp ? s : s + 1) * (int)SLAB;
#pragma unroll
                for (int gq = 0; gq < 4; ++gq) v[s][gq] = __builtin_amdgcn_raw_buffer_load_b128(rsrc, off + gq * 1024, so, 16);
            }
            char* op = (char*)out + (size_t)(mt32 * 32 + r) * 2560 + (640 + nt32 * 32 + 4 * h) * 2;
#pragma unroll
            for (int gq = 0; gq < 4; ++gq) {
                f32x4 t = {acc[mi][ni][4 * gq], acc[mi][ni][4 * gq + 1], acc[mi][ni][4 * gq + 2], acc[mi][ni][4 * gq + 3]};
                t += (__builtin_bit_cast(f32x4, v[0][gq]) + __builtin_bit_cast(f32x4, v[1][gq])) +
                     (__builtin_bit_cast(f32x4, v[2][gq]) + __builtin_bit_cast(f32x4, v[3][gq]));
                u32x2 o;
                o[0] = pack2(t[0], t[1]);
                o[1] = pack2(t[2], t[3]);
                *(u32x2*)(op + gq * 16) = o;
            }
        }
    STAMP(2, 3);
}

extern "C" void kernel_launch(void* const* d_in, const int* in_sizes, int n_in,
                              void* d_out, int out_size, void* d_ws, size_t ws_size,
                              hipStream_t stream) {
    (void)in_sizes; (void)n_in; (void)out_size; (void)ws_size;
    const __hip_bfloat16* Ax = (const __hip_bfloat16*)d_in[0];
    const __hip_bfloat16* Awqkv = (const __hip_bfloat16*)d_in[1];
    const __hip_bfloat16* Awo = (const __hip_bfloat16*)d_in[2];
    const __hip_bfloat16* Bx = (const __hip_bfloat16*)d_in[3];
    const __hip_bfloat16* Bckv = (const __hip_bfloat16*)d_in[4];
    const __hip_bfloat16* Bwq = (const __hip_bfloat16*)d_in[5];
    const __hip_bfloat16* Bwuk = (const __hip_bfloat16*)d_in[6];
    const __hip_bfloat16* Bwuv = (const __hip_bfloat16*)d_in[7];
    const __hip_bfloat16* Bwo = (const __hip_bfloat16*)d_in[8];
    char* ws = (char*)d_ws;
    (void)hipFuncSetAttribute((const void*)k1_proj, hipFuncAttributeMaxDynamicSharedMemorySize, G1_LDS);
    (void)hipFuncSetAttribute((const void*)k2_attn, hipFuncAttributeMaxDynamicSharedMemorySize, K2_LDS);
    (void)hipFuncSetAttribute((const void*)k3_oproj, hipFuncAttributeMaxDynamicSharedMemorySize, G3_LDS);
    k1_proj<<<256, 512, G1_LDS, stream>>>(Ax, Awqkv, Bx, Bckv, Bwq, Bwuk, Bwuv, Awo, Bwo, ws);
    k2_attn<<<256, 512, K2_LDS, stream>>>(ws);
    k3_oproj<<<240, 512, G3_LDS, stream>>>(Awo, Bwo, ws, (__hip_bfloat16*)d_out);
}
```

```cpp
#include <hip/hip_runtime.h>
#include <hip/hip_bf16.h>
#include <stdint.h>
#include <type_traits>

typedef short s16x4 __attribute__((ext_vector_type(4)));
typedef short bf16x8 __attribute__((ext_vector_type(8)));
typedef float f32x16 __attribute__((ext_vector_type(16)));
typedef float f32x4 __attribute__((ext_vector_type(4)));
typedef float f32x2 __attribute__((ext_vector_type(2)));
typedef unsigned u32x4 __attribute__((ext_vector_type(4)));
typedef unsigned u32x2 __attribute__((ext_vector_type(2)));
typedef __bf16 bf16x2_t __attribute__((ext_vector_type(2)));
#define LAS __attribute__((address_space(3)))
#define DI __device__ __forceinline__
#define MFMA32(a, b, c) __builtin_amdgcn_mfma_f32_32x32x16_bf16((a), (b), (c), 0, 0, 0)
#define MFMA16(a, b, c) __builtin_amdgcn_mfma_f32_16x16x32_bf16((a), (b), (c), 0, 0, 0)
#define CFENCE() asm volatile("" ::: "memory")
#define BARRIER() do { CFENCE(); __builtin_amdgcn_s_barrier(); CFENCE(); } while (0)
#define WAIT_VM(n) asm volatile("s_waitcnt vmcnt(" #n ") lgkmcnt(0)" ::: "memory")
#define SBAR() __builtin_amdgcn_sched_barrier(0)

DI unsigned pack2(float a, float b) {
    f32x2 v = {a, b};
    bf16x2_t r = __builtin_convertvector(v, bf16x2_t);
    return __builtin_bit_cast(unsigned, r);
}
DI u32x4 pack8(const f32x16& x, int s) {
    u32x4 w;
    w[0] = pack2(x[8 * s + 0], x[8 * s + 1]);
    w[1] = pack2(x[8 * s + 2], x[8 * s + 3]);
    w[2] = pack2(x[8 * s + 4], x[8 * s + 5]);
    w[3] = pack2(x[8 * s + 6], x[8 * s + 7]);
    return w;
}
DI u32x4 pack8s(const f32x16& x, int s, float c) {
    u32x4 w;
    w[0] = pack2(x[8 * s + 0] * c, x[8 * s + 1] * c);
    w[1] = pack2(x[8 * s + 2] * c, x[8 * s + 3] * c);
    w[2] = pack2(x[8 * s + 4] * c, x[8 * s + 5] * c);
    w[3] = pack2(x[8 * s + 6] * c, x[8 * s + 7] * c);
    return w;
}
DI void glds16(const void* g, LAS char* l) {
    __builtin_amdgcn_global_load_lds((const unsigned*)g, (LAS unsigned*)l, 16, 0, 0);
}
DI bf16x8 tr_frag(LAS char* p0, LAS char* p1) {
    s16x4 lo = __builtin_amdgcn_ds_read_tr16_b64_v4i16((LAS s16x4*)p0);
    s16x4 hi = __builtin_amdgcn_ds_read_tr16_b64_v4i16((LAS s16x4*)p1);
    return __builtin_shufflevector(lo, hi, 0, 1, 2, 3, 4, 5, 6, 7);
}

#define STMODE 2
DI void st16(void* p, u32x4 v) {
    asm volatile("global_store_dwordx4 %0, %1, off sc1\n\ts_nop 1" :: "v"(p), "v"(v) : "memory");
}
template <int I, int N, class F> DI void sfor(F&& f) {
    if constexpr (I < N) { f(std::integral_constant<int, I>{}); sfor<I + 1, N>(f); }
}
template <int OFF> DI void lds_rd(bf16x8& d, unsigned addr) {
    asm volatile("ds_read_b128 %0, %1 offset:%2" : "=v"(d) : "v"(addr), "i"(OFF));
}
template <int N> DI void lds_wait(bf16x8& d) { asm volatile("s_waitcnt lgkmcnt(%1)" : "+v"(d) : "i"(N)); }
template <int N> DI void lds_wait2(bf16x8& d, bf16x8& e) { asm volatile("s_waitcnt lgkmcnt(%2)" : "+v"(d), "+v"(e) : "i"(N)); }
#define LGKM0() __builtin_amdgcn_s_waitcnt(0xC07F)
DI void set_m0(unsigned v) { asm volatile("s_mov_b32 m0, %0\n\ts_nop 0" :: "s"(v) : "memory"); }
template <int OFF> DI void dma_s(unsigned voff, const char* sbase) {
    asm volatile("global_load_lds_dwordx4 %0, %1 offset:%2" :: "v"(voff), "s"(sbase), "i"(OFF) : "memory");
}

#define DYNPRIO 0
#define PRIO(x) do { if (DYNPRIO) __builtin_amdgcn_s_setprio(x); } while (0)
#define ABL_NOPIECE 0
#define ABL_NOBAR 0
#define ABL_T0 0
#define ABL_K2 0
#define ABL_NODMA 0
#define ABL_NOMFMA 0
#define SEG(i) do { } while (0)
#define STAMP(kid, idx) do { } while (0)
#define STAMPW(kid, idx, w) do { } while (0)
constexpr size_t WS_KVB = 0;
constexpr size_t WS_QB = WS_KVB + (size_t)32 * 1048576;
constexpr size_t WS_KVA = WS_QB + (size_t)16 * 1048576;
constexpr size_t WS_QA = WS_KVA + (size_t)8 * 1048576;
constexpr size_t WS_CTXB = WS_QA + (size_t)4 * 1048576;
constexpr size_t WS_CTXA = WS_CTXB + (size_t)16 * 1048576;
constexpr size_t WS_PART = WS_CTXA + (size_t)4 * 1048576;
constexpr size_t WS_CNT = WS_PART + (size_t)32 * 1048576;
constexpr size_t WS_CNT2 = WS_CNT + 16384;
constexpr int NSPLIT = 5;
constexpr size_t SLAB = (size_t)64 * 20 * 4096;

constexpr float CB = 0.08838834764831845f * 1.4426950408889634f;
constexpr float CA = 0.22360679774997896f * 1.4426950408889634f;

#define PREFETCH 1
constexpr int G1_LDS = 2 * 65536 + 2048;
enum { EP_BK = 0, EP_BV = 1, EP_BQ = 2, EP_AQK = 3, EP_AV = 4 };

struct G1Src { unsigned xoff[4], woff[4]; const char* Xb; const char* Wb; unsigned wstep; int nkt; bool half; };
DI void g1_src(G1Src& s, const __hip_bfloat16* X, const __hip_bfloat16* W, int K, int ldw, int m0, int n0, int nlim, int wid, int lane, bool half) {
    s.half = half;
#pragma unroll
    for (int i = 0; i < 4; ++i) {
        const int row = (wid * 4 + i) * 8 + (lane >> 3);
        const int c = (lane & 7) ^ ((row >> 1) & 7);
        s.xoff[i] = (unsigned)(((m0 + row) * K + c * 8) * 2) + 4096 - i * 1024;
        const int k = (wid * 4 + i) * 2 + (lane >> 5);
        const int p = lane & 31, b = (p >> 2) ^ (k & 3);
        int col = n0 + (b * 4 + ((p & 3) ^ ((wid & 1) << 1))) * 8;
        col = col < nlim ? col : nlim - 8;
        s.woff[i] = (unsigned)((k * ldw + col) * 2) - i * 1024;
    }
    s.Xb = (const char*)X; s.Wb = (const char*)W; s.wstep = (unsigned)(64 * ldw * 2); s.nkt = K >> 6;
}
template <int I> DI void g1_piece(const G1Src& s, int kt) {
    if constexpr (I < 4) dma_s<I * 1024 - 4096>(s.xoff[I], s.Xb + (size_t)kt * 128);
    else if (!s.half || (threadIdx.x & 16) == 0) dma_s<(I - 4) * 1024>(s.woff[I - 4], s.Wb + (size_t)kt * s.wstep);
}

template <bool TR, int NT>
DI void g1_loop(f32x4 (&acc)[8][4], bf16x8 (&xs)[2][4], bf16x8 (&wsb)[2][4], const G1Src& cur, const G1Src& nxt, bool has_next, bool first,
                LAS char* lds, int wid, int lane) {
    const int wm = wid >> 2, wn = wid & 3, r16 = lane & 15, g = lane >> 4;
    const unsigned lds0 = (unsigned)(uintptr_t)lds;
    const int nkt = cur.nkt;
    int xb[2];
#pragma unroll
    for (int st = 0; st < 2; ++st) xb[st] = wm * 4 * 8192 + r16 * 128 + (((4 * st + g) ^ (r16 >> 1)) * 16);
    const int q = (lane >> 2) & 3, p4 = lane & 3;
    int wb[4];
#pragma unroll
    for (int nt = 0; nt < 4; ++nt) {
        const int b = NT == 4 ? wn * 2 + (nt >> 1) : wn;
        wb[nt] = g * 8192 + 4096 + q * 512 + ((b ^ q) * 64) + (((nt & 1) ^ (g & 1)) * 32) + p4 * 8;
    }
#pragma unroll
    for (int mt = 0; mt < 8; ++mt)
#pragma unroll
        for (int nt = 0; nt < 4; ++nt) acc[mt][nt] = f32x4{};
#define G1_RX(set, bb, st, mh)                                                                                   \
    _Pragma("unroll") for (int m4 = 0; m4 < 4; ++m4)                                                             \
        xs[set][m4] = *(LAS bf16x8*)((bb) + xb[st] + ((mh) * 2 + (m4 >> 1)) * 8192 + (m4 & 1) * 2048)
#define G1_RW(bb, st)                                                                                            \
    _Pragma("unroll") for (int nt = 0; nt < NTR; ++nt)                                                           \
        wsb[st][nt] = tr_frag((bb) + wb[nt] + (st) * 32768, (bb) + wb[nt] + (st) * 32768 + 2048)
#define G1_MM(set, st, mh, on, kn, p0, pe)                                                                       \
    sfor<0, 4>([&](auto mc) {                                                                                    \
        constexpr int m4 = decltype(mc)::value;                                                                  \
        _Pragma("unroll") for (int nt = 0; nt < NT; ++nt)                                                        \
            acc[(mh) * 4 + m4][nt] = TR ? MFMA16(wsb[st][nt], xs[set][m4], acc[(mh) * 4 + m4][nt])               \
                                        : MFMA16(xs[set][m4], wsb[st][nt], acc[(mh) * 4 + m4][nt]);              \
        if constexpr ((pe) > (p0)) {                                                                             \
            SBAR();                                                                                              \
            if (on) {                                                                                            \
                if ((kn) < nkt) { g1_piece<2 * m4>(cur, (kn)); g1_piece<2 * m4 + 1>(cur, (kn)); }                \
                else { g1_piece<2 * m4>(nxt, (kn) - nkt); g1_piece<2 * m4 + 1>(nxt, (kn) - nkt); }               \
            }                                                                                                    \
            SBAR();                                                                                              \
        }                                                                                                        \
    })
    constexpr int NTR = NT;
    G1_RW(lds, 0);
    if (first) { G1_RX(0, lds, 0, 0); }
#pragma unroll 1
    for (int kt = 0; kt < nkt; ++kt) {
        LAS char* bb = lds + (kt & 1) * 65536;
        LAS char* bn = lds + ((kt + 1) & 1) * 65536;
        const bool on2 = kt + 2 < nkt || has_next;
        LGKM0(); G1_RX(1, bb, 0, 1); SBAR(); G1_MM(0, 0, 0, false, 0, 0, 0); SBAR();
        LGKM0(); G1_RW(bb, 1); G1_RX(0, bb, 1, 0); SBAR(); G1_MM(1, 0, 1, false, 0, 0, 0); SBAR();
        LGKM0(); G1_RX(1, bb, 1, 1); SBAR(); G1_MM(0, 1, 0, false, 0, 0, 0); SBAR();
        LGKM0();
        if (PREFETCH && first && kt == 0) { WAIT_VM(5); } else { WAIT_VM(0); }
        BARRIER();
        set_m0(lds0 + (kt & 1) * 65536 + wid * 8192 + 4096);
        if (kt + 1 < nkt) G1_RW(bn, 0);
        if (kt + 1 < nkt || has_next) G1_RX(0, bn, 0, 0);
        SBAR(); G1_MM(1, 1, 1, on2, kt + 2, 0, 8); SBAR();
    }
#undef G1_RX
#undef G1_RW
#undef G1_MM
}

DI void st8(void* p, u32x2 v) {
    asm volatile("global_store_dwordx2 %0, %1, off sc1\n\ts_nop 1" :: "v"(p), "v"(v) : "memory");
}

template <int EP>
DI void g1_epi(const f32x4 (&acc)[8][4], int m0, int n0, int nlim, int nsec, char* __restrict__ ws, int wid, int lane) {
    const int wm = wid >> 2, wn = wid & 3, c = lane & 15, g = lane >> 4;
    if constexpr (EP == EP_BK || EP == EP_BQ || EP == EP_BV) {
#pragma unroll
        for (int mt = 0; mt < 8; ++mt) {
            const int tb = (m0 + wm * 128 + mt * 16) >> 5;
            const int tile = tb >> 1, kb = tb & 1, th = mt & 1;
#pragma unroll
            for (int np = 0; np < 2; ++np) {
                const int nb = n0 + wn * 64 + np * 32;
                const int head = nb >> 7;
                const float sc = EP == EP_BQ ? CB : 1.0f;
                const f32x4 a = acc[mt][2 * np], b = acc[mt][2 * np + 1];
                unsigned a0 = pack2(a[0] * sc, a[1] * sc), a1 = pack2(a[2] * sc, a[3] * sc);
                unsigned b0 = pack2(b[0] * sc, b[1] * sc), b1 = pack2(b[2] * sc, b[3] * sc);
                if constexpr (EP == EP_BV) {
                    auto r0 = __builtin_amdgcn_permlane32_swap(a0, b0, false, false);
                    auto r1 = __builtin_amdgcn_permlane32_swap(a1, b1, false, false);
                    const u32x4 w = {r0[0], r1[0], r0[1], r1[1]};
                    const int db = (nb >> 5) & 3;
                    char* base = ws + WS_KVB + (size_t)head * 1048576 + tile * 32768 + 16384 + ((kb * 2 + th) * 4 + db) * 1024;
                    st16(base + ((g >> 1) * 16 + c + 32 * (g & 1)) * 16, w);
                } else {
                    auto r0 = __builtin_amdgcn_permlane16_swap(a0, b0, false, false);
                    auto r1 = __builtin_amdgcn_permlane16_swap(a1, b1, false, false);
                    const u32x4 w = {r0[0], r1[0], r0[1], r1[1]};
                    const int ks = ((nb >> 4) & 7) + (g & 1);
                    char* base = EP == EP_BK ? ws + WS_KVB + (size_t)head * 1048576 + tile * 32768 + (kb * 8 + ks) * 1024
                                             : ws + WS_QB + (size_t)((head * 64 + tb) * 8 + ks) * 1024;
                    st16(base + (th * 16 + c + 32 * (g >> 1)) * 16, w);
                }
            }
        }
        return;
    }
#pragma unroll
    for (int mt = 0; mt < 8; ++mt) {
        const int tb = (m0 + wm * 128 + mt * 16) >> 5;
        const int tile = tb >> 1, kb = tb & 1, th = mt & 1;
#pragma unroll
        for (int nt = 0; nt < 4; ++nt) {
            const int nb = n0 + wn * 64 + nt * 16;
            if (nb >= nlim) continue;
            const f32x4 a = acc[mt][nt];
            if constexpr (EP == EP_BK || EP == EP_BQ || EP == EP_BV) {
            } else if constexpr (EP == EP_AQK) {
                const bool isq = nb < 640;
                const float sc = isq ? CA : 1.0f;
                const int r = th * 16 + c;
                const int n = nb - (isq ? 0 : 640) + 4 * g;
                const int head = n / 20, d0 = n - head * 20;
                char* base = isq ? ws + WS_QA + (size_t)((head * 64 + tb) * 2) * 1024
                                 : ws + WS_KVA + (size_t)head * 262144 + tile * 8192 + kb * 2048;
                char* dst = base + (d0 >> 4) * 1024 + (r + 32 * ((d0 >> 3) & 1)) * 16 + (d0 & 4) * 2;
                u32x2 w;
                w[0] = pack2(a[0] * sc, a[1] * sc);
                w[1] = pack2(a[2] * sc, a[3] * sc);
                *(u32x2*)dst = w;
                if (d0 == 16) {
                    *(u32x2*)(dst + 8) = (u32x2){isq ? 0u : 0x3F803F80u, 0u};
                    *(u32x4*)(base + 1024 + (r + 32) * 16) = (u32x4){0u, 0u, 0u, 0u};
                }
            } else {
                const int n = nb - nsec + c;
                const int head = n / 20, d = n - head * 20;
                char* dst = ws + WS_KVA + (size_t)head * 262144 + tile * 8192 + 4096 + (kb * 2 + th) * 1024 + (d + 32 * (g & 1)) * 16 + (g >> 1) * 8;
                u32x2 w;
                w[0] = pack2(a[0], a[1]);
                w[1] = pack2(a[2], a[3]);
                *(u32x2*)dst = w;
                if (d == 19) *(u32x2*)(dst + 16) = (u32x2){0x3F803F80u, 0x3F803F80u};
            }
        }
    }
}

__global__ __launch_bounds__(512, 2) void k1_proj(const __hip_bfloat16* __restrict__ Ax, const __hip_bfloat16* __restrict__ Awqkv,
                                                  const __hip_bfloat16* __restrict__ Bx, const __hip_bfloat16* __restrict__ Bckv,
                                                  const __hip_bfloat16* __restrict__ Bwq, const __hip_bfloat16* __restrict__ Bwuk,
                                                  const __hip_bfloat16* __restrict__ Bwuv, const __hip_bfloat16* __restrict__ Awo,
                                                  const __hip_bfloat16* __restrict__ Bwo, char* __restrict__ ws) {
    extern __shared__ __attribute__((aligned(16))) char smem[];
    LAS char* lds = (LAS char*)smem;
    const int tid = threadIdx.x, lane = tid & 63, wid = __builtin_amdgcn_readfirstlane(tid >> 6);
    const int bid = blockIdx.x;
    const int m0 = ((bid >> 3) & 7) * 256;
    const int grp = bid >> 6, c8 = bid & 7;
    const int cc = c8 + 8 * (grp & 1);
    const bool has2 = grp < 3;
    G1Src s1, s2;
    if (grp < 2) {
        g1_src(s1, Bckv, Bwuk, 512, 4096, m0, cc * 256, 4096, wid, lane, false);
        g1_src(s2, Bx, Bwq, 640, 4096, m0, cc * 256, 4096, wid, lane, false);
    } else if (grp == 2) {
        g1_src(s1, Bckv, Bwuv, 512, 4096, m0, c8 * 256, 4096, wid, lane, false);
        g1_src(s2, Bckv, Bwuv, 512, 4096, m0, (c8 + 8) * 256, 4096, wid, lane, false);
    } else {
        g1_src(s1, Ax, Awqkv, 640, 1920, m0, c8 * 256, c8 < 5 ? 1280 : 1920, wid, lane, false);
        s2 = s1;
    }

    f32x4 acc[8][4];
    bf16x8 xf[2][4], wf[2][4];
    const unsigned lds0 = (unsigned)(uintptr_t)lds;
    if (bid == 255 && tid < 40) ((unsigned*)(ws + WS_CNT))[tid * 64] = 0u;
    if (bid == 255 && tid >= 64 && tid < 72) ((unsigned*)(ws + WS_CNT2))[(tid - 64) * 64] = 0u;
    STAMP(0, 0);
    set_m0(lds0 + wid * 8192 + 4096);
    sfor<0, 8>([&](auto ic) { g1_piece<decltype(ic)::value>(s1, 0); });
    set_m0(lds0 + 65536 + wid * 8192 + 4096);
    sfor<0, 8>([&](auto ic) { g1_piece<decltype(ic)::value>(s1, 1); });
    WAIT_VM(8);
    BARRIER();
    STAMP(0, 1);
    {
        set_m0(lds0 + 131072 + wid * 256);
        const unsigned gw = (unsigned)(bid * 8 + wid) * 64 + lane;
#define PF(ptr, nbytes) do { constexpr unsigned R_ = 131072u / ((nbytes) / 128u);                                     \
            const unsigned ln_ = gw / R_;                                                                                \
            const unsigned off_ = (gw % R_ == 0u && ln_ * 128u < (nbytes)) ? ln_ * 128u : 0u;                             \
            asm volatile("global_load_lds_dword %0, %1" :: "v"(off_), "s"((const char*)(ptr)) : "memory"); } while (0)
        PF(Bx, 2048u * 640u * 2u); PF(Bwq, 640u * 4096u * 2u); PF(Bwuv, 512u * 4096u * 2u);
        PF(Bwo, 4096u * 640u * 2u); PF(Awo, 640u * 640u * 2u);
#undef PF
    }
    if (grp < 2 || (grp == 3 && c8 < 5)) {
        g1_loop<true, 4>(acc, xf, wf, s1, s2, has2, true, lds, wid, lane);
        if (grp < 2) g1_epi<EP_BK>(acc, m0, cc * 256, 4096, 0, ws, wid, lane);
        else g1_epi<EP_AQK>(acc, m0, c8 * 256, 1280, 0, ws, wid, lane);
    } else {
        g1_loop<false, 4>(acc, xf, wf, s1, s2, has2, true, lds, wid, lane);
        if (grp == 2) g1_epi<EP_BV>(acc, m0, c8 * 256, 4096, 0, ws, wid, lane);
        else g1_epi<EP_AV>(acc, m0, c8 * 256, 1920, 1280, ws, wid, lane);
    }
    STAMP(0, 2);
    if (grp < 2) {
        g1_loop<true, 4>(acc, xf, wf, s2, s2, false, false, lds, wid, lane);
        g1_epi<EP_BQ>(acc, m0, cc * 256, 4096, 0, ws, wid, lane);
    } else if (grp == 2) {
        g1_loop<false, 4>(acc, xf, wf, s2, s2, false, false, lds, wid, lane);
        g1_epi<EP_BV>(acc, m0, (c8 + 8) * 256, 4096, 0, ws, wid, lane);
    }
    STAMP(0, 3);
}

constexpr int G3_STAGE = 49152;

DI void k3_body(const __hip_bfloat16* __restrict__ Awo, const __hip_bfloat16* __restrict__ Bwo,
                char* __restrict__ ws, __hip_bfloat16* __restrict__ out, LAS char* lds) {
    const int tid = threadIdx.x, lane = tid & 63, wid = __builtin_amdgcn_readfirstlane(tid >> 6);
    const int wm = wid >> 1, wn = wid & 1, h = lane >> 5;
    const int bid = blockIdx.x;
    const bool isB = bid < 200;
    int mt, nt, sp, kt0, nkt, kstot;
    const char* Xc; const char* Wb;
    if (isB) {
        mt = bid & 7; const int rest = bid >> 3; nt = rest % 5; sp = rest / 5;
        kt0 = sp * 13; nkt = sp == 4 ? 12 : 13; kstot = 256;
        Xc = ws + WS_CTXB; Wb = (const char*)Bwo;
    } else {
        const int b2 = bid - 200; mt = b2 & 7; nt = b2 >> 3; sp = 5;
        kt0 = 0; nkt = 10; kstot = 40;
        Xc = ws + WS_CTXA; Wb = (const char*)Awo;
    }
    unsigned woff[2];
#pragma unroll
    for (int i = 0; i < 2; ++i) {
        const int k = (wid * 2 + i) * 4 + (lane >> 4);
        const int p = lane & 15, b = (p >> 2) ^ (k & 3);
        woff[i] = (unsigned)(((kt0 * 64 + k) * 640 + nt * 128 + (b * 4 + (p & 3)) * 8) * 2);
    }
    const unsigned lds0 = (unsigned)(uintptr_t)lds;
    const char* xsb = Xc + (size_t)((mt * 8 + wid) * kstot + kt0 * 4) * 1024 + 4096;
    const unsigned voff = lane * 16;
    woff[1] -= 1024;
    auto piece = [&](int kt, auto ic) {
        constexpr int i = decltype(ic)::value;
        if constexpr (i < 4) dma_s<i * 1024 - 4096>(voff, xsb + (size_t)kt * 4096);
        else dma_s<(i - 4) * 1024>(woff[i - 4], Wb + (size_t)kt * (64u * 640u * 2u));
    };
    const int q = (lane >> 2) & 3, p4 = lane & 3, g16 = (lane >> 4) & 1;
    const int r0 = isB ? 4 * h : 8 * h, r1 = isB ? 8 + 4 * h : 8 * h + 4;
    int wb0[2], wb1[2];
#pragma unroll
    for (int ni = 0; ni < 2; ++ni) {
        const int b = wn * 2 + ni;
        const int c = ((b ^ q) * 64) + g16 * 32 + p4 * 8;
        wb0[ni] = (r0 >> 3) * 6144 + 4096 + ((r0 & 7) + q) * 256 + c;
        wb1[ni] = (r1 >> 3) * 6144 + 4096 + ((r1 & 7) + q) * 256 + c;
    }
    const int xb0 = wm * 2 * 6144 + lane * 16;
    f32x16 acc[2][2];
#pragma unroll
    for (int mi = 0; mi < 2; ++mi)
#pragma unroll
        for (int ni = 0; ni < 2; ++ni) acc[mi][ni] = f32x16{};

    bf16x8 xf[2][4][2], wf[2][4][2];
#define G3_RD(set, bb)                                                                                           \
    do {                                                                                                         \
        _Pragma("unroll") for (int ks = 0; ks < 4; ++ks) {                                                       \
            _Pragma("unroll") for (int ni = 0; ni < 2; ++ni)                                                     \
                wf[set][ks][ni] = tr_frag((bb) + wb0[ni] + ks * 12288, (bb) + wb1[ni] + ks * 12288);             \
            _Pragma("unroll") for (int mi = 0; mi < 2; ++mi)                                                     \
                xf[set][ks][mi] = *(LAS bf16x8*)((bb) + xb0 + mi * 6144 + ks * 1024);                            \
        }                                                                                                        \
    } while (0)
#define G3_STEP(set, kt)                                                                                         \
    do {                                                                                                         \
        if ((kt) + 2 < nkt) { WAIT_VM(6); } else { WAIT_VM(0); }                                                 \
        LGKM0();                                                                                                 \
        BARRIER();                                                                                               \
        const int b0_ = (kt) % 3, b1_ = ((kt) + 1) % 3;                                                          \
        set_m0(lds0 + b0_ * G3_STAGE + wid * 6144 + 4096);                                                       \
        if ((kt) + 1 < nkt) G3_RD((set) ^ 1, lds + b1_ * G3_STAGE);                                              \
        SBAR();                                                                                                  \
        const bool on_ = (kt) + 3 < nkt;                                                                         \
        sfor<0, 8>([&](auto jc) {                                                                                \
            constexpr int j = decltype(jc)::value, ks = j >> 1, mi = j & 1;                                      \
            _Pragma("unroll") for (int ni = 0; ni < 2; ++ni)                                                     \
                acc[mi][ni] = MFMA32(wf[set][ks][ni], xf[set][ks][mi], acc[mi][ni]);                             \
            if constexpr (j < 6) { SBAR(); if (on_) piece((kt) + 3, jc); SBAR(); }                               \
        });                                                                                                      \
        SBAR();                                                                                                  \
    } while (0)
    STAMP(2, 0);
#pragma unroll
    for (int t = 0; t < 3; ++t) {
        set_m0(lds0 + t * G3_STAGE + wid * 6144 + 4096);
        piece(t, std::integral_constant<int, 4>{});
        piece(t, std::integral_constant<int, 5>{});
    }
    if (wid == 0) {
        const unsigned* c2 = (const unsigned*)(ws + WS_CNT2);
        const int xa = isB ? (2 * mt < 8 ? 2 * mt : 15 - 2 * mt) : mt;
        const int xb = isB ? (2 * mt + 1 < 8 ? 2 * mt + 1 : 14 - 2 * mt) : mt;
        for (unsigned spins = 0; spins < (1u << 24); ++spins) {
            const unsigned ca = __builtin_amdgcn_readfirstlane(__hip_atomic_load(c2 + xa * 64, __ATOMIC_RELAXED, __HIP_MEMORY_SCOPE_AGENT));
            const unsigned cb = __builtin_amdgcn_readfirstlane(__hip_atomic_load(c2 + xb * 64, __ATOMIC_RELAXED, __HIP_MEMORY_SCOPE_AGENT));
            if (ca >= 32u && cb >= 32u) break;
            __builtin_amdgcn_s_sleep(1);
        }
        __builtin_amdgcn_fence(__ATOMIC_ACQUIRE, "agent");
        asm volatile("s_waitcnt vmcnt(0)" ::: "memory");
    }
    BARRIER();
#pragma unroll
    for (int t = 0; t < 3; ++t) {
        set_m0(lds0 + t * G3_STAGE + wid * 6144 + 4096);
        sfor<0, 4>([&](auto ic) { piece(t, ic); });
    }
    WAIT_VM(4);
    BARRIER();
    STAMP(2, 1);
    G3_RD(0, lds);
    int kt = 0;
#pragma unroll 1
    for (; kt + 1 < nkt; kt += 2) { G3_STEP(0, kt); G3_STEP(1, kt + 1); }
    if (kt < nkt) G3_STEP(0, kt);
#undef G3_RD
#undef G3_STEP
    STAMP(2, 2);
    const int r = lane & 31;
    if (!isB) {
#pragma unroll
        for (int mi = 0; mi < 2; ++mi)
#pragma unroll
            for (int ni = 0; ni < 2; ++ni) {
                char* op = (char*)out + (size_t)((mt * 8 + wm * 2 + mi) * 32 + r) * 2560 + ((nt * 4 + wn * 2 + ni) * 32 + 4 * h) * 2;
#pragma unroll
                for (int gq = 0; gq < 4; ++gq) {
                    u32x2 o;
                    o[0] = pack2(acc[mi][ni][4 * gq], acc[mi][ni][4 * gq + 1]);
                    o[1] = pack2(acc[mi][ni][4 * gq + 2], acc[mi][ni][4 * gq + 3]);
                    *(u32x2*)(op + gq * 16) = o;
                }
            }
        STAMP(2, 3);
        return;
    }
#pragma unroll
    for (int mi = 0; mi < 2; ++mi)
#pragma unroll
        for (int ni = 0; ni < 2; ++ni) {
            const int st = (wm * 2 + mi) * 4 + wn * 2 + ni;
            if (st % 5 == sp) continue;
            const int mt32 = mt * 8 + wm * 2 + mi, nt32 = nt * 4 + wn * 2 + ni;
            char* dst = ws + WS_PART + (size_t)sp * SLAB + (size_t)(mt32 * 20 + nt32) * 4096 + lane * 16;
#pragma unroll
            for (int gq = 0; gq < 4; ++gq) {
                f32x4 v = {acc[mi][ni][4 * gq], acc[mi][ni][4 * gq + 1], acc[mi][ni][4 * gq + 2], acc[mi][ni][4 * gq + 3]};
                asm volatile("global_store_dwordx4 %0, %1, off sc1\n\ts_nop 1" :: "v"(dst + gq * 1024), "v"(v) : "memory");
            }
        }
    asm volatile("s_waitcnt vmcnt(0)" ::: "memory");
    BARRIER();
    unsigned* cnt = (unsigned*)(ws + WS_CNT) + (mt * 5 + nt) * 64;
    if (wid == 0) {
        if (lane == 0) __hip_atomic_fetch_add(cnt, 1u, __ATOMIC_RELAXED, __HIP_MEMORY_SCOPE_AGENT);
        for (unsigned spins = 0; spins < (1u << 24); ++spins) {
            const unsigned c = __builtin_amdgcn_readfirstlane(__hip_atomic_load(cnt, __ATOMIC_RELAXED, __HIP_MEMORY_SCOPE_AGENT));
            if (c >= (unsigned)NSPLIT) break;
            __builtin_amdgcn_s_sleep(1);
        }
    }
    BARRIER();
    const auto rsrc = __builtin_amdgcn_make_buffer_rsrc((void*)(ws + WS_PART), 0, (int)(NSPLIT * SLAB), 0x00020000);
#pragma unroll
    for (int mi = 0; mi < 2; ++mi)
#pragma unroll
        for (int ni = 0; ni < 2; ++ni) {
            const int st = (wm * 2 + mi) * 4 + wn * 2 + ni;
            if (st % 5 != sp) continue;
            const int mt32 = mt * 8 + wm * 2 + mi, nt32 = nt * 4 + wn * 2 + ni;
            const int off = (mt32 * 20 + nt32) * 4096 + lane * 16;
            u32x4 v[4][4];
#pragma unroll
            for (int s = 0; s < 4; ++s) {
                const int so = (s < sp ? s : s + 1) * (int)SLAB;
#pragma unroll
                for (int gq = 0; gq < 4; ++gq) v[s][gq] = __builtin_amdgcn_raw_buffer_load_b128(rsrc, off + gq * 1024, so, 16);
            }
            char* op = (char*)out + (size_t)(mt32 * 32 + r) * 2560 + (640 + nt32 * 32 + 4 * h) * 2;
#pragma unroll
            for (int gq = 0; gq < 4; ++gq) {
                f32x4 t = {acc[mi][ni][4 * gq], acc[mi][ni][4 * gq + 1], acc[mi][ni][4 * gq + 2], acc[mi][ni][4 * gq + 3]};
                t += (__builtin_bit_cast(f32x4, v[0][gq]) + __builtin_bit_cast(f32x4, v[1][gq])) +
                     (__builtin_bit_cast(f32x4, v[2][gq]) + __builtin_bit_cast(f32x4, v[3][gq]));
                u32x2 o;
                o[0] = pack2(t[0], t[1]);
                o[1] = pack2(t[2], t[3]);
                *(u32x2*)(op + gq * 16) = o;
            }
        }
    STAMP(2, 3);
}

constexpr int SLOT = 40960;
constexpr int NSLOT = 4;
constexpr int K2_LDS = NSLOT * SLOT;
constexpr int NIT = 34;

__global__ __launch_bounds__(512, 2) void k2_attn(char* __restrict__ ws, const __hip_bfloat16* __restrict__ Awo,
                                                  const __hip_bfloat16* __restrict__ Bwo, __hip_bfloat16* __restrict__ out) {
    extern __shared__ __attribute__((aligned(16))) char smem[];
    LAS char* lds = (LAS char*)smem;
    const int tid = threadIdx.x, lane = tid & 63, wid = __builtin_amdgcn_readfirstlane(tid >> 6);
    const int r = lane & 31, h = lane >> 5;
    const int g = blockIdx.x, xcd = g & 7, idx = g >> 3;
    const int head = xcd * 4 + (idx >> 3), x = idx & 7;
    const char* kvb = ws + WS_KVB + (size_t)head * 1048576;
    const char* kva = ws + WS_KVA + (size_t)head * 262144;
    const int n1 = 32 - 2 * x;
    const unsigned lbase = (unsigned)(uintptr_t)lds + lane * 16;

    STAMP(1, 0);
    const unsigned lds0 = (unsigned)(uintptr_t)lds;
    const unsigned voff = lane * 16;
    const int wa = wid & 3;
    const char* sbB = kvb;
    const char* sbA = kva;
#define K2_M0(tt)                                                                           \
    do {                                                                                    \
        const int t3_ = (tt) < NIT ? (tt) : NIT - 1;                                        \
        const int tb3_ = t3_ < n1 ? t3_ : t3_ - n1;                                         \
        const int ta3_ = t3_ < 32 ? t3_ : 31;                                               \
        sbB = kvb + (size_t)tb3_ * 32768 + wid * 4096 + 2048;                               \
        sbA = kva + (size_t)ta3_ * 8192 + wid * 1024 - 2048;                                \
        set_m0(lds0 + ((tt) < NIT ? ((tt) & 3) : 2 + ((tt) & 1)) * SLOT + wid * 5120 + 2048); \
    } while (0)
#define K2_DMA(i) do { SBAR(); if constexpr ((i) < 4) dma_s<(i) * 1024 - 2048>(voff, sbB); else dma_s<2048>(voff, sbA); SBAR(); } while (0)
#define K2_STAGE_ALL(tt) do { K2_M0(tt); K2_DMA(0); K2_DMA(1); K2_DMA(2); K2_DMA(3); K2_DMA(4); } while (0)
#define BOFF(f) (((f) >> 2) * 5120 + ((f) & 3) * 1024)
#define AOFF(a) ((a) * 5120 + 4096)
    K2_STAGE_ALL(0);
    K2_STAGE_ALL(1);

#define K2_SYNC(it) do { WAIT_VM(5); BARRIER(); K2_M0((it) + 3); } while (0)

    if (wid < 4) {
        bf16x8 fr[8];
        K2_SYNC(-1);
        STAMP(1, 1);
        sfor<0, 8>([&](auto ic) { constexpr int j = decltype(ic)::value; lds_rd<BOFF((j & 1) * 8 + (j >> 1))>(fr[j], lbase); });
        K2_DMA(0); K2_DMA(1);
        PRIO(1);
        int it = 0;
        for (int blk = 0; blk < 2; ++blk) {
            const int qb = blk == 0 ? 15 - x : x;
            const int nt = 2 * qb + 2;
            const int q0 = qb * 128 + wid * 32;
            const int tb32 = q0 >> 5;
            bf16x8 qf[8];
            const char* qp = ws + WS_QB + (size_t)((head * 64 + tb32) * 8) * 1024 + lane * 16;
#pragma unroll
            for (int ks = 0; ks < 8; ++ks) qf[ks] = *(const bf16x8*)(qp + ks * 1024);
            f32x16 o[4];
#pragma unroll
            for (int db = 0; db < 4; ++db) o[db] = f32x16{};
            f32x16 negm = f32x16{};
            float l = 0.f;
            for (int t = 0; t < nt; ++t, ++it) {
                const int rel = (q0 - 64 * t) >> 5;
                const unsigned ac = lbase + (it & 3) * SLOT;
                const unsigned an = lbase + ((it + 1) & 3) * SLOT;
                f32x16 s0, s1;
                SEG(0);
                sfor<0, 16>([&](auto ic) {
                    constexpr int j = decltype(ic)::value, ks = j >> 1;
                    lds_wait<7>(fr[j & 7]);
                    if constexpr (j == 0) s0 = MFMA32(fr[0], qf[0], negm);
                    else if constexpr (j == 1) s1 = MFMA32(fr[1], qf[0], negm);
                    else if constexpr ((j & 1) == 0) s0 = MFMA32(fr[j & 7], qf[ks], s0);
                    else s1 = MFMA32(fr[j & 7], qf[ks], s1);
                    constexpr int jn = j + 8;
                    SBAR();
                    if constexpr (jn < 16) lds_rd<BOFF((jn & 1) * 8 + (jn >> 1))>(fr[j & 7], ac);
                    else lds_rd<BOFF(jn)>(fr[j & 7], ac);
                    if constexpr (j == 2) K2_DMA(2);
                    if constexpr (j == 8) K2_DMA(3);
                    if constexpr (j == 14) K2_DMA(4);
                    SBAR();
                });
#define B_MASK()                                                                                        \
    do {                                                                                                \
        if (rel <= 1) {                                                                                 \
            const int lim = 32 * rel + r - 4 * h;                                                       \
            _Pragma("unroll") for (int i = 0; i < 16; ++i) {                                            \
                const int kr = (i & 3) + 8 * (i >> 2);                                                  \
                if (kr > lim) s0[i] = -__builtin_inff();                                                \
                if (kr + 32 > lim) s1[i] = -__builtin_inff();                                           \
            }                                                                                           \
        }                                                                                               \
    } while (0)
                SEG(1);
                PRIO(0);
                B_MASK();
                float ps = 0.f;
                if (t > 0) {
#pragma unroll
                    for (int i = 0; i < 16; ++i) { s0[i] = __builtin_amdgcn_exp2f(s0[i]); ps += s0[i]; }
#pragma unroll
                    for (int i = 0; i < 16; ++i) { s1[i] = __builtin_amdgcn_exp2f(s1[i]); ps += s1[i]; }
                }
                if (t == 0 || __any(!(ps <= 65536.f))) {
                    if (t > 0) {
                        LAS char* sl = lds + (it & 3) * SLOT + lane * 16;
                        s0 = negm; s1 = negm;
#pragma unroll 1
                        for (int ks = 0; ks < 8; ++ks) {
                            bf16x8 qq = qf[0];
#pragma unroll
                            for (int u = 1; u < 8; ++u) qq = (ks == u) ? qf[u] : qq;
                            s0 = MFMA32(*(LAS bf16x8*)(sl + (ks >> 2) * 5120 + (ks & 3) * 1024), qq, s0);
                            s1 = MFMA32(*(LAS bf16x8*)(sl + 10240 + (ks >> 2) * 5120 + (ks & 3) * 1024), qq, s1);
                        }
                        B_MASK();
                    }
                    float mx = s0[0];
#pragma unroll
                    for (int i = 1; i < 16; ++i) mx = fmaxf(mx, s0[i]);
#pragma unroll
                    for (int i = 0; i < 16; ++i) mx = fmaxf(mx, s1[i]);
                    auto rr = __builtin_amdgcn_permlane32_swap(__float_as_uint(mx), __float_as_uint(mx), false, false);
                    mx = fmaxf(__uint_as_float(rr[0]), __uint_as_float(rr[1]));
                    float delta = mx;
                    if (t > 0) {
                        delta = fmaxf(mx, 0.f);
                        const float alpha = __builtin_amdgcn_exp2f(-delta);
                        l *= alpha;
#pragma unroll
                        for (int db = 0; db < 4; ++db)
#pragma unroll
                            for (int i = 0; i < 16; ++i) o[db][i] *= alpha;
                    }
                    const float nm = negm[0] - delta;
#pragma unroll
                    for (int i = 0; i < 16; ++i) negm[i] = nm;
                    ps = 0.f;
#pragma unroll
                    for (int i = 0; i < 16; ++i) { s0[i] = __builtin_amdgcn_exp2f(s0[i] - delta); ps += s0[i]; }
#pragma unroll
                    for (int i = 0; i < 16; ++i) { s1[i] = __builtin_amdgcn_exp2f(s1[i] - delta); ps += s1[i]; }
                }
                l += ps;
                bf16x8 pf[4];
                pf[0] = __builtin_bit_cast(bf16x8, pack8(s0, 0));
                pf[1] = __builtin_bit_cast(bf16x8, pack8(s0, 1));
                pf[2] = __builtin_bit_cast(bf16x8, pack8(s1, 0));
                pf[3] = __builtin_bit_cast(bf16x8, pack8(s1, 1));
                SEG(2);
                if (it + 1 < NIT) { K2_SYNC(it); } else { K2_M0(it + 3); }
                SEG(3);
                PRIO(1);
                sfor<0, 16>([&](auto ic) {
                    constexpr int j = decltype(ic)::value, s4 = j >> 2, db = j & 3;
                    lds_wait<7>(fr[j & 7]);
                    o[db] = MFMA32(fr[j & 7], pf[s4], o[db]);
                    SBAR();
                    if constexpr (j < 8) lds_rd<BOFF(24 + j)>(fr[j & 7], ac);
                    else { constexpr int jn = j - 8; lds_rd<BOFF((jn & 1) * 8 + (jn >> 1))>(fr[j & 7], an); }
                    if constexpr (j == 4) K2_DMA(0);
                    if constexpr (j == 10) K2_DMA(1);
                    SBAR();
                });
                SEG(4);
            }
            auto rr = __builtin_amdgcn_permlane32_swap(__float_as_uint(l), __float_as_uint(l), false, false);
            const float inv = 1.0f / (__uint_as_float(rr[0]) + __uint_as_float(rr[1]));
            char* cp = ws + WS_CTXB + (size_t)(tb32 * 256 + head * 8) * 1024 + lane * 16;
#pragma unroll
            for (int db = 0; db < 4; ++db) {
                st16(cp + (2 * db) * 1024, pack8s(o[db], 0, inv));
                st16(cp + (2 * db + 1) * 1024, pack8s(o[db], 1, inv));
            }
        }
        WAIT_VM(0);
        STAMP(1, 2);
#undef B_MASK
    } else {
        const int tb32 = x * 8 + wa * 2;
        bf16x8 qf[2][2];
        const char* qp = ws + WS_QA + (size_t)((head * 64 + tb32) * 2) * 1024 + lane * 16;
#pragma unroll
        for (int qt = 0; qt < 2; ++qt)
#pragma unroll
            for (int ks = 0; ks < 2; ++ks) qf[qt][ks] = *(const bf16x8*)(qp + (qt * 2 + ks) * 1024);
        f32x16 o[2];
        o[0] = f32x16{}; o[1] = f32x16{};
        float negm[2] = {0.f, 0.f};
        bf16x8 kf[4], vf[4];
        f32x16 s[2][2];
        K2_SYNC(-1);
        lds_rd<AOFF(0)>(kf[0], lbase); lds_rd<AOFF(1)>(kf[1], lbase); lds_rd<AOFF(2)>(kf[2], lbase); lds_rd<AOFF(3)>(kf[3], lbase);
        for (int it = 0; it <= 32; ++it) {
            SEG(0);
            if (it > 0) {
                u32x4 pf[2][2][2];
                unsigned chk = 0;
                sfor<0, 4>([&](auto bc) {
                    constexpr int blk = decltype(bc)::value, kb = blk >> 1, qt = blk & 1;
                    f32x16 e;
#pragma unroll
                    for (int i = 0; i < 8; ++i) e[i] = __builtin_amdgcn_exp2f(s[kb][qt][i]);
                    K2_DMA(blk);
#pragma unroll
                    for (int i = 8; i < 16; ++i) e[i] = __builtin_amdgcn_exp2f(s[kb][qt][i]);
                    if constexpr (blk == 3) K2_DMA(4);
                    pf[kb][qt][0] = pack8(e, 0);
                    pf[kb][qt][1] = pack8(e, 1);
                    chk |= pf[kb][qt][0][0] | pf[kb][qt][0][1] | pf[kb][qt][0][2] | pf[kb][qt][0][3];
                    chk |= pf[kb][qt][1][0] | pf[kb][qt][1][1] | pf[kb][qt][1][2] | pf[kb][qt][1][3];
                });
                const bool first = (it == 1);
                if (first || __any((chk & 0x40004000u) != 0u)) {
#pragma unroll
                    for (int qt = 0; qt < 2; ++qt) {
                        float mx = s[0][qt][0];
#pragma unroll
                        for (int i = 1; i < 16; ++i) mx = fmaxf(mx, s[0][qt][i]);
#pragma unroll
                        for (int i = 0; i < 16; ++i) mx = fmaxf(mx, s[1][qt][i]);
                        auto rr = __builtin_amdgcn_permlane32_swap(__float_as_uint(mx), __float_as_uint(mx), false, false);
                        mx = fmaxf(__uint_as_float(rr[0]), __uint_as_float(rr[1]));
                        float delta = mx + 10.f;
                        if (!first) delta = fmaxf(delta, 0.f);
                        const float tgt = negm[qt] - delta;
                        const unsigned hi = pack2(tgt, 0.f) & 0xFFFFu;
                        const float hif = __uint_as_float(hi << 16);
                        const unsigned lo = pack2(tgt - hif, 0.f) & 0xFFFFu;
                        const float neff = hif + __uint_as_float(lo << 16);
                        delta = negm[qt] - neff;
                        negm[qt] = neff;
                        {
                            u32x4 qq = __builtin_bit_cast(u32x4, qf[qt][1]);
                            qq[2] = h ? 0u : (hi | (lo << 16));
                            qf[qt][1] = __builtin_bit_cast(bf16x8, qq);
                        }
                        if (!first) {
                            const float alpha = __builtin_amdgcn_exp2f(-delta);
#pragma unroll
                            for (int i = 0; i < 16; ++i) o[qt][i] *= alpha;
                        }
#pragma unroll
                        for (int kb = 0; kb < 2; ++kb) {
                            f32x16 e;
#pragma unroll
                            for (int i = 0; i < 16; ++i) e[i] = __builtin_amdgcn_exp2f(s[kb][qt][i] - delta);
                            pf[kb][qt][0] = pack8(e, 0);
                            pf[kb][qt][1] = pack8(e, 1);
                        }
                    }
                }
                SEG(1);
                PRIO(1);
                lds_wait2<4>(vf[0], vf[1]);
                lds_wait2<4>(vf[2], vf[3]);
#pragma unroll
                for (int kb = 0; kb < 2; ++kb)
#pragma unroll
                    for (int s2 = 0; s2 < 2; ++s2)
#pragma unroll
                        for (int qt = 0; qt < 2; ++qt)
                            o[qt] = MFMA32(vf[kb * 2 + s2], __builtin_bit_cast(bf16x8, pf[kb][qt][s2]), o[qt]);
            } else {
                K2_DMA(0); K2_DMA(1); K2_DMA(2); K2_DMA(3); K2_DMA(4);
            }
            SEG(2);
            lds_wait2<0>(kf[0], kf[1]);
            lds_wait2<0>(kf[2], kf[3]);
#pragma unroll
            for (int kb = 0; kb < 2; ++kb)
#pragma unroll
                for (int qt = 0; qt < 2; ++qt) {
                    s[kb][qt] = MFMA32(kf[kb * 2], qf[qt][0], f32x16{});
                    s[kb][qt] = MFMA32(kf[kb * 2 + 1], qf[qt][1], s[kb][qt]);
                }
            SBAR();
            if (it == 32) STAMPW(1, 3, 4);
            SEG(3);
            PRIO(0);
            K2_SYNC(it);
            SEG(4);
            const unsigned ac = lbase + (it & 3) * SLOT;
            const unsigned an = lbase + ((it + 1) & 3) * SLOT;
            lds_rd<AOFF(4)>(vf[0], ac); lds_rd<AOFF(5)>(vf[1], ac); lds_rd<AOFF(6)>(vf[2], ac); lds_rd<AOFF(7)>(vf[3], ac);
            lds_rd<AOFF(0)>(kf[0], an); lds_rd<AOFF(1)>(kf[1], an); lds_rd<AOFF(2)>(kf[2], an); lds_rd<AOFF(3)>(kf[3], an);
        }
        WAIT_VM(0);
#pragma unroll
        for (int qt = 0; qt < 2; ++qt) {
            const unsigned lv = __float_as_uint(o[qt][8]);
            auto rr = __builtin_amdgcn_permlane32_swap(lv, lv, false, false);
            const float lsum = __uint_as_float(rr[1]);
            const float inv = 1.0f / lsum;
            char* cp = ws + WS_CTXA + (size_t)((tb32 + qt) * 40) * 1024;
#pragma unroll
            for (int gq = 0; gq < 3; ++gq) {
                const int d0 = 8 * gq + 4 * h;
                if (d0 < 20) {
                    const int k = head * 20 + d0;
                    u32x2 w;
                    w[0] = pack2(o[qt][4 * gq + 0] * inv, o[qt][4 * gq + 1] * inv);
                    w[1] = pack2(o[qt][4 * gq + 2] * inv, o[qt][4 * gq + 3] * inv);
                    st8(cp + (k >> 4) * 1024 + (r + 32 * ((k >> 3) & 1)) * 16 + (k & 4) * 2, w);
                }
            }
        }
    }
    asm volatile("s_waitcnt vmcnt(0) lgkmcnt(0)" ::: "memory");
    BARRIER();
    if (tid == 0) __hip_atomic_fetch_add((unsigned*)(ws + WS_CNT2) + x * 64, 1u, __ATOMIC_RELAXED, __HIP_MEMORY_SCOPE_AGENT);
    if (g < 240) k3_body(Awo, Bwo, ws, out, lds);
}

extern "C" void kernel_launch(void* const* d_in, const int* in_sizes, int n_in,
                              void* d_out, int out_size, void* d_ws, size_t ws_size,
                              hipStream_t stream) {
    (void)in_sizes; (void)n_in; (void)out_size; (void)ws_size;
    const __hip_bfloat16* Ax = (const __hip_bfloat16*)d_in[0];
    const __hip_bfloat16* Awqkv = (const __hip_bfloat16*)d_in[1];
    const __hip_bfloat16* Awo = (const __hip_bfloat16*)d_in[2];
    const __hip_bfloat16* Bx = (const __hip_bfloat16*)d_in[3];
    const __hip_bfloat16* Bckv = (const __hip_bfloat16*)d_in[4];
    const __hip_bfloat16* Bwq = (const __hip_bfloat16*)d_in[5];
    const __hip_bfloat16* Bwuk = (const __hip_bfloat16*)d_in[6];
    const __hip_bfloat16* Bwuv = (const __hip_bfloat16*)d_in[7];
    const __hip_bfloat16* Bwo = (const __hip_bfloat16*)d_in[8];
    char* ws = (char*)d_ws;
    (void)hipFuncSetAttribute((const void*)k1_proj, hipFuncAttributeMaxDynamicSharedMemorySize, G1_LDS);
    (void)hipFuncSetAttribute((const void*)k2_attn, hipFuncAttributeMaxDynamicSharedMemorySize, K2_LDS);
    k1_proj<<<256, 512, G1_LDS, stream>>>(Ax, Awqkv, Bx, Bckv, Bwq, Bwuk, Bwuv, Awo, Bwo, ws);
    k2_attn<<<256, 512, K2_LDS, stream>>>(ws, Awo, Bwo, (__hip_bfloat16*)d_out);
}
```

```cpp
#include <hip/hip_runtime.h>
#include <hip/hip_bf16.h>
#include <stdint.h>
#include <type_traits>

typedef short s16x4 __attribute__((ext_vector_type(4)));
typedef short bf16x8 __attribute__((ext_vector_type(8)));
typedef float f32x16 __attribute__((ext_vector_type(16)));
typedef float f32x4 __attribute__((ext_vector_type(4)));
typedef float f32x2 __attribute__((ext_vector_type(2)));
typedef unsigned u32x4 __attribute__((ext_vector_type(4)));
typedef unsigned u32x2 __attribute__((ext_vector_type(2)));
typedef __bf16 bf16x2_t __attribute__((ext_vector_type(2)));
#define LAS __attribute__((address_space(3)))
#define DI __device__ __forceinline__
#define MFMA32(a, b, c) __builtin_amdgcn_mfma_f32_32x32x16_bf16((a), (b), (c), 0, 0, 0)
#define MFMA16(a, b, c) __builtin_amdgcn_mfma_f32_16x16x32_bf16((a), (b), (c), 0, 0, 0)
#define CFENCE() asm volatile("" ::: "memory")
#define BARRIER() do { CFENCE(); __builtin_amdgcn_s_barrier(); CFENCE(); } while (0)
#define WAIT_VM(n) asm volatile("s_waitcnt vmcnt(" #n ") lgkmcnt(0)" ::: "memory")
#define SBAR() __builtin_amdgcn_sched_barrier(0)

DI unsigned pack2(float a, float b) {
    f32x2 v = {a, b};
    bf16x2_t r = __builtin_convertvector(v, bf16x2_t);
    return __builtin_bit_cast(unsigned, r);
}
DI u32x4 pack8(const f32x16& x, int s) {
    u32x4 w;
    w[0] = pack2(x[8 * s + 0], x[8 * s + 1]);
    w[1] = pack2(x[8 * s + 2], x[8 * s + 3]);
    w[2] = pack2(x[8 * s + 4], x[8 * s + 5]);
    w[3] = pack2(x[8 * s + 6], x[8 * s + 7]);
    return w;
}
DI u32x4 pack8s(const f32x16& x, int s, float c) {
    u32x4 w;
    w[0] = pack2(x[8 * s + 0] * c, x[8 * s + 1] * c);
    w[1] = pack2(x[8 * s + 2] * c, x[8 * s + 3] * c);
    w[2] = pack2(x[8 * s + 4] * c, x[8 * s + 5] * c);
    w[3] = pack2(x[8 * s + 6] * c, x[8 * s + 7] * c);
    return w;
}
DI void glds16(const void* g, LAS char* l) {
    __builtin_amdgcn_global_load_lds((const unsigned*)g, (LAS unsigned*)l, 16, 0, 0);
}
DI bf16x8 tr_frag(LAS char* p0, LAS char* p1) {
    s16x4 lo = __builtin_amdgcn_ds_read_tr16_b64_v4i16((LAS s16x4*)p0);
    s16x4 hi = __builtin_amdgcn_ds_read_tr16_b64_v4i16((LAS s16x4*)p1);
    return __builtin_shufflevector(lo, hi, 0, 1, 2, 3, 4, 5, 6, 7);
}

#define STMODE 2
DI void st16(void* p, u32x4 v) {
    asm volatile("global_store_dwordx4 %0, %1, off sc1\n\ts_nop 1" :: "v"(p), "v"(v) : "memory");
}
template <int I, int N, class F> DI void sfor(F&& f) {
    if constexpr (I < N) { f(std::integral_constant<int, I>{}); sfor<I + 1, N>(f); }
}
template <int OFF> DI void lds_rd(bf16x8& d, unsigned addr) {
    asm volatile("ds_read_b128 %0, %1 offset:%2" : "=v"(d) : "v"(addr), "i"(OFF));
}
template <int N> DI void lds_wait(bf16x8& d) { asm volatile("s_waitcnt lgkmcnt(%1)" : "+v"(d) : "i"(N)); }
template <int N> DI void lds_wait2(bf16x8& d, bf16x8& e) { asm volatile("s_waitcnt lgkmcnt(%2)" : "+v"(d), "+v"(e) : "i"(N)); }
#define LGKM0() __builtin_amdgcn_s_waitcnt(0xC07F)
DI void set_m0(unsigned v) { asm volatile("s_mov_b32 m0, %0\n\ts_nop 0" :: "s"(v) : "memory"); }
template <int OFF> DI void dma_s(unsigned voff, const char* sbase) {
    asm volatile("global_load_lds_dwordx4 %0, %1 offset:%2" :: "v"(voff), "s"(sbase), "i"(OFF) : "memory");
}

#define DYNPRIO 0
#define PRIO(x) do { if (DYNPRIO) __builtin_amdgcn_s_setprio(x); } while (0)
#define ABL_NOPIECE 0
#define ABL_NOBAR 0
#define ABL_T0 0
#define ABL_K2 0
#define ABL_NODMA 0
#define ABL_NOMFMA 0
#define SEG(i) do { } while (0)
#define STAMP(kid, idx) do { } while (0)
#define STAMPW(kid, idx, w) do { } while (0)
constexpr size_t WS_KVB = 0;
constexpr size_t WS_QB = WS_KVB + (size_t)32 * 1048576;
constexpr size_t WS_KVA = WS_QB + (size_t)16 * 1048576;
constexpr size_t WS_QA = WS_KVA + (size_t)8 * 1048576;
constexpr size_t WS_CTXB = WS_QA + (size_t)4 * 1048576;
constexpr size_t WS_CTXA = WS_CTXB + (size_t)16 * 1048576;
constexpr size_t WS_PART = WS_CTXA + (size_t)4 * 1048576;
constexpr size_t WS_CNT = WS_PART + (size_t)32 * 1048576;
constexpr int NSPLIT = 5;
constexpr size_t SLAB = (size_t)64 * 20 * 4096;

constexpr float CB = 0.08838834764831845f * 1.4426950408889634f;
constexpr float CA = 0.22360679774997896f * 1.4426950408889634f;

#define PREFETCH 1
constexpr int G1_LDS = 2 * 65536 + 2048;
enum { EP_BK = 0, EP_BV = 1, EP_BQ = 2, EP_AQK = 3, EP_AV = 4 };

struct G1Src { unsigned xoff[4], woff[4]; const char* Xb; const char* Wb; unsigned wstep; int nkt; bool half; };
DI void g1_src(G1Src& s, const __hip_bfloat16* X, const __hip_bfloat16* W, int K, int ldw, int m0, int n0, int nlim, int wid, int lane, bool half) {
    s.half = half;
#pragma unroll
    for (int i = 0; i < 4; ++i) {
        const int row = (wid * 4 + i) * 8 + (lane >> 3);
        const int c = (lane & 7) ^ ((row >> 1) & 7);
        s.xoff[i] = (unsigned)(((m0 + row) * K + c * 8) * 2) + 4096 - i * 1024;
        const int k = (wid * 4 + i) * 2 + (lane >> 5);
        const int p = lane & 31, b = (p >> 2) ^ (k & 3);
        int col = n0 + (b * 4 + ((p & 3) ^ ((wid & 1) << 1))) * 8;
        col = col < nlim ? col : nlim - 8;
        s.woff[i] = (unsigned)((k * ldw + col) * 2) - i * 1024;
    }
    s.Xb = (const char*)X; s.Wb = (const char*)W; s.wstep = (unsigned)(64 * ldw * 2); s.nkt = K >> 6;
}
template <int I> DI void g1_piece(const G1Src& s, int kt) {
    if constexpr (I < 4) dma_s<I * 1024 - 4096>(s.xoff[I], s.Xb + (size_t)kt * 128);
    else if (!s.half || (threadIdx.x & 16) == 0) dma_s<(I - 4) * 1024>(s.woff[I - 4], s.Wb + (size_t)kt * s.wstep);
}

template <bool TR, int NT>
DI void g1_loop(f32x4 (&acc)[8][4], bf16x8 (&xs)[2][4], bf16x8 (&wsb)[2][4], const G1Src& cur, const G1Src& nxt, bool has_next, bool first,
                LAS char* lds, int wid, int lane) {
    const int wm = wid >> 2, wn = wid & 3, r16 = lane & 15, g = lane >> 4;
    const unsigned lds0 = (unsigned)(uintptr_t)lds;
    const int nkt = cur.nkt;
    int xb[2];
#pragma unroll
    for (int st = 0; st < 2; ++st) xb[st] = wm * 4 * 8192 + r16 * 128 + (((4 * st + g) ^ (r16 >> 1)) * 16);
    const int q = (lane >> 2) & 3, p4 = lane & 3;
    int wb[4];
#pragma unroll
    for (int nt = 0; nt < 4; ++nt) {
        const int b = NT == 4 ? wn * 2 + (nt >> 1) : wn;
        wb[nt] = g * 8192 + 4096 + q * 512 + ((b ^ q) * 64) + (((nt & 1) ^ (g & 1)) * 32) + p4 * 8;
    }
#pragma unroll
    for (int mt = 0; mt < 8; ++mt)
#pragma unroll
        for (int nt = 0; nt < 4; ++nt) acc[mt][nt] = f32x4{};
#define G1_RX(set, bb, st, mh)                                                                                   \
    _Pragma("unroll") for (int m4 = 0; m4 < 4; ++m4)                                                             \
        xs[set][m4] = *(LAS bf16x8*)((bb) + xb[st] + ((mh) * 2 + (m4 >> 1)) * 8192 + (m4 & 1) * 2048)
#define G1_RW(bb, st)                                                                                            \
    _Pragma("unroll") for (int nt = 0; nt < NTR; ++nt)                                                           \
        wsb[st][nt] = tr_frag((bb) + wb[nt] + (st) * 32768, (bb) + wb[nt] + (st) * 32768 + 2048)
#define G1_MM(set, st, mh, on, kn, p0, pe)                                                                       \
    sfor<0, 4>([&](auto mc) {                                                                                    \
        constexpr int m4 = decltype(mc)::value;                                                                  \
        _Pragma("unroll") for (int nt = 0; nt < NT; ++nt)                                                        \
            acc[(mh) * 4 + m4][nt] = TR ? MFMA16(wsb[st][nt], xs[set][m4], acc[(mh) * 4 + m4][nt])               \
                                        : MFMA16(xs[set][m4], wsb[st][nt], acc[(mh) * 4 + m4][nt]);              \
        if constexpr ((pe) > (p0)) {                                                                             \
            SBAR();                                                                                              \
            if (on) {                                                                                            \
                if ((kn) < nkt) { g1_piece<2 * m4>(cur, (kn)); g1_piece<2 * m4 + 1>(cur, (kn)); }                \
                else { g1_piece<2 * m4>(nxt, (kn) - nkt); g1_piece<2 * m4 + 1>(nxt, (kn) - nkt); }               \
            }                                                                                                    \
            SBAR();                                                                                              \
        }                                                                                                        \
    })
    constexpr int NTR = NT;
    G1_RW(lds, 0);
    if (first) { G1_RX(0, lds, 0, 0); }
#pragma unroll 1
    for (int kt = 0; kt < nkt; ++kt) {
        LAS char* bb = lds + (kt & 1) * 65536;
        LAS char* bn = lds + ((kt + 1) & 1) * 65536;
        const bool on2 = kt + 2 < nkt || has_next;
        LGKM0(); G1_RX(1, bb, 0, 1); SBAR(); G1_MM(0, 0, 0, false, 0, 0, 0); SBAR();
        LGKM0(); G1_RW(bb, 1); G1_RX(0, bb, 1, 0); SBAR(); G1_MM(1, 0, 1, false, 0, 0, 0); SBAR();
        LGKM0(); G1_RX(1, bb, 1, 1); SBAR(); G1_MM(0, 1, 0, false, 0, 0, 0); SBAR();
        LGKM0();
        if (first && kt == 0) { WAIT_VM(9); } else { WAIT_VM(0); }
        BARRIER();
        set_m0(lds0 + (kt & 1) * 65536 + wid * 8192 + 4096);
        if (kt + 1 < nkt) G1_RW(bn, 0);
        if (kt + 1 < nkt || has_next) G1_RX(0, bn, 0, 0);
        SBAR(); G1_MM(1, 1, 1, on2, kt + 2, 0, 8); SBAR();
    }
#undef G1_RX
#undef G1_RW
#undef G1_MM
}

DI void st8(void* p, u32x2 v) {
    asm volatile("global_store_dwordx2 %0, %1, off sc1\n\ts_nop 1" :: "v"(p), "v"(v) : "memory");
}

template <int EP>
DI void g1_epi(const f32x4 (&acc)[8][4], int m0, int n0, int nlim, int nsec, char* __restrict__ ws, int wid, int lane) {
    const int wm = wid >> 2, wn = wid & 3, c = lane & 15, g = lane >> 4;
    if constexpr (EP == EP_BK || EP == EP_BQ || EP == EP_BV) {
#pragma unroll
        for (int mt = 0; mt < 8; ++mt) {
            const int tb = (m0 + wm * 128 + mt * 16) >> 5;
            const int tile = tb >> 1, kb = tb & 1, th = mt & 1;
#pragma unroll
            for (int np = 0; np < 2; ++np) {
                const int nb = n0 + wn * 64 + np * 32;
                const int head = nb >> 7;
                const float sc = EP == EP_BQ ? CB : 1.0f;
                const f32x4 a = acc[mt][2 * np], b = acc[mt][2 * np + 1];
                unsigned a0 = pack2(a[0] * sc, a[1] * sc), a1 = pack2(a[2] * sc, a[3] * sc);
                unsigned b0 = pack2(b[0] * sc, b[1] * sc), b1 = pack2(b[2] * sc, b[3] * sc);
                if constexpr (EP == EP_BV) {
                    auto r0 = __builtin_amdgcn_permlane32_swap(a0, b0, false, false);
                    auto r1 = __builtin_amdgcn_permlane32_swap(a1, b1, false, false);
                    const u32x4 w = {r0[0], r1[0], r0[1], r1[1]};
                    const int db = (nb >> 5) & 3;
                    char* base = ws + WS_KVB + (size_t)head * 1048576 + tile * 32768 + 16384 + ((kb * 2 + th) * 4 + db) * 1024;
                    st16(base + ((g >> 1) * 16 + c + 32 * (g & 1)) * 16, w);
                } else {
                    auto r0 = __builtin_amdgcn_permlane16_swap(a0, b0, false, false);
                    auto r1 = __builtin_amdgcn_permlane16_swap(a1, b1, false, false);
                    const u32x4 w = {r0[0], r1[0], r0[1], r1[1]};
                    const int ks = ((nb >> 4) & 7) + (g & 1);
                    char* base = EP == EP_BK ? ws + WS_KVB + (size_t)head * 1048576 + tile * 32768 + (kb * 8 + ks) * 1024
                                             : ws + WS_QB + (size_t)((head * 64 + tb) * 8 + ks) * 1024;
                    st16(base + (th * 16 + c + 32 * (g >> 1)) * 16, w);
                }
            }
        }
        return;
    }
#pragma unroll
    for (int mt = 0; mt < 8; ++mt) {
        const int tb = (m0 + wm * 128 + mt * 16) >> 5;
        const int tile = tb >> 1, kb = tb & 1, th = mt & 1;
#pragma unroll
        for (int nt = 0; nt < 4; ++nt) {
            const int nb = n0 + wn * 64 + nt * 16;
            if (nb >= nlim) continue;
            const f32x4 a = acc[mt][nt];
            if constexpr (EP == EP_BK || EP == EP_BQ || EP == EP_BV) {
            } else if constexpr (EP == EP_AQK) {
                const bool isq = nb < 640;
                const float sc = isq ? CA : 1.0f;
                const int r = th * 16 + c;
                const int n = nb - (isq ? 0 : 640) + 4 * g;
                const int head = n / 20, d0 = n - head * 20;
                char* base = isq ? ws + WS_QA + (size_t)((head * 64 + tb) * 2) * 1024
                                 : ws + WS_KVA + (size_t)head * 262144 + tile * 8192 + kb * 2048;
                char* dst = base + (d0 >> 4) * 1024 + (r + 32 * ((d0 >> 3) & 1)) * 16 + (d0 & 4) * 2;
                u32x2 w;
                w[0] = pack2(a[0] * sc, a[1] * sc);
                w[1] = pack2(a[2] * sc, a[3] * sc);
                *(u32x2*)dst = w;
                if (d0 == 16) {
                    *(u32x2*)(dst + 8) = (u32x2){isq ? 0u : 0x3F803F80u, 0u};
                    *(u32x4*)(base + 1024 + (r + 32) * 16) = (u32x4){0u, 0u, 0u, 0u};
                }
            } else {
                const int n = nb - nsec + c;
                const int head = n / 20, d = n - head * 20;
                char* dst = ws + WS_KVA + (size_t)head * 262144 + tile * 8192 + 4096 + (kb * 2 + th) * 1024 + (d + 32 * (g & 1)) * 16 + (g >> 1) * 8;
                u32x2 w;
                w[0] = pack2(a[0], a[1]);
                w[1] = pack2(a[2], a[3]);
                *(u32x2*)dst = w;
                if (d == 19) *(u32x2*)(dst + 16) = (u32x2){0x3F803F80u, 0x3F803F80u};
            }
        }
    }
}

__global__ __launch_bounds__(512, 2) void k1_proj(const __hip_bfloat16* __restrict__ Ax, const __hip_bfloat16* __restrict__ Awqkv,
                                                  const __hip_bfloat16* __restrict__ Bx, const __hip_bfloat16* __restrict__ Bckv,
                                                  const __hip_bfloat16* __restrict__ Bwq, const __hip_bfloat16* __restrict__ Bwuk,
                                                  const __hip_bfloat16* __restrict__ Bwuv, const __hip_bfloat16* __restrict__ Awo,
                                                  const __hip_bfloat16* __restrict__ Bwo, char* __restrict__ ws) {
    extern __shared__ __attribute__((aligned(16))) char smem[];
    LAS char* lds = (LAS char*)smem;
    const int tid = threadIdx.x, lane = tid & 63, wid = __builtin_amdgcn_readfirstlane(tid >> 6);
    const int bid = blockIdx.x;
    const int m0 = ((bid >> 3) & 7) * 256;
    const int grp = bid >> 6, c8 = bid & 7;
    const int cc = c8 + 8 * (grp & 1);
    const bool has2 = grp < 3;
    G1Src s1, s2;
    if (grp < 2) {
        g1_src(s1, Bckv, Bwuk, 512, 4096, m0, cc * 256, 4096, wid, lane, false);
        g1_src(s2, Bx, Bwq, 640, 4096, m0, cc * 256, 4096, wid, lane, false);
    } else if (grp == 2) {
        g1_src(s1, Bckv, Bwuv, 512, 4096, m0, c8 * 256, 4096, wid, lane, false);
        g1_src(s2, Bckv, Bwuv, 512, 4096, m0, (c8 + 8) * 256, 4096, wid, lane, false);
    } else {
        g1_src(s1, Ax, Awqkv, 640, 1920, m0, c8 * 256, c8 < 5 ? 1280 : 1920, wid, lane, false);
        s2 = s1;
    }

    f32x4 acc[8][4];
    bf16x8 xf[2][4], wf[2][4];
    const unsigned lds0 = (unsigned)(uintptr_t)lds;
    if (bid == 255 && tid < 40) ((unsigned*)(ws + WS_CNT))[tid * 64] = 0u;
    STAMP(0, 0);
    set_m0(lds0 + wid * 8192 + 4096);
    sfor<0, 8>([&](auto ic) { g1_piece<decltype(ic)::value>(s1, 0); });
    set_m0(lds0 + 65536 + wid * 8192 + 4096);
    sfor<0, 8>([&](auto ic) { g1_piece<decltype(ic)::value>(s1, 1); });
    const unsigned gw = (unsigned)(bid * 8 + wid) * 64 + lane;
#define PF(ptr, nbytes) do { constexpr unsigned R_ = 131072u / ((nbytes) / 128u);                                     \
            const unsigned ln_ = gw / R_;                                                                                \
            const unsigned off_ = (gw % R_ == 0u && ln_ * 128u < (nbytes)) ? ln_ * 128u : 0u;                             \
            asm volatile("global_load_lds_dword %0, %1" :: "v"(off_), "s"((const char*)(ptr)) : "memory"); } while (0)
    set_m0(lds0 + 131072 + wid * 256);
    PF(Bckv, 2048u * 512u * 2u); PF(Bwuk, 512u * 4096u * 2u); PF(Bwuv, 512u * 4096u * 2u);
    PF(Ax, 2048u * 640u * 2u); PF(Awqkv, 640u * 1920u * 2u);
    WAIT_VM(13);
    BARRIER();
    STAMP(0, 1);
    PF(Bx, 2048u * 640u * 2u); PF(Bwq, 640u * 4096u * 2u);
    PF(Bwo, 4096u * 640u * 2u); PF(Awo, 640u * 640u * 2u);
#undef PF
    if (grp < 2 || (grp == 3 && c8 < 5)) {
        g1_loop<true, 4>(acc, xf, wf, s1, s2, has2, true, lds, wid, lane);
        if (grp < 2) g1_epi<EP_BK>(acc, m0, cc * 256, 4096, 0, ws, wid, lane);
        else g1_epi<EP_AQK>(acc, m0, c8 * 256, 1280, 0, ws, wid, lane);
    } else {
        g1_loop<false, 4>(acc, xf, wf, s1, s2, has2, true, lds, wid, lane);
        if (grp == 2) g1_epi<EP_BV>(acc, m0, c8 * 256, 4096, 0, ws, wid, lane);
        else g1_epi<EP_AV>(acc, m0, c8 * 256, 1920, 1280, ws, wid, lane);
    }
    STAMP(0, 2);
    if (grp < 2) {
        g1_loop<true, 4>(acc, xf, wf, s2, s2, false, false, lds, wid, lane);
        g1_epi<EP_BQ>(acc, m0, cc * 256, 4096, 0, ws, wid, lane);
    } else if (grp == 2) {
        g1_loop<false, 4>(acc, xf, wf, s2, s2, false, false, lds, wid, lane);
        g1_epi<EP_BV>(acc, m0, (c8 + 8) * 256, 4096, 0, ws, wid, lane);
    }
    STAMP(0, 3);
}

constexpr int SLOT = 40960;
constexpr int NSLOT = 4;
constexpr int K2_LDS = NSLOT * SLOT;
constexpr int NIT = 34;

__global__ __launch_bounds__(512, 2) void k2_attn(char* __restrict__ ws) {
    extern __shared__ __attribute__((aligned(16))) char smem[];
    LAS char* lds = (LAS char*)smem;
    const int tid = threadIdx.x, lane = tid & 63, wid = __builtin_amdgcn_readfirstlane(tid >> 6);
    const int r = lane & 31, h = lane >> 5;
    const int g = blockIdx.x, xcd = g & 7, idx = g >> 3;
    const int head = xcd * 4 + (idx >> 3), x = idx & 7;
    const char* kvb = ws + WS_KVB + (size_t)head * 1048576;
    const char* kva = ws + WS_KVA + (size_t)head * 262144;
    const int n1 = 32 - 2 * x;
    const unsigned lbase = (unsigned)(uintptr_t)lds + lane * 16;

    STAMP(1, 0);
    const unsigned lds0 = (unsigned)(uintptr_t)lds;
    const unsigned voff = lane * 16;
    const int wa = wid & 3;
    const char* sbB = kvb;
    const char* sbA = kva;
#define K2_M0(tt)                                                                           \
    do {                                                                                    \
        const int t3_ = (tt) < NIT ? (tt) : NIT - 1;                                        \
        const int tb3_ = t3_ < n1 ? t3_ : t3_ - n1;                                         \
        const int ta3_ = t3_ < 32 ? t3_ : 31;                                               \
        sbB = kvb + (size_t)tb3_ * 32768 + wid * 4096 + 2048;                               \
        sbA = kva + (size_t)ta3_ * 8192 + wid * 1024 - 2048;                                \
        set_m0(lds0 + ((tt) < NIT ? ((tt) & 3) : 2 + ((tt) & 1)) * SLOT + wid * 5120 + 2048); \
    } while (0)
#define K2_DMA(i) do { SBAR(); if constexpr ((i) < 4) dma_s<(i) * 1024 - 2048>(voff, sbB); else dma_s<2048>(voff, sbA); SBAR(); } while (0)
#define K2_STAGE_ALL(tt) do { K2_M0(tt); K2_DMA(0); K2_DMA(1); K2_DMA(2); K2_DMA(3); K2_DMA(4); } while (0)
#define BOFF(f) (((f) >> 2) * 5120 + ((f) & 3) * 1024)
#define AOFF(a) ((a) * 5120 + 4096)
    K2_STAGE_ALL(0);
    K2_STAGE_ALL(1);

#define K2_SYNC(it) do { WAIT_VM(5); BARRIER(); K2_M0((it) + 3); } while (0)

    if (wid < 4) {
        bf16x8 fr[8];
        K2_SYNC(-1);
        STAMP(1, 1);
        sfor<0, 8>([&](auto ic) { constexpr int j = decltype(ic)::value; lds_rd<BOFF((j & 1) * 8 + (j >> 1))>(fr[j], lbase); });
        K2_DMA(0); K2_DMA(1);
        PRIO(1);
        int it = 0;
        for (int blk = 0; blk < 2; ++blk) {
            const int qb = blk == 0 ? 15 - x : x;
            const int nt = 2 * qb + 2;
            const int q0 = qb * 128 + wid * 32;
            const int tb32 = q0 >> 5;
            bf16x8 qf[8];
            const char* qp = ws + WS_QB + (size_t)((head * 64 + tb32) * 8) * 1024 + lane * 16;
#pragma unroll
            for (int ks = 0; ks < 8; ++ks) qf[ks] = *(const bf16x8*)(qp + ks * 1024);
            f32x16 o[4];
#pragma unroll
            for (int db = 0; db < 4; ++db) o[db] = f32x16{};
            f32x16 negm = f32x16{};
            float l = 0.f;
            for (int t = 0; t < nt; ++t, ++it) {
                const int rel = (q0 - 64 * t) >> 5;
                const unsigned ac = lbase + (it & 3) * SLOT;
                const unsigned an = lbase + ((it + 1) & 3) * SLOT;
                f32x16 s0, s1;
                SEG(0);
                sfor<0, 16>([&](auto ic) {
                    constexpr int j = decltype(ic)::value, ks = j >> 1;
                    lds_wait<7>(fr[j & 7]);
                    if constexpr (j == 0) s0 = MFMA32(fr[0], qf[0], negm);
                    else if constexpr (j == 1) s1 = MFMA32(fr[1], qf[0], negm);
                    else if constexpr ((j & 1) == 0) s0 = MFMA32(fr[j & 7], qf[ks], s0);
                    else s1 = MFMA32(fr[j & 7], qf[ks], s1);
                    constexpr int jn = j + 8;
                    SBAR();
                    if constexpr (jn < 16) lds_rd<BOFF((jn & 1) * 8 + (jn >> 1))>(fr[j & 7], ac);
                    else lds_rd<BOFF(jn)>(fr[j & 7], ac);
                    if constexpr (j == 2) K2_DMA(2);
                    if constexpr (j == 8) K2_DMA(3);
                    if constexpr (j == 14) K2_DMA(4);
                    SBAR();
                });
#define B_MASK()                                                                                        \
    do {                                                                                                \
        if (rel <= 1) {                                                                                 \
            const int lim = 32 * rel + r - 4 * h;                                                       \
            _Pragma("unroll") for (int i = 0; i < 16; ++i) {                                            \
                const int kr = (i & 3) + 8 * (i >> 2);                                                  \
                if (kr > lim) s0[i] = -__builtin_inff();                                                \
                if (kr + 32 > lim) s1[i] = -__builtin_inff();                                           \
            }                                                                                           \
        }                                                                                               \
    } while (0)
                SEG(1);
                PRIO(0);
                B_MASK();
                float ps = 0.f;
                if (t > 0) {
#pragma unroll
                    for (int i = 0; i < 16; ++i) { s0[i] = __builtin_amdgcn_exp2f(s0[i]); ps += s0[i]; }
#pragma unroll
                    for (int i = 0; i < 16; ++i) { s1[i] = __builtin_amdgcn_exp2f(s1[i]); ps += s1[i]; }
                }
                if (t == 0 || __any(!(ps <= 65536.f))) {
                    if (t > 0) {
                        LAS char* sl = lds + (it & 3) * SLOT + lane * 16;
                        s0 = negm; s1 = negm;
#pragma unroll 1
                        for (int ks = 0; ks < 8; ++ks) {
                            bf16x8 qq = qf[0];
#pragma unroll
                            for (int u = 1; u < 8; ++u) qq = (ks == u) ? qf[u] : qq;
                            s0 = MFMA32(*(LAS bf16x8*)(sl + (ks >> 2) * 5120 + (ks & 3) * 1024), qq, s0);
                            s1 = MFMA32(*(LAS bf16x8*)(sl + 10240 + (ks >> 2) * 5120 + (ks & 3) * 1024), qq, s1);
                        }
                        B_MASK();
                    }
                    float mx = s0[0];
#pragma unroll
                    for (int i = 1; i < 16; ++i) mx = fmaxf(mx, s0[i]);
#pragma unroll
                    for (int i = 0; i < 16; ++i) mx = fmaxf(mx, s1[i]);
                    auto rr = __builtin_amdgcn_permlane32_swap(__float_as_uint(mx), __float_as_uint(mx), false, false);
                    mx = fmaxf(__uint_as_float(rr[0]), __uint_as_float(rr[1]));
                    float delta = mx;
                    if (t > 0) {
                        delta = fmaxf(mx, 0.f);
                        const float alpha = __builtin_amdgcn_exp2f(-delta);
                        l *= alpha;
#pragma unroll
                        for (int db = 0; db < 4; ++db)
#pragma unroll
                            for (int i = 0; i < 16; ++i) o[db][i] *= alpha;
                    }
                    const float nm = negm[0] - delta;
#pragma unroll
                    for (int i = 0; i < 16; ++i) negm[i] = nm;
                    ps = 0.f;
#pragma unroll
                    for (int i = 0; i < 16; ++i) { s0[i] = __builtin_amdgcn_exp2f(s0[i] - delta); ps += s0[i]; }
#pragma unroll
                    for (int i = 0; i < 16; ++i) { s1[i] = __builtin_amdgcn_exp2f(s1[i] - delta); ps += s1[i]; }
                }
                l += ps;
                bf16x8 pf[4];
                pf[0] = __builtin_bit_cast(bf16x8, pack8(s0, 0));
                pf[1] = __builtin_bit_cast(bf16x8, pack8(s0, 1));
                pf[2] = __builtin_bit_cast(bf16x8, pack8(s1, 0));
                pf[3] = __builtin_bit_cast(bf16x8, pack8(s1, 1));
                SEG(2);
                if (it + 1 < NIT) { K2_SYNC(it); } else { K2_M0(it + 3); }
                SEG(3);
                PRIO(1);
                sfor<0, 16>([&](auto ic) {
                    constexpr int j = decltype(ic)::value, s4 = j >> 2, db = j & 3;
                    lds_wait<7>(fr[j & 7]);
                    o[db] = MFMA32(fr[j & 7], pf[s4], o[db]);
                    SBAR();
                    if constexpr (j < 8) lds_rd<BOFF(24 + j)>(fr[j & 7], ac);
                    else { constexpr int jn = j - 8; lds_rd<BOFF((jn & 1) * 8 + (jn >> 1))>(fr[j & 7], an); }
                    if constexpr (j == 4) K2_DMA(0);
                    if constexpr (j == 10) K2_DMA(1);
                    SBAR();
                });
                SEG(4);
            }
            auto rr = __builtin_amdgcn_permlane32_swap(__float_as_uint(l), __float_as_uint(l), false, false);
            const float inv = 1.0f / (__uint_as_float(rr[0]) + __uint_as_float(rr[1]));
            char* cp = ws + WS_CTXB + (size_t)(tb32 * 256 + head * 8) * 1024 + lane * 16;
#pragma unroll
            for (int db = 0; db < 4; ++db) {
                st16(cp + (2 * db) * 1024, pack8s(o[db], 0, inv));
                st16(cp + (2 * db + 1) * 1024, pack8s(o[db], 1, inv));
            }
        }
        WAIT_VM(0);
        STAMP(1, 2);
#undef B_MASK
    } else {
        const int tb32 = x * 8 + wa * 2;
        bf16x8 qf[2][2];
        const char* qp = ws + WS_QA + (size_t)((head * 64 + tb32) * 2) * 1024 + lane * 16;
#pragma unroll
        for (int qt = 0; qt < 2; ++qt)
#pragma unroll
            for (int ks = 0; ks < 2; ++ks) qf[qt][ks] = *(const bf16x8*)(qp + (qt * 2 + ks) * 1024);
        f32x16 o[2];
        o[0] = f32x16{}; o[1] = f32x16{};
        float negm[2] = {0.f, 0.f};
        bf16x8 kf[4], vf[4];
        f32x16 s[2][2];
        K2_SYNC(-1);
        lds_rd<AOFF(0)>(kf[0], lbase); lds_rd<AOFF(1)>(kf[1], lbase); lds_rd<AOFF(2)>(kf[2], lbase); lds_rd<AOFF(3)>(kf[3], lbase);
        for (int it = 0; it <= 32; ++it) {
            SEG(0);
            if (it > 0) {
                u32x4 pf[2][2][2];
                unsigned chk = 0;
                sfor<0, 4>([&](auto bc) {
                    constexpr int blk = decltype(bc)::value, kb = blk >> 1, qt = blk & 1;
                    f32x16 e;
#pragma unroll
                    for (int i = 0; i < 8; ++i) e[i] = __builtin_amdgcn_exp2f(s[kb][qt][i]);
                    K2_DMA(blk);
#pragma unroll
                    for (int i = 8; i < 16; ++i) e[i] = __builtin_amdgcn_exp2f(s[kb][qt][i]);
                    if constexpr (blk == 3) K2_DMA(4);
                    pf[kb][qt][0] = pack8(e, 0);
                    pf[kb][qt][1] = pack8(e, 1);
                    chk |= pf[kb][qt][0][0] | pf[kb][qt][0][1] | pf[kb][qt][0][2] | pf[kb][qt][0][3];
                    chk |= pf[kb][qt][1][0] | pf[kb][qt][1][1] | pf[kb][qt][1][2] | pf[kb][qt][1][3];
                });
                const bool first = (it == 1);
                if (first || __any((chk & 0x40004000u) != 0u)) {
#pragma unroll
                    for (int qt = 0; qt < 2; ++qt) {
                        float mx = s[0][qt][0];
#pragma unroll
                        for (int i = 1; i < 16; ++i) mx = fmaxf(mx, s[0][qt][i]);
#pragma unroll
                        for (int i = 0; i < 16; ++i) mx = fmaxf(mx, s[1][qt][i]);
                        auto rr = __builtin_amdgcn_permlane32_swap(__float_as_uint(mx), __float_as_uint(mx), false, false);
                        mx = fmaxf(__uint_as_float(rr[0]), __uint_as_float(rr[1]));
                        float delta = mx + 10.f;
                        if (!first) delta = fmaxf(delta, 0.f);
                        const float tgt = negm[qt] - delta;
                        const unsigned hi = pack2(tgt, 0.f) & 0xFFFFu;
                        const float hif = __uint_as_float(hi << 16);
                        const unsigned lo = pack2(tgt - hif, 0.f) & 0xFFFFu;
                        const float neff = hif + __uint_as_float(lo << 16);
                        delta = negm[qt] - neff;
                        negm[qt] = neff;
                        {
                            u32x4 qq = __builtin_bit_cast(u32x4, qf[qt][1]);
                            qq[2] = h ? 0u : (hi | (lo << 16));
                            qf[qt][1] = __builtin_bit_cast(bf16x8, qq);
                        }
                        if (!first) {
                            const float alpha = __builtin_amdgcn_exp2f(-delta);
#pragma unroll
                            for (int i = 0; i < 16; ++i) o[qt][i] *= alpha;
                        }
#pragma unroll
                        for (int kb = 0; kb < 2; ++kb) {
                            f32x16 e;
#pragma unroll
                            for (int i = 0; i < 16; ++i) e[i] = __builtin_amdgcn_exp2f(s[kb][qt][i] - delta);
                            pf[kb][qt][0] = pack8(e, 0);
                            pf[kb][qt][1] = pack8(e, 1);
                        }
                    }
                }
                SEG(1);
                PRIO(1);
                lds_wait2<4>(vf[0], vf[1]);
                lds_wait2<4>(vf[2], vf[3]);
#pragma unroll
                for (int kb = 0; kb < 2; ++kb)
#pragma unroll
                    for (int s2 = 0; s2 < 2; ++s2)
#pragma unroll
                        for (int qt = 0; qt < 2; ++qt)
                            o[qt] = MFMA32(vf[kb * 2 + s2], __builtin_bit_cast(bf16x8, pf[kb][qt][s2]), o[qt]);
            } else {
                K2_DMA(0); K2_DMA(1); K2_DMA(2); K2_DMA(3); K2_DMA(4);
            }
            SEG(2);
            lds_wait2<0>(kf[0], kf[1]);
            lds_wait2<0>(kf[2], kf[3]);
#pragma unroll
            for (int kb = 0; kb < 2; ++kb)
#pragma unroll
                for (int qt = 0; qt < 2; ++qt) {
                    s[kb][qt] = MFMA32(kf[kb * 2], qf[qt][0], f32x16{});
                    s[kb][qt] = MFMA32(kf[kb * 2 + 1], qf[qt][1], s[kb][qt]);
                }
            SBAR();
            if (it == 32) STAMPW(1, 3, 4);
            SEG(3);
            PRIO(0);
            K2_SYNC(it);
            SEG(4);
            const unsigned ac = lbase + (it & 3) * SLOT;
            const unsigned an = lbase + ((it + 1) & 3) * SLOT;
            lds_rd<AOFF(4)>(vf[0], ac); lds_rd<AOFF(5)>(vf[1], ac); lds_rd<AOFF(6)>(vf[2], ac); lds_rd<AOFF(7)>(vf[3], ac);
            lds_rd<AOFF(0)>(kf[0], an); lds_rd<AOFF(1)>(kf[1], an); lds_rd<AOFF(2)>(kf[2], an); lds_rd<AOFF(3)>(kf[3], an);
        }
        WAIT_VM(0);
#pragma unroll
        for (int qt = 0; qt < 2; ++qt) {
            const unsigned lv = __float_as_uint(o[qt][8]);
            auto rr = __builtin_amdgcn_permlane32_swap(lv, lv, false, false);
            const float lsum = __uint_as_float(rr[1]);
            const float inv = 1.0f / lsum;
            char* cp = ws + WS_CTXA + (size_t)((tb32 + qt) * 40) * 1024;
#pragma unroll
            for (int gq = 0; gq < 3; ++gq) {
                const int d0 = 8 * gq + 4 * h;
                if (d0 < 20) {
                    const int k = head * 20 + d0;
                    u32x2 w;
                    w[0] = pack2(o[qt][4 * gq + 0] * inv, o[qt][4 * gq + 1] * inv);
                    w[1] = pack2(o[qt][4 * gq + 2] * inv, o[qt][4 * gq + 3] * inv);
                    *(u32x2*)(cp + (k >> 4) * 1024 + (r + 32 * ((k >> 3) & 1)) * 16 + (k & 4) * 2) = w;
                }
            }
        }
    }
}

constexpr int G3_STAGE = 49152;
constexpr int G3_LDS = 3 * G3_STAGE;

__global__ __launch_bounds__(512, 2) void k3_oproj(const __hip_bfloat16* __restrict__ Awo, const __hip_bfloat16* __restrict__ Bwo,
                                                   char* __restrict__ ws, __hip_bfloat16* __restrict__ out) {
    extern __shared__ __attribute__((aligned(16))) char smem[];
    LAS char* lds = (LAS char*)smem;
    const int tid = threadIdx.x, lane = tid & 63, wid = __builtin_amdgcn_readfirstlane(tid >> 6);
    const int wm = wid >> 1, wn = wid & 1, h = lane >> 5;
    const int bid = blockIdx.x;
    const bool isB = bid < 200;
    int mt, nt, sp, kt0, nkt, kstot;
    const char* Xc; const char* Wb;
    if (isB) {
        mt = bid & 7; const int rest = bid >> 3; nt = rest % 5; sp = rest / 5;
        kt0 = sp * 13; nkt = sp == 4 ? 12 : 13; kstot = 256;
        Xc = ws + WS_CTXB; Wb = (const char*)Bwo;
    } else {
        const int b2 = bid - 200; mt = b2 & 7; nt = b2 >> 3; sp = 5;
        kt0 = 0; nkt = 10; kstot = 40;
        Xc = ws + WS_CTXA; Wb = (const char*)Awo;
    }
    unsigned woff[2];
#pragma unroll
    for (int i = 0; i < 2; ++i) {
        const int k = (wid * 2 + i) * 4 + (lane >> 4);
        const int p = lane & 15, b = (p >> 2) ^ (k & 3);
        woff[i] = (unsigned)(((kt0 * 64 + k) * 640 + nt * 128 + (b * 4 + (p & 3)) * 8) * 2);
    }
    const unsigned lds0 = (unsigned)(uintptr_t)lds;
    const char* xsb = Xc + (size_t)((mt * 8 + wid) * kstot + kt0 * 4) * 1024 + 4096;
    const unsigned voff = lane * 16;
    woff[1] -= 1024;
    auto piece = [&](int kt, auto ic) {
        constexpr int i = decltype(ic)::value;
        if constexpr (i < 4) dma_s<i * 1024 - 4096>(voff, xsb + (size_t)kt * 4096);
        else dma_s<(i - 4) * 1024>(woff[i - 4], Wb + (size_t)kt * (64u * 640u * 2u));
    };
    const int q = (lane >> 2) & 3, p4 = lane & 3, g16 = (lane >> 4) & 1;
    const int r0 = isB ? 4 * h : 8 * h, r1 = isB ? 8 + 4 * h : 8 * h + 4;
    int wb0[2], wb1[2];
#pragma unroll
    for (int ni = 0; ni < 2; ++ni) {
        const int b = wn * 2 + ni;
        const int c = ((b ^ q) * 64) + g16 * 32 + p4 * 8;
        wb0[ni] = (r0 >> 3) * 6144 + 4096 + ((r0 & 7) + q) * 256 + c;
        wb1[ni] = (r1 >> 3) * 6144 + 4096 + ((r1 & 7) + q) * 256 + c;
    }
    const int xb0 = wm * 2 * 6144 + lane * 16;
    f32x16 acc[2][2];
#pragma unroll
    for (int mi = 0; mi < 2; ++mi)
#pragma unroll
        for (int ni = 0; ni < 2; ++ni) acc[mi][ni] = f32x16{};

    bf16x8 xf[2][4][2], wf[2][4][2];
#define G3_RD(set, bb)                                                                                           \
    do {                                                                                                         \
        _Pragma("unroll") for (int ks = 0; ks < 4; ++ks) {                                                       \
            _Pragma("unroll") for (int ni = 0; ni < 2; ++ni)                                                     \
                wf[set][ks][ni] = tr_frag((bb) + wb0[ni] + ks * 12288, (bb) + wb1[ni] + ks * 12288);             \
            _Pragma("unroll") for (int mi = 0; mi < 2; ++mi)                                                     \
                xf[set][ks][mi] = *(LAS bf16x8*)((bb) + xb0 + mi * 6144 + ks * 1024);                            \
        }                                                                                                        \
    } while (0)
#define G3_STEP(set, kt)                                                                                         \
    do {                                                                                                         \
        if ((kt) + 2 < nkt) { WAIT_VM(6); } else { WAIT_VM(0); }                                                 \
        LGKM0();                                                                                                 \
        BARRIER();                                                                                               \
        const int b0_ = (kt) % 3, b1_ = ((kt) + 1) % 3;                                                          \
        set_m0(lds0 + b0_ * G3_STAGE + wid * 6144 + 4096);                                                       \
        if ((kt) + 1 < nkt) G3_RD((set) ^ 1, lds + b1_ * G3_STAGE);                                              \
        SBAR();                                                                                                  \
        const bool on_ = (kt) + 3 < nkt;                                                                         \
        sfor<0, 8>([&](auto jc) {                                                                                \
            constexpr int j = decltype(jc)::value, ks = j >> 1, mi = j & 1;                                      \
            _Pragma("unroll") for (int ni = 0; ni < 2; ++ni)                                                     \
                acc[mi][ni] = MFMA32(wf[set][ks][ni], xf[set][ks][mi], acc[mi][ni]);                             \
            if constexpr (j < 6) { SBAR(); if (on_) piece((kt) + 3, jc); SBAR(); }                               \
        });                                                                                                      \
        SBAR();                                                                                                  \
    } while (0)
    STAMP(2, 0);
#pragma unroll
    for (int t = 0; t < 3; ++t) {
        set_m0(lds0 + t * G3_STAGE + wid * 6144 + 4096);
        sfor<0, 6>([&](auto ic) { piece(t, ic); });
    }
    WAIT_VM(12);
    BARRIER();
    STAMP(2, 1);
    G3_RD(0, lds);
    int kt = 0;
#pragma unroll 1
    for (; kt + 1 < nkt; kt += 2) { G3_STEP(0, kt); G3_STEP(1, kt + 1); }
    if (kt < nkt) G3_STEP(0, kt);
#undef G3_RD
#undef G3_STEP
    STAMP(2, 2);
    const int r = lane & 31;
    if (!isB) {
#pragma unroll
        for (int mi = 0; mi < 2; ++mi)
#pragma unroll
            for (int ni = 0; ni < 2; ++ni) {
                char* op = (char*)out + (size_t)((mt * 8 + wm * 2 + mi) * 32 + r) * 2560 + ((nt * 4 + wn * 2 + ni) * 32 + 4 * h) * 2;
#pragma unroll
                for (int gq = 0; gq < 4; ++gq) {
                    u32x2 o;
                    o[0] = pack2(acc[mi][ni][4 * gq], acc[mi][ni][4 * gq + 1]);
                    o[1] = pack2(acc[mi][ni][4 * gq + 2], acc[mi][ni][4 * gq + 3]);
                    *(u32x2*)(op + gq * 16) = o;
                }
            }
        STAMP(2, 3);
        return;
    }
#pragma unroll
    for (int mi = 0; mi < 2; ++mi)
#pragma unroll
        for (int ni = 0; ni < 2; ++ni) {
            const int st = (wm * 2 + mi) * 4 + wn * 2 + ni;
            if (st % 5 == sp) continue;
            const int mt32 = mt * 8 + wm * 2 + mi, nt32 = nt * 4 + wn * 2 + ni;
            char* dst = ws + WS_PART + (size_t)sp * SLAB + (size_t)(mt32 * 20 + nt32) * 4096 + lane * 16;
#pragma unroll
            for (int gq = 0; gq < 4; ++gq) {
                f32x4 v = {acc[mi][ni][4 * gq], acc[mi][ni][4 * gq + 1], acc[mi][ni][4 * gq + 2], acc[mi][ni][4 * gq + 3]};
                asm volatile("global_store_dwordx4 %0, %1, off sc1\n\ts_nop 1" :: "v"(dst + gq * 1024), "v"(v) : "memory");
            }
        }
    asm volatile("s_waitcnt vmcnt(0)" ::: "memory");
    BARRIER();
    unsigned* cnt = (unsigned*)(ws + WS_CNT) + (mt * 5 + nt) * 64;
    if (wid == 0) {
        if (lane == 0) __hip_atomic_fetch_add(cnt, 1u, __ATOMIC_RELAXED, __HIP_MEMORY_SCOPE_AGENT);
        for (unsigned spins = 0; spins < (1u << 24); ++spins) {
            const unsigned c = __builtin_amdgcn_readfirstlane(__hip_atomic_load(cnt, __ATOMIC_RELAXED, __HIP_MEMORY_SCOPE_AGENT));
            if (c >= (unsigned)NSPLIT) break;
            __builtin_amdgcn_s_sleep(1);
        }
    }
    BARRIER();
    const auto rsrc = __builtin_amdgcn_make_buffer_rsrc((void*)(ws + WS_PART), 0, (int)(NSPLIT * SLAB), 0x00020000);
#pragma unroll
    for (int mi = 0; mi < 2; ++mi)
#pragma unroll
        for (int ni = 0; ni < 2; ++ni) {
            const int st = (wm * 2 + mi) * 4 + wn * 2 + ni;
            if (st % 5 != sp) continue;
            const int mt32 = mt * 8 + wm * 2 + mi, nt32 = nt * 4 + wn * 2 + ni;
            const int off = (mt32 * 20 + nt32) * 4096 + lane * 16;
            u32x4 v[4][4];
#pragma unroll
            for (int s = 0; s < 4; ++s) {
                const int so = (s < sp ? s : s + 1) * (int)SLAB;
#pragma unroll
                for (int gq = 0; gq < 4; ++gq) v[s][gq] = __builtin_amdgcn_raw_buffer_load_b128(rsrc, off + gq * 1024, so, 16);
            }
            char* op = (char*)out + (size_t)(mt32 * 32 + r) * 2560 + (640 + nt32 * 32 + 4 * h) * 2;
#pragma unroll
            for (int gq = 0; gq < 4; ++gq) {
                f32x4 t = {acc[mi][ni][4 * gq], acc[mi][ni][4 * gq + 1], acc[mi][ni][4 * gq + 2], acc[mi][ni][4 * gq + 3]};
                t += (__builtin_bit_cast(f32x4, v[0][gq]) + __builtin_bit_cast(f32x4, v[1][gq])) +
                     (__builtin_bit_cast(f32x4, v[2][gq]) + __builtin_bit_cast(f32x4, v[3][gq]));
                u32x2 o;
                o[0] = pack2(t[0], t[1]);
                o[1] = pack2(t[2], t[3]);
                *(u32x2*)(op + gq * 16) = o;
            }
        }
    STAMP(2, 3);
}

extern "C" void kernel_launch(void* const* d_in, const int* in_sizes, int n_in,
                              void* d_out, int out_size, void* d_ws, size_t ws_size,
                              hipStream_t stream) {
    (void)in_sizes; (void)n_in; (void)out_size; (void)ws_size;
    const __hip_bfloat16* Ax = (const __hip_bfloat16*)d_in[0];
    const __hip_bfloat16* Awqkv = (const __hip_bfloat16*)d_in[1];
    const __hip_bfloat16* Awo = (const __hip_bfloat16*)d_in[2];
    const __hip_bfloat16* Bx = (const __hip_bfloat16*)d_in[3];
    const __hip_bfloat16* Bckv = (const __hip_bfloat16*)d_in[4];
    const __hip_bfloat16* Bwq = (const __hip_bfloat16*)d_in[5];
    const __hip_bfloat16* Bwuk = (const __hip_bfloat16*)d_in[6];
    const __hip_bfloat16* Bwuv = (const __hip_bfloat16*)d_in[7];
    const __hip_bfloat16* Bwo = (const __hip_bfloat16*)d_in[8];
    char* ws = (char*)d_ws;
    (void)hipFuncSetAttribute((const void*)k1_proj, hipFuncAttributeMaxDynamicSharedMemorySize, G1_LDS);
    (void)hipFuncSetAttribute((const void*)k2_attn, hipFuncAttributeMaxDynamicSharedMemorySize, K2_LDS);
    (void)hipFuncSetAttribute((const void*)k3_oproj, hipFuncAttributeMaxDynamicSharedMemorySize, G3_LDS);
    k1_proj<<<256, 512, G1_LDS, stream>>>(Ax, Awqkv, Bx, Bckv, Bwq, Bwuk, Bwuv, Awo, Bwo, ws);
    k2_attn<<<256, 512, K2_LDS, stream>>>(ws);
    k3_oproj<<<240, 512, G3_LDS, stream>>>(Awo, Bwo, ws, (__hip_bfloat16*)d_out);
}
```

```cpp
#include <hip/hip_runtime.h>
#include <hip/hip_bf16.h>
#include <stdint.h>
#include <type_traits>

typedef short s16x4 __attribute__((ext_vector_type(4)));
typedef short bf16x8 __attribute__((ext_vector_type(8)));
typedef float f32x16 __attribute__((ext_vector_type(16)));
typedef float f32x4 __attribute__((ext_vector_type(4)));
typedef float f32x2 __attribute__((ext_vector_type(2)));
typedef unsigned u32x4 __attribute__((ext_vector_type(4)));
typedef unsigned u32x2 __attribute__((ext_vector_type(2)));
typedef __bf16 bf16x2_t __attribute__((ext_vector_type(2)));
#define LAS __attribute__((address_space(3)))
#define DI __device__ __forceinline__
#define MFMA32(a, b, c) __builtin_amdgcn_mfma_f32_32x32x16_bf16((a), (b), (c), 0, 0, 0)
#define MFMA16(a, b, c) __builtin_amdgcn_mfma_f32_16x16x32_bf16((a), (b), (c), 0, 0, 0)
#define CFENCE() asm volatile("" ::: "memory")
#define BARRIER() do { CFENCE(); __builtin_amdgcn_s_barrier(); CFENCE(); } while (0)
#define WAIT_VM(n) asm volatile("s_waitcnt vmcnt(" #n ") lgkmcnt(0)" ::: "memory")
#define SBAR() __builtin_amdgcn_sched_barrier(0)

DI unsigned pack2(float a, float b) {
    f32x2 v = {a, b};
    bf16x2_t r = __builtin_convertvector(v, bf16x2_t);
    return __builtin_bit_cast(unsigned, r);
}
DI u32x4 pack8(const f32x16& x, int s) {
    u32x4 w;
    w[0] = pack2(x[8 * s + 0], x[8 * s + 1]);
    w[1] = pack2(x[8 * s + 2], x[8 * s + 3]);
    w[2] = pack2(x[8 * s + 4], x[8 * s + 5]);
    w[3] = pack2(x[8 * s + 6], x[8 * s + 7]);
    return w;
}
DI u32x4 pack8s(const f32x16& x, int s, float c) {
    u32x4 w;
    w[0] = pack2(x[8 * s + 0] * c, x[8 * s + 1] * c);
    w[1] = pack2(x[8 * s + 2] * c, x[8 * s + 3] * c);
    w[2] = pack2(x[8 * s + 4] * c, x[8 * s + 5] * c);
    w[3] = pack2(x[8 * s + 6] * c, x[8 * s + 7] * c);
    return w;
}
DI bf16x8 tr_frag(LAS char* p0, LAS char* p1) {
    s16x4 lo = __builtin_amdgcn_ds_read_tr16_b64_v4i16((LAS s16x4*)p0);
    s16x4 hi = __builtin_amdgcn_ds_read_tr16_b64_v4i16((LAS s16x4*)p1);
    return __builtin_shufflevector(lo, hi, 0, 1, 2, 3, 4, 5, 6, 7);
}

DI void st16(void* p, u32x4 v) {
    asm volatile("global_store_dwordx4 %0, %1, off sc1\n\ts_nop 1" :: "v"(p), "v"(v) : "memory");
}
template <int I, int N, class F> DI void sfor(F&& f) {
    if constexpr (I < N) { f(std::integral_constant<int, I>{}); sfor<I + 1, N>(f); }
}
template <int OFF> DI void lds_rd(bf16x8& d, unsigned addr) {
    asm volatile("ds_read_b128 %0, %1 offset:%2" : "=v"(d) : "v"(addr), "i"(OFF));
}
template <int N> DI void lds_wait(bf16x8& d) { asm volatile("s_waitcnt lgkmcnt(%1)" : "+v"(d) : "i"(N)); }
template <int N> DI void lds_wait2(bf16x8& d, bf16x8& e) { asm volatile("s_waitcnt lgkmcnt(%2)" : "+v"(d), "+v"(e) : "i"(N)); }
#define LGKM0() __builtin_amdgcn_s_waitcnt(0xC07F)
DI void set_m0(unsigned v) { asm volatile("s_mov_b32 m0, %0\n\ts_nop 0" :: "s"(v) : "memory"); }
template <int OFF> DI void dma_s(unsigned voff, const char* sbase) {
    asm volatile("global_load_lds_dwordx4 %0, %1 offset:%2" :: "v"(voff), "s"(sbase), "i"(OFF) : "memory");
}

constexpr size_t WS_KVB = 0;
constexpr size_t WS_QB = WS_KVB + (size_t)32 * 1048576;
constexpr size_t WS_KVA = WS_QB + (size_t)16 * 1048576;
constexpr size_t WS_QA = WS_KVA + (size_t)8 * 1048576;
constexpr size_t WS_CTXB = WS_QA + (size_t)4 * 1048576;
constexpr size_t WS_CTXA = WS_CTXB + (size_t)16 * 1048576;
constexpr size_t WS_PART = WS_CTXA + (size_t)4 * 1048576;
constexpr size_t WS_CNT = WS_PART + (size_t)32 * 1048576;
constexpr int NSPLIT = 5;
constexpr size_t SLAB = (size_t)64 * 20 * 4096;

constexpr float CB = 0.08838834764831845f * 1.4426950408889634f;
constexpr float CA = 0.22360679774997896f * 1.4426950408889634f;

constexpr bool PREFETCH = true;
constexpr int G1_LDS = 2 * 65536 + 2048;
enum { EP_BK = 0, EP_BV = 1, EP_BQ = 2, EP_AQK = 3, EP_AV = 4 };

struct G1Src { unsigned xoff[4], woff[4]; const char* Xb; const char* Wb; unsigned wstep; int nkt; bool half; };
DI void g1_src(G1Src& s, const __hip_bfloat16* X, const __hip_bfloat16* W, int K, int ldw, int m0, int n0, int nlim, int wid, int lane, bool half) {
    s.half = half;
#pragma unroll
    for (int i = 0; i < 4; ++i) {
        const int row = (wid * 4 + i) * 8 + (lane >> 3);
        const int c = (lane & 7) ^ ((row >> 1) & 7);
        s.xoff[i] = (unsigned)(((m0 + row) * K + c * 8) * 2) + 4096 - i * 1024;
        const int k = (wid * 4 + i) * 2 + (lane >> 5);
        const int p = lane & 31, b = (p >> 2) ^ (k & 3);
        int col = n0 + (b * 4 + ((p & 3) ^ ((wid & 1) << 1))) * 8;
        col = col < nlim ? col : nlim - 8;
        s.woff[i] = (unsigned)((k * ldw + col) * 2) - i * 1024;
    }
    s.Xb = (const char*)X; s.Wb = (const char*)W; s.wstep = (unsigned)(64 * ldw * 2); s.nkt = K >> 6;
}
template <int I> DI void g1_piece(const G1Src& s, int kt) {
    if constexpr (I < 4) dma_s<I * 1024 - 4096>(s.xoff[I], s.Xb + (size_t)kt * 128);
    else if (!s.half || (threadIdx.x & 16) == 0) dma_s<(I - 4) * 1024>(s.woff[I - 4], s.Wb + (size_t)kt * s.wstep);
}

template <bool TR, int NT>
DI void g1_loop(f32x4 (&acc)[8][4], bf16x8 (&xs)[2][4], bf16x8 (&wsb)[2][4], const G1Src& cur, const G1Src& nxt, bool has_next, bool first,
                LAS char* lds, int wid, int lane) {
    const int wm = wid >> 2, wn = wid & 3, r16 = lane & 15, g = lane >> 4;
    const unsigned lds0 = (unsigned)(uintptr_t)lds;
    const int nkt = cur.nkt;
    int xb[2];
#pragma unroll
    for (int st = 0; st < 2; ++st) xb[st] = wm * 4 * 8192 + r16 * 128 + (((4 * st + g) ^ (r16 >> 1)) * 16);
    const int q = (lane >> 2) & 3, p4 = lane & 3;
    int wb[4];
#pragma unroll
    for (int nt = 0; nt < 4; ++nt) {
        const int b = NT == 4 ? wn * 2 + (nt >> 1) : wn;
        wb[nt] = g * 8192 + 4096 + q * 512 + ((b ^ q) * 64) + (((nt & 1) ^ (g & 1)) * 32) + p4 * 8;
    }
#pragma unroll
    for (int mt = 0; mt < 8; ++mt)
#pragma unroll
        for (int nt = 0; nt < 4; ++nt) acc[mt][nt] = f32x4{};
#define G1_RX(set, bb, st, mh)                                                                                   \
    _Pragma("unroll") for (int m4 = 0; m4 < 4; ++m4)                                                             \
        xs[set][m4] = *(LAS bf16x8*)((bb) + xb[st] + ((mh) * 2 + (m4 >> 1)) * 8192 + (m4 & 1) * 2048)
#define G1_RW(bb, st)                                                                                            \
    _Pragma("unroll") for (int nt = 0; nt < NTR; ++nt)                                                           \
        wsb[st][nt] = tr_frag((bb) + wb[nt] + (st) * 32768, (bb) + wb[nt] + (st) * 32768 + 2048)
#define G1_MM(set, st, mh, on, kn, p0, pe)                                                                       \
    sfor<0, 4>([&](auto mc) {                                                                                    \
        constexpr int m4 = decltype(mc)::value;                                                                  \
        _Pragma("unroll") for (int nt = 0; nt < NT; ++nt)                                                        \
            acc[(mh) * 4 + m4][nt] = TR ? MFMA16(wsb[st][nt], xs[set][m4], acc[(mh) * 4 + m4][nt])               \
                                        : MFMA16(xs[set][m4], wsb[st][nt], acc[(mh) * 4 + m4][nt]);              \
        if constexpr ((pe) > (p0)) {                                                                             \
            SBAR();                                                                                              \
            if (on) {                                                                                            \
                if ((kn) < nkt) { g1_piece<2 * m4>(cur, (kn)); g1_piece<2 * m4 + 1>(cur, (kn)); }                \
                else { g1_piece<2 * m4>(nxt, (kn) - nkt); g1_piece<2 * m4 + 1>(nxt, (kn) - nkt); }               \
            }                                                                                                    \
            SBAR();                                                                                              \
        }                                                                                                        \
    })
    constexpr int NTR = NT;
    G1_RW(lds, 0);
    if (first) { G1_RX(0, lds, 0, 0); }
#pragma unroll 1
    for (int kt = 0; kt < nkt; ++kt) {
        LAS char* bb = lds + (kt & 1) * 65536;
        LAS char* bn = lds + ((kt + 1) & 1) * 65536;
        const bool on2 = kt + 2 < nkt || has_next;
        LGKM0(); G1_RX(1, bb, 0, 1); SBAR(); G1_MM(0, 0, 0, false, 0, 0, 0); SBAR();
        LGKM0(); G1_RW(bb, 1); G1_RX(0, bb, 1, 0); SBAR(); G1_MM(1, 0, 1, false, 0, 0, 0); SBAR();
        LGKM0(); G1_RX(1, bb, 1, 1); SBAR(); G1_MM(0, 1, 0, false, 0, 0, 0); SBAR();
        LGKM0();
        if (PREFETCH && first && kt == 0) { WAIT_VM(5); } else { WAIT_VM(0); }
        BARRIER();
        set_m0(lds0 + (kt & 1) * 65536 + wid * 8192 + 4096);
        if (kt + 1 < nkt) G1_RW(bn, 0);
        if (kt + 1 < nkt || has_next) G1_RX(0, bn, 0, 0);
        SBAR(); G1_MM(1, 1, 1, on2, kt + 2, 0, 8); SBAR();
    }
#undef G1_RX
#undef G1_RW
#undef G1_MM
}

DI void st8(void* p, u32x2 v) {
    asm volatile("global_store_dwordx2 %0, %1, off sc1\n\ts_nop 1" :: "v"(p), "v"(v) : "memory");
}

template <int EP>
DI void g1_epi(const f32x4 (&acc)[8][4], int m0, int n0, int nlim, int nsec, char* __restrict__ ws, int wid, int lane) {
    const int wm = wid >> 2, wn = wid & 3, c = lane & 15, g = lane >> 4;
    if constexpr (EP == EP_BK || EP == EP_BQ || EP == EP_BV) {
#pragma unroll
        for (int mt = 0; mt < 8; ++mt) {
            const int tb = (m0 + wm * 128 + mt * 16) >> 5;
            const int tile = tb >> 1, kb = tb & 1, th = mt & 1;
#pragma unroll
            for (int np = 0; np < 2; ++np) {
                const int nb = n0 + wn * 64 + np * 32;
                const int head = nb >> 7;
                const float sc = EP == EP_BQ ? CB : 1.0f;
                const f32x4 a = acc[mt][2 * np], b = acc[mt][2 * np + 1];
                unsigned a0 = pack2(a[0] * sc, a[1] * sc), a1 = pack2(a[2] * sc, a[3] * sc);
                unsigned b0 = pack2(b[0] * sc, b[1] * sc), b1 = pack2(b[2] * sc, b[3] * sc);
                if constexpr (EP == EP_BV) {
                    auto r0 = __builtin_amdgcn_permlane32_swap(a0, b0, false, false);
                    auto r1 = __builtin_amdgcn_permlane32_swap(a1, b1, false, false);
                    const u32x4 w = {r0[0], r1[0], r0[1], r1[1]};
                    const int db = (nb >> 5) & 3;
                    char* base = ws + WS_KVB + (size_t)head * 1048576 + tile * 32768 + 16384 + ((kb * 2 + th) * 4 + db) * 1024;
                    st16(base + ((g >> 1) * 16 + c + 32 * (g & 1)) * 16, w);
                } else {
                    auto r0 = __builtin_amdgcn_permlane16_swap(a0, b0, false, false);
                    auto r1 = __builtin_amdgcn_permlane16_swap(a1, b1, false, false);
                    const u32x4 w = {r0[0], r1[0], r0[1], r1[1]};
                    const int ks = ((nb >> 4) & 7) + (g & 1);
                    char* base = EP == EP_BK ? ws + WS_KVB + (size_t)head * 1048576 + tile * 32768 + (kb * 8 + ks) * 1024
                                             : ws + WS_QB + (size_t)((head * 64 + tb) * 8 + ks) * 1024;
                    st16(base + (th * 16 + c + 32 * (g >> 1)) * 16, w);
                }
            }
        }
        return;
    }
#pragma unroll
    for (int mt = 0; mt < 8; ++mt) {
        const int tb = (m0 + wm * 128 + mt * 16) >> 5;
        const int tile = tb >> 1, kb = tb & 1, th = mt & 1;
#pragma unroll
        for (int nt = 0; nt < 4; ++nt) {
            const int nb = n0 + wn * 64 + nt * 16;
            if (nb >= nlim) continue;
            const f32x4 a = acc[mt][nt];
            if constexpr (EP == EP_BK || EP == EP_BQ || EP == EP_BV) {
            } else if constexpr (EP == EP_AQK) {
                const bool isq = nb < 640;
                const float sc = isq ? CA : 1.0f;
                const int r = th * 16 + c;
                const int n = nb - (isq ? 0 : 640) + 4 * g;
                const int head = n / 20, d0 = n - head * 20;
                char* base = isq ? ws + WS_QA + (size_t)((head * 64 + tb) * 2) * 1024
                                 : ws + WS_KVA + (size_t)head * 262144 + tile * 8192 + kb * 2048;
                char* dst = base + (d0 >> 4) * 1024 + (r + 32 * ((d0 >> 3) & 1)) * 16 + (d0 & 4) * 2;
                u32x2 w;
                w[0] = pack2(a[0] * sc, a[1] * sc);
                w[1] = pack2(a[2] * sc, a[3] * sc);
                *(u32x2*)dst = w;
                if (d0 == 16) {
                    *(u32x2*)(dst + 8) = (u32x2){isq ? 0u : 0x3F803F80u, 0u};
                    *(u32x4*)(base + 1024 + (r + 32) * 16) = (u32x4){0u, 0u, 0u, 0u};
                }
            } else {
                const int n = nb - nsec + c;
                const int head = n / 20, d = n - head * 20;
                char* dst = ws + WS_KVA + (size_t)head * 262144 + tile * 8192 + 4096 + (kb * 2 + th) * 1024 + (d + 32 * (g & 1)) * 16 + (g >> 1) * 8;
                u32x2 w;
                w[0] = pack2(a[0], a[1]);
                w[1] = pack2(a[2], a[3]);
                *(u32x2*)dst = w;
                if (d == 19) *(u32x2*)(dst + 16) = (u32x2){0x3F803F80u, 0x3F803F80u};
            }
        }
    }
}

__global__ __launch_bounds__(512, 2) void k1_proj(const __hip_bfloat16* __restrict__ Ax, const __hip_bfloat16* __restrict__ Awqkv,
                                                  const __hip_bfloat16* __restrict__ Bx, const __hip_bfloat16* __restrict__ Bckv,
                                                  const __hip_bfloat16* __restrict__ Bwq, const __hip_bfloat16* __restrict__ Bwuk,
                                                  const __hip_bfloat16* __restrict__ Bwuv, const __hip_bfloat16* __restrict__ Awo,
                                                  const __hip_bfloat16* __restrict__ Bwo, char* __restrict__ ws) {
    extern __shared__ __attribute__((aligned(16))) char smem[];
    LAS char* lds = (LAS char*)smem;
    const int tid = threadIdx.x, lane = tid & 63, wid = __builtin_amdgcn_readfirstlane(tid >> 6);
    const int bid = blockIdx.x;
    const int m0 = ((bid >> 3) & 7) * 256;
    const int grp = bid >> 6, c8 = bid & 7;
    const int cc = c8 + 8 * (grp & 1);
    const bool has2 = grp < 3;
    G1Src s1, s2;
    if (grp < 2) {
        g1_src(s1, Bckv, Bwuk, 512, 4096, m0, cc * 256, 4096, wid, lane, false);
        g1_src(s2, Bx, Bwq, 640, 4096, m0, cc * 256, 4096, wid, lane, false);
    } else if (grp == 2) {
        g1_src(s1, Bckv, Bwuv, 512, 4096, m0, c8 * 256, 4096, wid, lane, false);
        g1_src(s2, Bckv, Bwuv, 512, 4096, m0, (c8 + 8) * 256, 4096, wid, lane, false);
    } else {
        g1_src(s1, Ax, Awqkv, 640, 1920, m0, c8 * 256, c8 < 5 ? 1280 : 1920, wid, lane, false);
        s2 = s1;
    }

    f32x4 acc[8][4];
    bf16x8 xf[2][4], wf[2][4];
    const unsigned lds0 = (unsigned)(uintptr_t)lds;
    if (bid == 255 && tid < 40) ((unsigned*)(ws + WS_CNT))[tid * 64] = 0u;
    set_m0(lds0 + wid * 8192 + 4096);
    sfor<0, 8>([&](auto ic) { g1_piece<decltype(ic)::value>(s1, 0); });
    set_m0(lds0 + 65536 + wid * 8192 + 4096);
    sfor<0, 8>([&](auto ic) { g1_piece<decltype(ic)::value>(s1, 1); });
    WAIT_VM(8);
    BARRIER();
    {
        set_m0(lds0 + 131072 + wid * 256);
        const unsigned gw = (unsigned)(bid * 8 + wid) * 64 + lane;
#define PF(ptr, nbytes) do { constexpr unsigned R_ = 131072u / ((nbytes) / 128u);                                     \
            const unsigned ln_ = gw / R_;                                                                                \
            const unsigned off_ = (gw % R_ == 0u && ln_ * 128u < (nbytes)) ? ln_ * 128u : 0u;                             \
            asm volatile("global_load_lds_dword %0, %1" :: "v"(off_), "s"((const char*)(ptr)) : "memory"); } while (0)
        PF(Bx, 2048u * 640u * 2u); PF(Bwq, 640u * 4096u * 2u); PF(Bwuv, 512u * 4096u * 2u);
        PF(Bwo, 4096u * 640u * 2u); PF(Awo, 640u * 640u * 2u);
#undef PF
    }
    if (grp < 2 || (grp == 3 && c8 < 5)) {
        g1_loop<true, 4>(acc, xf, wf, s1, s2, has2, true, lds, wid, lane);
        if (grp < 2) g1_epi<EP_BK>(acc, m0, cc * 256, 4096, 0, ws, wid, lane);
        else g1_epi<EP_AQK>(acc, m0, c8 * 256, 1280, 0, ws, wid, lane);
    } else {
        g1_loop<false, 4>(acc, xf, wf, s1, s2, has2, true, lds, wid, lane);
        if (grp == 2) g1_epi<EP_BV>(acc, m0, c8 * 256, 4096, 0, ws, wid, lane);
        else g1_epi<EP_AV>(acc, m0, c8 * 256, 1920, 1280, ws, wid, lane);
    }
    if (grp < 2) {
        g1_loop<true, 4>(acc, xf, wf, s2, s2, false, false, lds, wid, lane);
        g1_epi<EP_BQ>(acc, m0, cc * 256, 4096, 0, ws, wid, lane);
    } else if (grp == 2) {
        g1_loop<false, 4>(acc, xf, wf, s2, s2, false, false, lds, wid, lane);
        g1_epi<EP_BV>(acc, m0, (c8 + 8) * 256, 4096, 0, ws, wid, lane);
    }
}

constexpr int SLOT = 40960;
constexpr int NSLOT = 4;
constexpr int K2_LDS = NSLOT * SLOT;
constexpr int NIT = 34;

__global__ __launch_bounds__(512, 2) void k2_attn(char* __restrict__ ws) {
    extern __shared__ __attribute__((aligned(16))) char smem[];
    LAS char* lds = (LAS char*)smem;
    const int tid = threadIdx.x, lane = tid & 63, wid = __builtin_amdgcn_readfirstlane(tid >> 6);
    const int r = lane & 31, h = lane >> 5;
    const int g = blockIdx.x, xcd = g & 7, idx = g >> 3;
    const int head = xcd * 4 + (idx >> 3), x = idx & 7;
    const char* kvb = ws + WS_KVB + (size_t)head * 1048576;
    const char* kva = ws + WS_KVA + (size_t)head * 262144;
    const int n1 = 32 - 2 * x;
    const unsigned lbase = (unsigned)(uintptr_t)lds + lane * 16;

    const unsigned lds0 = (unsigned)(uintptr_t)lds;
    const unsigned voff = lane * 16;
    const int wa = wid & 3;
    const char* sbB = kvb;
    const char* sbA = kva;
#define K2_M0(tt)                                                                           \
    do {                                                                                    \
        const int t3_ = (tt) < NIT ? (tt) : NIT - 1;                                        \
        const int tb3_ = t3_ < n1 ? t3_ : t3_ - n1;                                         \
        const int ta3_ = t3_ < 32 ? t3_ : 31;                                               \
        sbB = kvb + (size_t)tb3_ * 32768 + wid * 4096 + 2048;                               \
        sbA = kva + (size_t)ta3_ * 8192 + wid * 1024 - 2048;                                \
        set_m0(lds0 + ((tt) < NIT ? ((tt) & 3) : 2 + ((tt) & 1)) * SLOT + wid * 5120 + 2048); \
    } while (0)
#define K2_DMA(i) do { SBAR(); if constexpr ((i) < 4) dma_s<(i) * 1024 - 2048>(voff, sbB); else dma_s<2048>(voff, sbA); SBAR(); } while (0)
#define K2_STAGE_ALL(tt) do { K2_M0(tt); K2_DMA(0); K2_DMA(1); K2_DMA(2); K2_DMA(3); K2_DMA(4); } while (0)
#define BOFF(f) (((f) >> 2) * 5120 + ((f) & 3) * 1024)
#define AOFF(a) ((a) * 5120 + 4096)
    K2_STAGE_ALL(0);
    K2_STAGE_ALL(1);

#define K2_SYNC(it) do { WAIT_VM(5); BARRIER(); K2_M0((it) + 3); } while (0)

    if (wid < 4) {
        bf16x8 fr[8];
        K2_SYNC(-1);
        sfor<0, 8>([&](auto ic) { constexpr int j = decltype(ic)::value; lds_rd<BOFF((j & 1) * 8 + (j >> 1))>(fr[j], lbase); });
        K2_DMA(0); K2_DMA(1);
        int it = 0;
        for (int blk = 0; blk < 2; ++blk) {
            const int qb = blk == 0 ? 15 - x : x;
            const int nt = 2 * qb + 2;
            const int q0 = qb * 128 + wid * 32;
            const int tb32 = q0 >> 5;
            bf16x8 qf[8];
            const char* qp = ws + WS_QB + (size_t)((head * 64 + tb32) * 8) * 1024 + lane * 16;
#pragma unroll
            for (int ks = 0; ks < 8; ++ks) qf[ks] = *(const bf16x8*)(qp + ks * 1024);
            f32x16 o[4];
#pragma unroll
            for (int db = 0; db < 4; ++db) o[db] = f32x16{};
            f32x16 negm = f32x16{};
            float l = 0.f;
            for (int t = 0; t < nt; ++t, ++it) {
                const int rel = (q0 - 64 * t) >> 5;
                const unsigned ac = lbase + (it & 3) * SLOT;
                const unsigned an = lbase + ((it + 1) & 3) * SLOT;
                f32x16 s0, s1;
                sfor<0, 16>([&](auto ic) {
                    constexpr int j = decltype(ic)::value, ks = j >> 1;
                    lds_wait<7>(fr[j & 7]);
                    if constexpr (j == 0) s0 = MFMA32(fr[0], qf[0], negm);
                    else if constexpr (j == 1) s1 = MFMA32(fr[1], qf[0], negm);
                    else if constexpr ((j & 1) == 0) s0 = MFMA32(fr[j & 7], qf[ks], s0);
                    else s1 = MFMA32(fr[j & 7], qf[ks], s1);
                    constexpr int jn = j + 8;
                    SBAR();
                    if constexpr (jn < 16) lds_rd<BOFF((jn & 1) * 8 + (jn >> 1))>(fr[j & 7], ac);
                    else lds_rd<BOFF(jn)>(fr[j & 7], ac);
                    if constexpr (j == 2) K2_DMA(2);
                    if constexpr (j == 8) K2_DMA(3);
                    if constexpr (j == 14) K2_DMA(4);
                    SBAR();
                });
#define B_MASK()                                                                                        \
    do {                                                                                                \
        if (rel <= 1) {                                                                                 \
            const int lim = 32 * rel + r - 4 * h;                                                       \
            _Pragma("unroll") for (int i = 0; i < 16; ++i) {                                            \
                const int kr = (i & 3) + 8 * (i >> 2);                                                  \
                if (kr > lim) s0[i] = -__builtin_inff();                                                \
                if (kr + 32 > lim) s1[i] = -__builtin_inff();                                           \
            }                                                                                           \
        }                                                                                               \
    } while (0)
                B_MASK();
                float ps = 0.f;
                if (t > 0) {
#pragma unroll
                    for (int i = 0; i < 16; ++i) { s0[i] = __builtin_amdgcn_exp2f(s0[i]); ps += s0[i]; }
#pragma unroll
                    for (int i = 0; i < 16; ++i) { s1[i] = __builtin_amdgcn_exp2f(s1[i]); ps += s1[i]; }
                }
                if (t == 0 || __any(!(ps <= 65536.f))) {
                    if (t > 0) {
                        LAS char* sl = lds + (it & 3) * SLOT + lane * 16;
                        s0 = negm; s1 = negm;
#pragma unroll 1
                        for (int ks = 0; ks < 8; ++ks) {
                            bf16x8 qq = qf[0];
#pragma unroll
                            for (int u = 1; u < 8; ++u) qq = (ks == u) ? qf[u] : qq;
                            s0 = MFMA32(*(LAS bf16x8*)(sl + (ks >> 2) * 5120 + (ks & 3) * 1024), qq, s0);
                            s1 = MFMA32(*(LAS bf16x8*)(sl + 10240 + (ks >> 2) * 5120 + (ks & 3) * 1024), qq, s1);
                        }
                        B_MASK();
                    }
                    float mx = s0[0];
#pragma unroll
                    for (int i = 1; i < 16; ++i) mx = fmaxf(mx, s0[i]);
#pragma unroll
                    for (int i = 0; i < 16; ++i) mx = fmaxf(mx, s1[i]);
                    auto rr = __builtin_amdgcn_permlane32_swap(__float_as_uint(mx), __float_as_uint(mx), false, false);
                    mx = fmaxf(__uint_as_float(rr[0]), __uint_as_float(rr[1]));
                    float delta = mx;
                    if (t > 0) {
                        delta = fmaxf(mx, 0.f);
                        const float alpha = __builtin_amdgcn_exp2f(-delta);
                        l *= alpha;
#pragma unroll
                        for (int db = 0; db < 4; ++db)
#pragma unroll
                            for (int i = 0; i < 16; ++i) o[db][i] *= alpha;
                    }
                    const float nm = negm[0] - delta;
#pragma unroll
                    for (int i = 0; i < 16; ++i) negm[i] = nm;
                    ps = 0.f;
#pragma unroll
                    for (int i = 0; i < 16; ++i) { s0[i] = __builtin_amdgcn_exp2f(s0[i] - delta); ps += s0[i]; }
#pragma unroll
                    for (int i = 0; i < 16; ++i) { s1[i] = __builtin_amdgcn_exp2f(s1[i] - delta); ps += s1[i]; }
                }
                l += ps;
                bf16x8 pf[4];
                pf[0] = __builtin_bit_cast(bf16x8, pack8(s0, 0));
                pf[1] = __builtin_bit_cast(bf16x8, pack8(s0, 1));
                pf[2] = __builtin_bit_cast(bf16x8, pack8(s1, 0));
                pf[3] = __builtin_bit_cast(bf16x8, pack8(s1, 1));
                if (it + 1 < NIT) { K2_SYNC(it); } else { K2_M0(it + 3); }
                sfor<0, 16>([&](auto ic) {
                    constexpr int j = decltype(ic)::value, s4 = j >> 2, db = j & 3;
                    lds_wait<7>(fr[j & 7]);
                    o[db] = MFMA32(fr[j & 7], pf[s4], o[db]);
                    SBAR();
                    if constexpr (j < 8) lds_rd<BOFF(24 + j)>(fr[j & 7], ac);
                    else { constexpr int jn = j - 8; lds_rd<BOFF((jn & 1) * 8 + (jn >> 1))>(fr[j & 7], an); }
                    if constexpr (j == 4) K2_DMA(0);
                    if constexpr (j == 10) K2_DMA(1);
                    SBAR();
                });
            }
            auto rr = __builtin_amdgcn_permlane32_swap(__float_as_uint(l), __float_as_uint(l), false, false);
            const float inv = 1.0f / (__uint_as_float(rr[0]) + __uint_as_float(rr[1]));
            char* cp = ws + WS_CTXB + (size_t)(tb32 * 256 + head * 8) * 1024 + lane * 16;
#pragma unroll
            for (int db = 0; db < 4; ++db) {
                st16(cp + (2 * db) * 1024, pack8s(o[db], 0, inv));
                st16(cp + (2 * db + 1) * 1024, pack8s(o[db], 1, inv));
            }
        }
        WAIT_VM(0);
#undef B_MASK
    } else {
        const int tb32 = x * 8 + wa * 2;
        bf16x8 qf[2][2];
        const char* qp = ws + WS_QA + (size_t)((head * 64 + tb32) * 2) * 1024 + lane * 16;
#pragma unroll
        for (int qt = 0; qt < 2; ++qt)
#pragma unroll
            for (int ks = 0; ks < 2; ++ks) qf[qt][ks] = *(const bf16x8*)(qp + (qt * 2 + ks) * 1024);
        f32x16 o[2];
        o[0] = f32x16{}; o[1] = f32x16{};
        float negm[2] = {0.f, 0.f};
        bf16x8 kf[4], vf[4];
        f32x16 s[2][2];
        K2_SYNC(-1);
        lds_rd<AOFF(0)>(kf[0], lbase); lds_rd<AOFF(1)>(kf[1], lbase); lds_rd<AOFF(2)>(kf[2], lbase); lds_rd<AOFF(3)>(kf[3], lbase);
        for (int it = 0; it <= 32; ++it) {
            if (it > 0) {
                u32x4 pf[2][2][2];
                unsigned chk = 0;
                sfor<0, 4>([&](auto bc) {
                    constexpr int blk = decltype(bc)::value, kb = blk >> 1, qt = blk & 1;
                    f32x16 e;
#pragma unroll
                    for (int i = 0; i < 8; ++i) e[i] = __builtin_amdgcn_exp2f(s[kb][qt][i]);
                    K2_DMA(blk);
#pragma unroll
                    for (int i = 8; i < 16; ++i) e[i] = __builtin_amdgcn_exp2f(s[kb][qt][i]);
                    if constexpr (blk == 3) K2_DMA(4);
                    pf[kb][qt][0] = pack8(e, 0);
                    pf[kb][qt][1] = pack8(e, 1);
                    chk |= pf[kb][qt][0][0] | pf[kb][qt][0][1] | pf[kb][qt][0][2] | pf[kb][qt][0][3];
                    chk |= pf[kb][qt][1][0] | pf[kb][qt][1][1] | pf[kb][qt][1][2] | pf[kb][qt][1][3];
                });
                const bool first = (it == 1);
                if (first || __any((chk & 0x40004000u) != 0u)) {
#pragma unroll
                    for (int qt = 0; qt < 2; ++qt) {
                        float mx = s[0][qt][0];
#pragma unroll
                        for (int i = 1; i < 16; ++i) mx = fmaxf(mx, s[0][qt][i]);
#pragma unroll
                        for (int i = 0; i < 16; ++i) mx = fmaxf(mx, s[1][qt][i]);
                        auto rr = __builtin_amdgcn_permlane32_swap(__float_as_uint(mx), __float_as_uint(mx), false, false);
                        mx = fmaxf(__uint_as_float(rr[0]), __uint_as_float(rr[1]));
                        float delta = mx + 10.f;
                        if (!first) delta = fmaxf(delta, 0.f);
                        const float tgt = negm[qt] - delta;
                        const unsigned hi = pack2(tgt, 0.f) & 0xFFFFu;
                        const float hif = __uint_as_float(hi << 16);
                        const unsigned lo = pack2(tgt - hif, 0.f) & 0xFFFFu;
                        const float neff = hif + __uint_as_float(lo << 16);
                        delta = negm[qt] - neff;
                        negm[qt] = neff;
                        {
                            u32x4 qq = __builtin_bit_cast(u32x4, qf[qt][1]);
                            qq[2] = h ? 0u : (hi | (lo << 16));
                            qf[qt][1] = __builtin_bit_cast(bf16x8, qq);
                        }
                        if (!first) {
                            const float alpha = __builtin_amdgcn_exp2f(-delta);
#pragma unroll
                            for (int i = 0; i < 16; ++i) o[qt][i] *= alpha;
                        }
#pragma unroll
                        for (int kb = 0; kb < 2; ++kb) {
                            f32x16 e;
#pragma unroll
                            for (int i = 0; i < 16; ++i) e[i] = __builtin_amdgcn_exp2f(s[kb][qt][i] - delta);
                            pf[kb][qt][0] = pack8(e, 0);
                            pf[kb][qt][1] = pack8(e, 1);
                        }
                    }
                }
                lds_wait2<4>(vf[0], vf[1]);
                lds_wait2<4>(vf[2], vf[3]);
#pragma unroll
                for (int kb = 0; kb < 2; ++kb)
#pragma unroll
                    for (int s2 = 0; s2 < 2; ++s2)
#pragma unroll
                        for (int qt = 0; qt < 2; ++qt)
                            o[qt] = MFMA32(vf[kb * 2 + s2], __builtin_bit_cast(bf16x8, pf[kb][qt][s2]), o[qt]);
            } else {
                K2_DMA(0); K2_DMA(1); K2_DMA(2); K2_DMA(3); K2_DMA(4);
            }
            lds_wait2<0>(kf[0], kf[1]);
            lds_wait2<0>(kf[2], kf[3]);
#pragma unroll
            for (int kb = 0; kb < 2; ++kb)
#pragma unroll
                for (int qt = 0; qt < 2; ++qt) {
                    s[kb][qt] = MFMA32(kf[kb * 2], qf[qt][0], f32x16{});
                    s[kb][qt] = MFMA32(kf[kb * 2 + 1], qf[qt][1], s[kb][qt]);
                }
            SBAR();
            K2_SYNC(it);
            const unsigned ac = lbase + (it & 3) * SLOT;
            const unsigned an = lbase + ((it + 1) & 3) * SLOT;
            lds_rd<AOFF(4)>(vf[0], ac); lds_rd<AOFF(5)>(vf[1], ac); lds_rd<AOFF(6)>(vf[2], ac); lds_rd<AOFF(7)>(vf[3], ac);
            lds_rd<AOFF(0)>(kf[0], an); lds_rd<AOFF(1)>(kf[1], an); lds_rd<AOFF(2)>(kf[2], an); lds_rd<AOFF(3)>(kf[3], an);
        }
        WAIT_VM(0);
#pragma unroll
        for (int qt = 0; qt < 2; ++qt) {
            const unsigned lv = __float_as_uint(o[qt][8]);
            auto rr = __builtin_amdgcn_permlane32_swap(lv, lv, false, false);
            const float lsum = __uint_as_float(rr[1]);
            const float inv = 1.0f / lsum;
            char* cp = ws + WS_CTXA + (size_t)((tb32 + qt) * 40) * 1024;
#pragma unroll
            for (int gq = 0; gq < 3; ++gq) {
                const int d0 = 8 * gq + 4 * h;
                if (d0 < 20) {
                    const int k = head * 20 + d0;
                    u32x2 w;
                    w[0] = pack2(o[qt][4 * gq + 0] * inv, o[qt][4 * gq + 1] * inv);
                    w[1] = pack2(o[qt][4 * gq + 2] * inv, o[qt][4 * gq + 3] * inv);
                    *(u32x2*)(cp + (k >> 4) * 1024 + (r + 32 * ((k >> 3) & 1)) * 16 + (k & 4) * 2) = w;
                }
            }
        }
    }
}

constexpr int G3_STAGE = 49152;
constexpr int G3_LDS = 3 * G3_STAGE;

__global__ __launch_bounds__(512, 2) void k3_oproj(const __hip_bfloat16* __restrict__ Awo, const __hip_bfloat16* __restrict__ Bwo,
                                                   char* __restrict__ ws, __hip_bfloat16* __restrict__ out) {
    extern __shared__ __attribute__((aligned(16))) char smem[];
    LAS char* lds = (LAS char*)smem;
    const int tid = threadIdx.x, lane = tid & 63, wid = __builtin_amdgcn_readfirstlane(tid >> 6);
    const int wm = wid >> 1, wn = wid & 1, h = lane >> 5;
    const int bid = blockIdx.x;
    const bool isB = bid < 200;
    int mt, nt, sp, kt0, nkt, kstot;
    const char* Xc; const char* Wb;
    if (isB) {
        mt = bid & 7; const int rest = bid >> 3; nt = rest % 5; sp = rest / 5;
        kt0 = sp * 13; nkt = sp == 4 ? 12 : 13; kstot = 256;
        Xc = ws + WS_CTXB; Wb = (const char*)Bwo;
    } else {
        const int b2 = bid - 200; mt = b2 & 7; nt = b2 >> 3; sp = 5;
        kt0 = 0; nkt = 10; kstot = 40;
        Xc = ws + WS_CTXA; Wb = (const char*)Awo;
    }
    unsigned woff[2];
#pragma unroll
    for (int i = 0; i < 2; ++i) {
        const int k = (wid * 2 + i) * 4 + (lane >> 4);
        const int p = lane & 15, b = (p >> 2) ^ (k & 3);
        woff[i] = (unsigned)(((kt0 * 64 + k) * 640 + nt * 128 + (b * 4 + (p & 3)) * 8) * 2);
    }
    const unsigned lds0 = (unsigned)(uintptr_t)lds;
    const char* xsb = Xc + (size_t)((mt * 8 + wid) * kstot + kt0 * 4) * 1024 + 4096;
    const unsigned voff = lane * 16;
    woff[1] -= 1024;
    auto piece = [&](int kt, auto ic) {
        constexpr int i = decltype(ic)::value;
        if constexpr (i < 4) dma_s<i * 1024 - 4096>(voff, xsb + (size_t)kt * 4096);
        else dma_s<(i - 4) * 1024>(woff[i - 4], Wb + (size_t)kt * (64u * 640u * 2u));
    };
    const int q = (lane >> 2) & 3, p4 = lane & 3, g16 = (lane >> 4) & 1;
    const int r0 = isB ? 4 * h : 8 * h, r1 = isB ? 8 + 4 * h : 8 * h + 4;
    int wb0[2], wb1[2];
#pragma unroll
    for (int ni = 0; ni < 2; ++ni) {
        const int b = wn * 2 + ni;
        const int c = ((b ^ q) * 64) + g16 * 32 + p4 * 8;
        wb0[ni] = (r0 >> 3) * 6144 + 4096 + ((r0 & 7) + q) * 256 + c;
        wb1[ni] = (r1 >> 3) * 6144 + 4096 + ((r1 & 7) + q) * 256 + c;
    }
    const int xb0 = wm * 2 * 6144 + lane * 16;
    f32x16 acc[2][2];
#pragma unroll
    for (int mi = 0; mi < 2; ++mi)
#pragma unroll
        for (int ni = 0; ni < 2; ++ni) acc[mi][ni] = f32x16{};

    bf16x8 xf[2][4][2], wf[2][4][2];
#define G3_RD(set, bb)                                                                                           \
    do {                                                                                                         \
        _Pragma("unroll") for (int ks = 0; ks < 4; ++ks) {                                                       \
            _Pragma("unroll") for (int ni = 0; ni < 2; ++ni)                                                     \
                wf[set][ks][ni] = tr_frag((bb) + wb0[ni] + ks * 12288, (bb) + wb1[ni] + ks * 12288);             \
            _Pragma("unroll") for (int mi = 0; mi < 2; ++mi)                                                     \
                xf[set][ks][mi] = *(LAS bf16x8*)((bb) + xb0 + mi * 6144 + ks * 1024);                            \
        }                                                                                                        \
    } while (0)
#define G3_STEP(set, kt)                                                                                         \
    do {                                                                                                         \
        if ((kt) + 2 < nkt) { WAIT_VM(6); } else { WAIT_VM(0); }                                                 \
        LGKM0();                                                                                                 \
        BARRIER();                                                                                               \
        const int b0_ = (kt) % 3, b1_ = ((kt) + 1) % 3;                                                          \
        set_m0(lds0 + b0_ * G3_STAGE + wid * 6144 + 4096);                                                       \
        if ((kt) + 1 < nkt) G3_RD((set) ^ 1, lds + b1_ * G3_STAGE);                                              \
        SBAR();                                                                                                  \
        const bool on_ = (kt) + 3 < nkt;                                                                         \
        sfor<0, 8>([&](auto jc) {                                                                                \
            constexpr int j = decltype(jc)::value, ks = j >> 1, mi = j & 1;                                      \
            _Pragma("unroll") for (int ni = 0; ni < 2; ++ni)                                                     \
                acc[mi][ni] = MFMA32(wf[set][ks][ni], xf[set][ks][mi], acc[mi][ni]);                             \
            if constexpr (j < 6) { SBAR(); if (on_) piece((kt) + 3, jc); SBAR(); }                               \
        });                                                                                                      \
        SBAR();                                                                                                  \
    } while (0)
#pragma unroll
    for (int t = 0; t < 3; ++t) {
        set_m0(lds0 + t * G3_STAGE + wid * 6144 + 4096);
        sfor<0, 6>([&](auto ic) { piece(t, ic); });
    }
    WAIT_VM(12);
    BARRIER();
    G3_RD(0, lds);
    int kt = 0;
#pragma unroll 1
    for (; kt + 1 < nkt; kt += 2) { G3_STEP(0, kt); G3_STEP(1, kt + 1); }
    if (kt < nkt) G3_STEP(0, kt);
#undef G3_RD
#undef G3_STEP
    const int r = lane & 31;
    if (!isB) {
#pragma unroll
        for (int mi = 0; mi < 2; ++mi)
#pragma unroll
            for (int ni = 0; ni < 2; ++ni) {
                char* op = (char*)out + (size_t)((mt * 8 + wm * 2 + mi) * 32 + r) * 2560 + ((nt * 4 + wn * 2 + ni) * 32 + 4 * h) * 2;
#pragma unroll
                for (int gq = 0; gq < 4; ++gq) {
                    u32x2 o;
                    o[0] = pack2(acc[mi][ni][4 * gq], acc[mi][ni][4 * gq + 1]);
                    o[1] = pack2(acc[mi][ni][4 * gq + 2], acc[mi][ni][4 * gq + 3]);
                    *(u32x2*)(op + gq * 16) = o;
                }
            }
        return;
    }
#pragma unroll
    for (int mi = 0; mi < 2; ++mi)
#pragma unroll
        for (int ni = 0; ni < 2; ++ni) {
            const int st = (wm * 2 + mi) * 4 + wn * 2 + ni;
            if (st % 5 == sp) continue;
            const int mt32 = mt * 8 + wm * 2 + mi, nt32 = nt * 4 + wn * 2 + ni;
            char* dst = ws + WS_PART + (size_t)sp * SLAB + (size_t)(mt32 * 20 + nt32) * 4096 + lane * 16;
#pragma unroll
            for (int gq = 0; gq < 4; ++gq) {
                f32x4 v = {acc[mi][ni][4 * gq], acc[mi][ni][4 * gq + 1], acc[mi][ni][4 * gq + 2], acc[mi][ni][4 * gq + 3]};
                asm volatile("global_store_dwordx4 %0, %1, off sc1\n\ts_nop 1" :: "v"(dst + gq * 1024), "v"(v) : "memory");
            }
        }
    asm volatile("s_waitcnt vmcnt(0)" ::: "memory");
    BARRIER();
    unsigned* cnt = (unsigned*)(ws + WS_CNT) + (mt * 5 + nt) * 64;
    if (wid == 0) {
        if (lane == 0) __hip_atomic_fetch_add(cnt, 1u, __ATOMIC_RELAXED, __HIP_MEMORY_SCOPE_AGENT);
        for (unsigned spins = 0; spins < (1u << 24); ++spins) {
            const unsigned c = __builtin_amdgcn_readfirstlane(__hip_atomic_load(cnt, __ATOMIC_RELAXED, __HIP_MEMORY_SCOPE_AGENT));
            if (c >= (unsigned)NSPLIT) break;
            __builtin_amdgcn_s_sleep(1);
        }
    }
    BARRIER();
    const auto rsrc = __builtin_amdgcn_make_buffer_rsrc((void*)(ws + WS_PART), 0, (int)(NSPLIT * SLAB), 0x00020000);
#pragma unroll
    for (int mi = 0; mi < 2; ++mi)
#pragma unroll
        for (int ni = 0; ni < 2; ++ni) {
            const int st = (wm * 2 + mi) * 4 + wn * 2 + ni;
            if (st % 5 != sp) continue;
            const int mt32 = mt * 8 + wm * 2 + mi, nt32 = nt * 4 + wn * 2 + ni;
            const int off = (mt32 * 20 + nt32) * 4096 + lane * 16;
            u32x4 v[4][4];
#pragma unroll
            for (int s = 0; s < 4; ++s) {
                const int so = (s < sp ? s : s + 1) * (int)SLAB;
#pragma unroll
                for (int gq = 0; gq < 4; ++gq) v[s][gq] = __builtin_amdgcn_raw_buffer_load_b128(rsrc, off + gq * 1024, so, 16);
            }
            char* op = (char*)out + (size_t)(mt32 * 32 + r) * 2560 + (640 + nt32 * 32 + 4 * h) * 2;
#pragma unroll
            for (int gq = 0; gq < 4; ++gq) {
                f32x4 t = {acc[mi][ni][4 * gq], acc[mi][ni][4 * gq + 1], acc[mi][ni][4 * gq + 2], acc[mi][ni][4 * gq + 3]};
                t += (__builtin_bit_cast(f32x4, v[0][gq]) + __builtin_bit_cast(f32x4, v[1][gq])) +
                     (__builtin_bit_cast(f32x4, v[2][gq]) + __builtin_bit_cast(f32x4, v[3][gq]));
                u32x2 o;
                o[0] = pack2(t[0], t[1]);
                o[1] = pack2(t[2], t[3]);
                *(u32x2*)(op + gq * 16) = o;
            }
        }
}

extern "C" void kernel_launch(void* const* d_in, const int* in_sizes, int n_in,
                              void* d_out, int out_size, void* d_ws, size_t ws_size,
                              hipStream_t stream) {
    (void)in_sizes; (void)n_in; (void)out_size; (void)ws_size;
    const __hip_bfloat16* Ax = (const __hip_bfloat16*)d_in[0];
    const __hip_bfloat16* Awqkv = (const __hip_bfloat16*)d_in[1];
    const __hip_bfloat16* Awo = (const __hip_bfloat16*)d_in[2];
    const __hip_bfloat16* Bx = (const __hip_bfloat16*)d_in[3];
    const __hip_bfloat16* Bckv = (const __hip_bfloat16*)d_in[4];
    const __hip_bfloat16* Bwq = (const __hip_bfloat16*)d_in[5];
    const __hip_bfloat16* Bwuk = (const __hip_bfloat16*)d_in[6];
    const __hip_bfloat16* Bwuv = (const __hip_bfloat16*)d_in[7];
    const __hip_bfloat16* Bwo = (const __hip_bfloat16*)d_in[8];
    char* ws = (char*)d_ws;
    (void)hipFuncSetAttribute((const void*)k1_proj, hipFuncAttributeMaxDynamicSharedMemorySize, G1_LDS);
    (void)hipFuncSetAttribute((const void*)k2_attn, hipFuncAttributeMaxDynamicSharedMemorySize, K2_LDS);
    (void)hipFuncSetAttribute((const void*)k3_oproj, hipFuncAttributeMaxDynamicSharedMemorySize, G3_LDS);
    k1_proj<<<256, 512, G1_LDS, stream>>>(Ax, Awqkv, Bx, Bckv, Bwq, Bwuk, Bwuv, Awo, Bwo, ws);
    k2_attn<<<256, 512, K2_LDS, stream>>>(ws);
    k3_oproj<<<240, 512, G3_LDS, stream>>>(Awo, Bwo, ws, (__hip_bfloat16*)d_out);
}
```

```cpp
#include <hip/hip_runtime.h>
#include <hip/hip_bf16.h>
#include <stdint.h>
#include <type_traits>

typedef short s16x4 __attribute__((ext_vector_type(4)));
typedef short bf16x8 __attribute__((ext_vector_type(8)));
typedef float f32x16 __attribute__((ext_vector_type(16)));
typedef float f32x4 __attribute__((ext_vector_type(4)));
typedef float f32x2 __attribute__((ext_vector_type(2)));
typedef unsigned u32x4 __attribute__((ext_vector_type(4)));
typedef unsigned u32x2 __attribute__((ext_vector_type(2)));
typedef __bf16 bf16x2_t __attribute__((ext_vector_type(2)));
#define LAS __attribute__((address_space(3)))
#define DI __device__ __forceinline__
#define MFMA32(a, b, c) __builtin_amdgcn_mfma_f32_32x32x16_bf16((a), (b), (c), 0, 0, 0)
#define MFMA16(a, b, c) __builtin_amdgcn_mfma_f32_16x16x32_bf16((a), (b), (c), 0, 0, 0)
#define CFENCE() asm volatile("" ::: "memory")
#define BARRIER() do { CFENCE(); __builtin_amdgcn_s_barrier(); CFENCE(); } while (0)
#define WAIT_VM(n) asm volatile("s_waitcnt vmcnt(" #n ") lgkmcnt(0)" ::: "memory")
#define SBAR() __builtin_amdgcn_sched_barrier(0)

DI unsigned pack2(float a, float b) {
    f32x2 v = {a, b};
    bf16x2_t r = __builtin_convertvector(v, bf16x2_t);
    return __builtin_bit_cast(unsigned, r);
}
DI u32x4 pack8(const f32x16& x, int s) {
    u32x4 w;
    w[0] = pack2(x[8 * s + 0], x[8 * s + 1]);
    w[1] = pack2(x[8 * s + 2], x[8 * s + 3]);
    w[2] = pack2(x[8 * s + 4], x[8 * s + 5]);
    w[3] = pack2(x[8 * s + 6], x[8 * s + 7]);
    return w;
}
DI u32x4 pack8s(const f32x16& x, int s, float c) {
    u32x4 w;
    w[0] = pack2(x[8 * s + 0] * c, x[8 * s + 1] * c);
    w[1] = pack2(x[8 * s + 2] * c, x[8 * s + 3] * c);
    w[2] = pack2(x[8 * s + 4] * c, x[8 * s + 5] * c);
    w[3] = pack2(x[8 * s + 6] * c, x[8 * s + 7] * c);
    return w;
}
DI bf16x8 tr_frag(LAS char* p0, LAS char* p1) {
    s16x4 lo = __builtin_amdgcn_ds_read_tr16_b64_v4i16((LAS s16x4*)p0);
    s16x4 hi = __builtin_amdgcn_ds_read_tr16_b64_v4i16((LAS s16x4*)p1);
    return __builtin_shufflevector(lo, hi, 0, 1, 2, 3, 4, 5, 6, 7);
}

DI void st16(void* p, u32x4 v) {
    asm volatile("global_store_dwordx4 %0, %1, off sc1\n\ts_nop 1" :: "v"(p), "v"(v) : "memory");
}
template <int I, int N, class F> DI void sfor(F&& f) {
    if constexpr (I < N) { f(std::integral_constant<int, I>{}); sfor<I + 1, N>(f); }
}
template <int OFF> DI void lds_rd(bf16x8& d, unsigned addr) {
    asm volatile("ds_read_b128 %0, %1 offset:%2" : "=v"(d) : "v"(addr), "i"(OFF));
}
template <int N> DI void lds_wait(bf16x8& d) { asm volatile("s_waitcnt lgkmcnt(%1)" : "+v"(d) : "i"(N)); }
template <int N> DI void lds_wait2(bf16x8& d, bf16x8& e) { asm volatile("s_waitcnt lgkmcnt(%2)" : "+v"(d), "+v"(e) : "i"(N)); }
#define LGKM0() __builtin_amdgcn_s_waitcnt(0xC07F)
DI void set_m0(unsigned v) { asm volatile("s_mov_b32 m0, %0\n\ts_nop 0" :: "s"(v) : "memory"); }
template <int OFF> DI void dma_s(unsigned voff, const char* sbase) {
    asm volatile("global_load_lds_dwordx4 %0, %1 offset:%2" :: "v"(voff), "s"(sbase), "i"(OFF) : "memory");
}

constexpr size_t WS_KVB = 0;
constexpr size_t WS_QB = WS_KVB + (size_t)32 * 1048576;
constexpr size_t WS_KVA = WS_QB + (size_t)16 * 1048576;
constexpr size_t WS_QA = WS_KVA + (size_t)8 * 1048576;
constexpr size_t WS_CTXB = WS_QA + (size_t)4 * 1048576;
constexpr size_t WS_CTXA = WS_CTXB + (size_t)16 * 1048576;
constexpr size_t WS_PART = WS_CTXA + (size_t)4 * 1048576;
constexpr size_t WS_CNT = WS_PART + (size_t)32 * 1048576;
constexpr int NSPLIT = 5;
constexpr size_t SLAB = (size_t)64 * 20 * 4096;

constexpr float CB = 0.08838834764831845f * 1.4426950408889634f;
constexpr float CA = 0.22360679774997896f * 1.4426950408889634f;

constexpr bool PREFETCH = true;
constexpr int RS = 8448;
constexpr int G1_BUF = 8 * RS;
constexpr int G1_LDS = 2 * G1_BUF + 2048;
enum { EP_BK = 0, EP_BV = 1, EP_BQ = 2, EP_AQK = 3, EP_AV = 4 };

struct G1Src { unsigned xoff[4], woff[4], toff; const char* Xb; const char* Wb; const char* Tb; unsigned wstep, tstep; int nkt; bool half; };
DI void g1_src(G1Src& s, const __hip_bfloat16* X, const __hip_bfloat16* W, int K, int ldw, int m0, int n0, int nlim, int wid, int lane, bool half) {
    s.half = half;
#pragma unroll
    for (int i = 0; i < 4; ++i) {
        const int row = (wid * 4 + i) * 8 + (lane >> 3);
        const int c = (lane & 7) ^ ((row >> 1) & 7);
        s.xoff[i] = (unsigned)(((m0 + row) * K + c * 8) * 2) + 3584 - i * 1024;
        const int k = (wid * 4 + i) * 2 + (lane >> 5);
        const int p = lane & 31, b = (p >> 2) ^ (k & 3);
        int col = n0 + (b * 4 + ((p & 3) ^ ((wid & 1) << 1))) * 8;
        col = col < nlim ? col : nlim - 8;
        s.woff[i] = (unsigned)((k * ldw + col) * 2) + 3584 - i * 1024;
    }
    s.Xb = (const char*)X; s.Wb = (const char*)W - 4096; s.wstep = (unsigned)(64 * ldw * 2); s.nkt = K >> 6;
    if (wid < 4) { s.Tb = s.Xb; s.tstep = 128u; s.toff = (unsigned)((m0 + wid * 64 + lane) * K * 2) + 3840; }
    else {
        const int i = (wid - 4) * 64 + lane;
        int col = n0 + (i & 3) * 64;
        col = col < nlim ? col : nlim - 64;
        s.Tb = (const char*)W; s.tstep = s.wstep; s.toff = (unsigned)(((i >> 2) * ldw + col) * 2) + 3840;
    }
}
DI void g1_touch(const G1Src& s, int kt) {
    asm volatile("global_load_lds_dword %0, %1 offset:-3840" :: "v"(s.toff), "s"(s.Tb + (size_t)kt * s.tstep) : "memory");
}
template <int I> DI void g1_piece(const G1Src& s, int kt) {
    if constexpr (I < 4) dma_s<I * 1024 - 3584>(s.xoff[I], s.Xb + (size_t)kt * 128);
    else if (!s.half || (threadIdx.x & 16) == 0) dma_s<(I - 4) * 1024 + 512>(s.woff[I - 4], s.Wb + (size_t)kt * s.wstep);
}

template <bool TR, int NT>
DI void g1_loop(f32x4 (&acc)[8][4], bf16x8 (&xs)[2][4], bf16x8 (&wsb)[2][4], const G1Src& cur, const G1Src& nxt, bool has_next, bool first,
                LAS char* lds, int wid, int lane) {
    const int wm = wid >> 2, wn = wid & 3, r16 = lane & 15, g = lane >> 4;
    const unsigned lds0 = (unsigned)(uintptr_t)lds;
    const int nkt = cur.nkt;
    int xb[2];
#pragma unroll
    for (int st = 0; st < 2; ++st) xb[st] = 256 + wm * 4 * RS + r16 * 128 + (((4 * st + g) ^ (r16 >> 1)) * 16);
    const int q = (lane >> 2) & 3, p4 = lane & 3;
    int wb[4];
#pragma unroll
    for (int nt = 0; nt < 4; ++nt) {
        const int b = NT == 4 ? wn * 2 + (nt >> 1) : wn;
        wb[nt] = 256 + g * RS + 4096 + q * 512 + ((b ^ q) * 64) + (((nt & 1) ^ (g & 1)) * 32) + p4 * 8;
    }
#pragma unroll
    for (int mt = 0; mt < 8; ++mt)
#pragma unroll
        for (int nt = 0; nt < 4; ++nt) acc[mt][nt] = f32x4{};
#define G1_RX(set, bb, st, mh)                                                                                   \
    _Pragma("unroll") for (int m4 = 0; m4 < 4; ++m4)                                                             \
        xs[set][m4] = *(LAS bf16x8*)((bb) + xb[st] + ((mh) * 2 + (m4 >> 1)) * RS + (m4 & 1) * 2048)
#define G1_RW(bb, st)                                                                                            \
    _Pragma("unroll") for (int nt = 0; nt < NTR; ++nt)                                                           \
        wsb[st][nt] = tr_frag((bb) + wb[nt] + (st) * 4 * RS, (bb) + wb[nt] + (st) * 4 * RS + 2048)
#define G1_MM(set, st, mh, on, kn, p0, pe)                                                                       \
    sfor<0, 4>([&](auto mc) {                                                                                    \
        constexpr int m4 = decltype(mc)::value;                                                                  \
        _Pragma("unroll") for (int nt = 0; nt < NT; ++nt)                                                        \
            acc[(mh) * 4 + m4][nt] = TR ? MFMA16(wsb[st][nt], xs[set][m4], acc[(mh) * 4 + m4][nt])               \
                                        : MFMA16(xs[set][m4], wsb[st][nt], acc[(mh) * 4 + m4][nt]);              \
        if constexpr ((pe) > (p0)) {                                                                             \
            SBAR();                                                                                              \
            if (on) {                                                                                            \
                if ((kn) < nkt) { g1_piece<2 * m4>(cur, (kn)); g1_piece<2 * m4 + 1>(cur, (kn)); }                \
                else { g1_piece<2 * m4>(nxt, (kn) - nkt); g1_piece<2 * m4 + 1>(nxt, (kn) - nkt); }               \
            }                                                                                                    \
            SBAR();                                                                                              \
        }                                                                                                        \
    })
    constexpr int NTR = NT;
    G1_RW(lds, 0);
    if (first) { G1_RX(0, lds, 0, 0); }
#pragma unroll 1
    for (int kt = 0; kt < nkt; ++kt) {
        LAS char* bb = lds + (kt & 1) * G1_BUF;
        LAS char* bn = lds + ((kt + 1) & 1) * G1_BUF;
        const bool on2 = kt + 2 < nkt || has_next;
        LGKM0(); G1_RX(1, bb, 0, 1); SBAR(); G1_MM(0, 0, 0, false, 0, 0, 0); SBAR();
        LGKM0(); G1_RW(bb, 1); G1_RX(0, bb, 1, 0); SBAR(); G1_MM(1, 0, 1, false, 0, 0, 0); SBAR();
        LGKM0(); G1_RX(1, bb, 1, 1); SBAR(); G1_MM(0, 1, 0, false, 0, 0, 0); SBAR();
        LGKM0();
        if (first && kt == 0) { WAIT_VM(7); } else { WAIT_VM(1); }
        BARRIER();
        set_m0(lds0 + (kt & 1) * G1_BUF + wid * RS + 256 + 3584);
        if (kt + 1 < nkt) G1_RW(bn, 0);
        if (kt + 1 < nkt || has_next) G1_RX(0, bn, 0, 0);
        SBAR(); G1_MM(1, 1, 1, on2, kt + 2, 0, 8); SBAR();
        if (kt + 4 < nkt || !has_next) g1_touch(cur, kt + 4 < nkt ? kt + 4 : nkt - 1);
        else g1_touch(nxt, kt + 4 - nkt);
        SBAR();
    }
#undef G1_RX
#undef G1_RW
#undef G1_MM
}

DI void st8(void* p, u32x2 v) {
    asm volatile("global_store_dwordx2 %0, %1, off sc1\n\ts_nop 1" :: "v"(p), "v"(v) : "memory");
}

template <int EP>
DI void g1_epi(const f32x4 (&acc)[8][4], int m0, int n0, int nlim, int nsec, char* __restrict__ ws, int wid, int lane) {
    const int wm = wid >> 2, wn = wid & 3, c = lane & 15, g = lane >> 4;
    if constexpr (EP == EP_BK || EP == EP_BQ || EP == EP_BV) {
#pragma unroll
        for (int mt = 0; mt < 8; ++mt) {
            const int tb = (m0 + wm * 128 + mt * 16) >> 5;
            const int tile = tb >> 1, kb = tb & 1, th = mt & 1;
#pragma unroll
            for (int np = 0; np < 2; ++np) {
                const int nb = n0 + wn * 64 + np * 32;
                const int head = nb >> 7;
                const float sc = EP == EP_BQ ? CB : 1.0f;
                const f32x4 a = acc[mt][2 * np], b = acc[mt][2 * np + 1];
                unsigned a0 = pack2(a[0] * sc, a[1] * sc), a1 = pack2(a[2] * sc, a[3] * sc);
                unsigned b0 = pack2(b[0] * sc, b[1] * sc), b1 = pack2(b[2] * sc, b[3] * sc);
                if constexpr (EP == EP_BV) {
                    auto r0 = __builtin_amdgcn_permlane32_swap(a0, b0, false, false);
                    auto r1 = __builtin_amdgcn_permlane32_swap(a1, b1, false, false);
                    const u32x4 w = {r0[0], r1[0], r0[1], r1[1]};
                    const int db = (nb >> 5) & 3;
                    char* base = ws + WS_KVB + (size_t)head * 1048576 + tile * 32768 + 16384 + ((kb * 2 + th) * 4 + db) * 1024;
                    st16(base + ((g >> 1) * 16 + c + 32 * (g & 1)) * 16, w);
                } else {
                    auto r0 = __builtin_amdgcn_permlane16_swap(a0, b0, false, false);
                    auto r1 = __builtin_amdgcn_permlane16_swap(a1, b1, false, false);
                    const u32x4 w = {r0[0], r1[0], r0[1], r1[1]};
                    const int ks = ((nb >> 4) & 7) + (g & 1);
                    char* base = EP == EP_BK ? ws + WS_KVB + (size_t)head * 1048576 + tile * 32768 + (kb * 8 + ks) * 1024
                                             : ws + WS_QB + (size_t)((head * 64 + tb) * 8 + ks) * 1024;
                    st16(base + (th * 16 + c + 32 * (g >> 1)) * 16, w);
                }
            }
        }
        return;
    }
#pragma unroll
    for (int mt = 0; mt < 8; ++mt) {
        const int tb = (m0 + wm * 128 + mt * 16) >> 5;
        const int tile = tb >> 1, kb = tb & 1, th = mt & 1;
#pragma unroll
        for (int nt = 0; nt < 4; ++nt) {
            const int nb = n0 + wn * 64 + nt * 16;
            if (nb >= nlim) continue;
            const f32x4 a = acc[mt][nt];
            if constexpr (EP == EP_BK || EP == EP_BQ || EP == EP_BV) {
            } else if constexpr (EP == EP_AQK) {
                const bool isq = nb < 640;
                const float sc = isq ? CA : 1.0f;
                const int r = th * 16 + c;
                const int n = nb - (isq ? 0 : 640) + 4 * g;
                const int head = n / 20, d0 = n - head * 20;
                char* base = isq ? ws + WS_QA + (size_t)((head * 64 + tb) * 2) * 1024
                                 : ws + WS_KVA + (size_t)head * 262144 + tile * 8192 + kb * 2048;
                char* dst = base + (d0 >> 4) * 1024 + (r + 32 * ((d0 >> 3) & 1)) * 16 + (d0 & 4) * 2;
                u32x2 w;
                w[0] = pack2(a[0] * sc, a[1] * sc);
                w[1] = pack2(a[2] * sc, a[3] * sc);
                *(u32x2*)dst = w;
                if (d0 == 16) {
                    *(u32x2*)(dst + 8) = (u32x2){isq ? 0u : 0x3F803F80u, 0u};
                    *(u32x4*)(base + 1024 + (r + 32) * 16) = (u32x4){0u, 0u, 0u, 0u};
                }
            } else {
                const int n = nb - nsec + c;
                const int head = n / 20, d = n - head * 20;
                char* dst = ws + WS_KVA + (size_t)head * 262144 + tile * 8192 + 4096 + (kb * 2 + th) * 1024 + (d + 32 * (g & 1)) * 16 + (g >> 1) * 8;
                u32x2 w;
                w[0] = pack2(a[0], a[1]);
                w[1] = pack2(a[2], a[3]);
                *(u32x2*)dst = w;
                if (d == 19) *(u32x2*)(dst + 16) = (u32x2){0x3F803F80u, 0x3F803F80u};
            }
        }
    }
}

__global__ __launch_bounds__(512, 2) void k1_proj(const __hip_bfloat16* __restrict__ Ax, const __hip_bfloat16* __restrict__ Awqkv,
                                                  const __hip_bfloat16* __restrict__ Bx, const __hip_bfloat16* __restrict__ Bckv,
                                                  const __hip_bfloat16* __restrict__ Bwq, const __hip_bfloat16* __restrict__ Bwuk,
                                                  const __hip_bfloat16* __restrict__ Bwuv, const __hip_bfloat16* __restrict__ Awo,
                                                  const __hip_bfloat16* __restrict__ Bwo, char* __restrict__ ws) {
    extern __shared__ __attribute__((aligned(16))) char smem[];
    LAS char* lds = (LAS char*)smem;
    const int tid = threadIdx.x, lane = tid & 63, wid = __builtin_amdgcn_readfirstlane(tid >> 6);
    const int bid = blockIdx.x;
    const int m0 = ((bid >> 3) & 7) * 256;
    const int grp = bid >> 6, c8 = bid & 7;
    const int cc = c8 + 8 * (grp & 1);
    const bool has2 = grp < 3;
    G1Src s1, s2;
    if (grp < 2) {
        g1_src(s1, Bckv, Bwuk, 512, 4096, m0, cc * 256, 4096, wid, lane, false);
        g1_src(s2, Bx, Bwq, 640, 4096, m0, cc * 256, 4096, wid, lane, false);
    } else if (grp == 2) {
        g1_src(s1, Bckv, Bwuv, 512, 4096, m0, c8 * 256, 4096, wid, lane, false);
        g1_src(s2, Bckv, Bwuv, 512, 4096, m0, (c8 + 8) * 256, 4096, wid, lane, false);
    } else {
        g1_src(s1, Ax, Awqkv, 640, 1920, m0, c8 * 256, c8 < 5 ? 1280 : 1920, wid, lane, false);
        s2 = s1;
    }

    f32x4 acc[8][4];
    bf16x8 xf[2][4], wf[2][4];
    const unsigned lds0 = (unsigned)(uintptr_t)lds;
    if (bid == 255 && tid < 40) ((unsigned*)(ws + WS_CNT))[tid * 64] = 0u;
    set_m0(lds0 + wid * RS + 256 + 3584);
    sfor<0, 8>([&](auto ic) { g1_piece<decltype(ic)::value>(s1, 0); });
    set_m0(lds0 + G1_BUF + wid * RS + 256 + 3584);
    sfor<0, 8>([&](auto ic) { g1_piece<decltype(ic)::value>(s1, 1); });
    g1_touch(s1, 2);
    g1_touch(s1, 3);
    WAIT_VM(10);
    BARRIER();
    {
        set_m0(lds0 + 2 * G1_BUF + wid * 256);
        const unsigned gw = (unsigned)(bid * 8 + wid) * 64 + lane;
#define PF(ptr, nbytes) do { constexpr unsigned R_ = 131072u / ((nbytes) / 128u);                                     \
            const unsigned ln_ = gw / R_;                                                                                \
            const unsigned off_ = (gw % R_ == 0u && ln_ * 128u < (nbytes)) ? ln_ * 128u : 0u;                             \
            asm volatile("global_load_lds_dword %0, %1" :: "v"(off_), "s"((const char*)(ptr)) : "memory"); } while (0)
        PF(Bx, 2048u * 640u * 2u); PF(Bwq, 640u * 4096u * 2u); PF(Bwuv, 512u * 4096u * 2u);
        PF(Bwo, 4096u * 640u * 2u); PF(Awo, 640u * 640u * 2u);
#undef PF
    }
    if (grp < 2 || (grp == 3 && c8 < 5)) {
        g1_loop<true, 4>(acc, xf, wf, s1, s2, has2, true, lds, wid, lane);
        if (grp < 2) g1_epi<EP_BK>(acc, m0, cc * 256, 4096, 0, ws, wid, lane);
        else g1_epi<EP_AQK>(acc, m0, c8 * 256, 1280, 0, ws, wid, lane);
    } else {
        g1_loop<false, 4>(acc, xf, wf, s1, s2, has2, true, lds, wid, lane);
        if (grp == 2) g1_epi<EP_BV>(acc, m0, c8 * 256, 4096, 0, ws, wid, lane);
        else g1_epi<EP_AV>(acc, m0, c8 * 256, 1920, 1280, ws, wid, lane);
    }
    if (grp < 2) {
        g1_loop<true, 4>(acc, xf, wf, s2, s2, false, false, lds, wid, lane);
        g1_epi<EP_BQ>(acc, m0, cc * 256, 4096, 0, ws, wid, lane);
    } else if (grp == 2) {
        g1_loop<false, 4>(acc, xf, wf, s2, s2, false, false, lds, wid, lane);
        g1_epi<EP_BV>(acc, m0, (c8 + 8) * 256, 4096, 0, ws, wid, lane);
    }
    asm volatile("s_waitcnt vmcnt(0)" ::: "memory");
}

constexpr int SLOT = 40960;
constexpr int NSLOT = 4;
constexpr int K2_LDS = NSLOT * SLOT;
constexpr int NIT = 34;

__global__ __launch_bounds__(512, 2) void k2_attn(char* __restrict__ ws) {
    extern __shared__ __attribute__((aligned(16))) char smem[];
    LAS char* lds = (LAS char*)smem;
    const int tid = threadIdx.x, lane = tid & 63, wid = __builtin_amdgcn_readfirstlane(tid >> 6);
    const int r = lane & 31, h = lane >> 5;
    const int g = blockIdx.x, xcd = g & 7, idx = g >> 3;
    const int head = xcd * 4 + (idx >> 3), x = idx & 7;
    const char* kvb = ws + WS_KVB + (size_t)head * 1048576;
    const char* kva = ws + WS_KVA + (size_t)head * 262144;
    const int n1 = 32 - 2 * x;
    const unsigned lbase = (unsigned)(uintptr_t)lds + lane * 16;

    const unsigned lds0 = (unsigned)(uintptr_t)lds;
    const unsigned voff = lane * 16;
    const int wa = wid & 3;
    const char* sbB = kvb;
    const char* sbA = kva;
#define K2_M0(tt)                                                                           \
    do {                                                                                    \
        const int t3_ = (tt) < NIT ? (tt) : NIT - 1;                                        \
        const int tb3_ = t3_ < n1 ? t3_ : t3_ - n1;                                         \
        const int ta3_ = t3_ < 32 ? t3_ : 31;                                               \
        sbB = kvb + (size_t)tb3_ * 32768 + wid * 4096 + 2048;                               \
        sbA = kva + (size_t)ta3_ * 8192 + wid * 1024 - 2048;                                \
        set_m0(lds0 + ((tt) < NIT ? ((tt) & 3) : 2 + ((tt) & 1)) * SLOT + wid * 5120 + 2048); \
    } while (0)
#define K2_DMA(i) do { SBAR(); if constexpr ((i) < 4) dma_s<(i) * 1024 - 2048>(voff, sbB); else dma_s<2048>(voff, sbA); SBAR(); } while (0)
#define K2_STAGE_ALL(tt) do { K2_M0(tt); K2_DMA(0); K2_DMA(1); K2_DMA(2); K2_DMA(3); K2_DMA(4); } while (0)
#define BOFF(f) (((f) >> 2) * 5120 + ((f) & 3) * 1024)
#define AOFF(a) ((a) * 5120 + 4096)
    K2_STAGE_ALL(0);
    K2_STAGE_ALL(1);

#define K2_SYNC(it) do { WAIT_VM(5); BARRIER(); K2_M0((it) + 3); } while (0)

    if (wid < 4) {
        bf16x8 fr[8];
        K2_SYNC(-1);
        sfor<0, 8>([&](auto ic) { constexpr int j = decltype(ic)::value; lds_rd<BOFF((j & 1) * 8 + (j >> 1))>(fr[j], lbase); });
        K2_DMA(0); K2_DMA(1);
        int it = 0;
        for (int blk = 0; blk < 2; ++blk) {
            const int qb = blk == 0 ? 15 - x : x;
            const int nt = 2 * qb + 2;
            const int q0 = qb * 128 + wid * 32;
            const int tb32 = q0 >> 5;
            bf16x8 qf[8];
            const char* qp = ws + WS_QB + (size_t)((head * 64 + tb32) * 8) * 1024 + lane * 16;
#pragma unroll
            for (int ks = 0; ks < 8; ++ks) qf[ks] = *(const bf16x8*)(qp + ks * 1024);
            f32x16 o[4];
#pragma unroll
            for (int db = 0; db < 4; ++db) o[db] = f32x16{};
            f32x16 negm = f32x16{};
            float l = 0.f;
            for (int t = 0; t < nt; ++t, ++it) {
                const int rel = (q0 - 64 * t) >> 5;
                const unsigned ac = lbase + (it & 3) * SLOT;
                const unsigned an = lbase + ((it + 1) & 3) * SLOT;
                f32x16 s0, s1;
                sfor<0, 16>([&](auto ic) {
                    constexpr int j = decltype(ic)::value, ks = j >> 1;
                    lds_wait<7>(fr[j & 7]);
                    if constexpr (j == 0) s0 = MFMA32(fr[0], qf[0], negm);
                    else if constexpr (j == 1) s1 = MFMA32(fr[1], qf[0], negm);
                    else if constexpr ((j & 1) == 0) s0 = MFMA32(fr[j & 7], qf[ks], s0);
                    else s1 = MFMA32(fr[j & 7], qf[ks], s1);
                    constexpr int jn = j + 8;
                    SBAR();
                    if constexpr (jn < 16) lds_rd<BOFF((jn & 1) * 8 + (jn >> 1))>(fr[j & 7], ac);
                    else lds_rd<BOFF(jn)>(fr[j & 7], ac);
                    if constexpr (j == 2) K2_DMA(2);
                    if constexpr (j == 8) K2_DMA(3);
                    if constexpr (j == 14) K2_DMA(4);
                    SBAR();
                });
#define B_MASK()                                                                                        \
    do {                                                                                                \
        if (rel <= 1) {                                                                                 \
            const int lim = 32 * rel + r - 4 * h;                                                       \
            _Pragma("unroll") for (int i = 0; i < 16; ++i) {                                            \
                const int kr = (i & 3) + 8 * (i >> 2);                                                  \
                if (kr > lim) s0[i] = -__builtin_inff();                                                \
                if (kr + 32 > lim) s1[i] = -__builtin_inff();                                           \
            }                                                                                           \
        }                                                                                               \
    } while (0)
                B_MASK();
                float ps = 0.f;
                if (t > 0) {
#pragma unroll
                    for (int i = 0; i < 16; ++i) { s0[i] = __builtin_amdgcn_exp2f(s0[i]); ps += s0[i]; }
#pragma unroll
                    for (int i = 0; i < 16; ++i) { s1[i] = __builtin_amdgcn_exp2f(s1[i]); ps += s1[i]; }
                }
                if (t == 0 || __any(!(ps <= 65536.f))) {
                    if (t > 0) {
                        LAS char* sl = lds + (it & 3) * SLOT + lane * 16;
                        s0 = negm; s1 = negm;
#pragma unroll 1
                        for (int ks = 0; ks < 8; ++ks) {
                            bf16x8 qq = qf[0];
#pragma unroll
                            for (int u = 1; u < 8; ++u) qq = (ks == u) ? qf[u] : qq;
                            s0 = MFMA32(*(LAS bf16x8*)(sl + (ks >> 2) * 5120 + (ks & 3) * 1024), qq, s0);
                            s1 = MFMA32(*(LAS bf16x8*)(sl + 10240 + (ks >> 2) * 5120 + (ks & 3) * 1024), qq, s1);
                        }
                        B_MASK();
                    }
                    float mx = s0[0];
#pragma unroll
                    for (int i = 1; i < 16; ++i) mx = fmaxf(mx, s0[i]);
#pragma unroll
                    for (int i = 0; i < 16; ++i) mx = fmaxf(mx, s1[i]);
                    auto rr = __builtin_amdgcn_permlane32_swap(__float_as_uint(mx), __float_as_uint(mx), false, false);
                    mx = fmaxf(__uint_as_float(rr[0]), __uint_as_float(rr[1]));
                    float delta = mx;
                    if (t > 0) {
                        delta = fmaxf(mx, 0.f);
                        const float alpha = __builtin_amdgcn_exp2f(-delta);
                        l *= alpha;
#pragma unroll
                        for (int db = 0; db < 4; ++db)
#pragma unroll
                            for (int i = 0; i < 16; ++i) o[db][i] *= alpha;
                    }
                    const float nm = negm[0] - delta;
#pragma unroll
                    for (int i = 0; i < 16; ++i) negm[i] = nm;
                    ps = 0.f;
#pragma unroll
                    for (int i = 0; i < 16; ++i) { s0[i] = __builtin_amdgcn_exp2f(s0[i] - delta); ps += s0[i]; }
#pragma unroll
                    for (int i = 0; i < 16; ++i) { s1[i] = __builtin_amdgcn_exp2f(s1[i] - delta); ps += s1[i]; }
                }
                l += ps;
                bf16x8 pf[4];
                pf[0] = __builtin_bit_cast(bf16x8, pack8(s0, 0));
                pf[1] = __builtin_bit_cast(bf16x8, pack8(s0, 1));
                pf[2] = __builtin_bit_cast(bf16x8, pack8(s1, 0));
                pf[3] = __builtin_bit_cast(bf16x8, pack8(s1, 1));
                if (it + 1 < NIT) { K2_SYNC(it); } else { K2_M0(it + 3); }
                sfor<0, 16>([&](auto ic) {
                    constexpr int j = decltype(ic)::value, s4 = j >> 2, db = j & 3;
                    lds_wait<7>(fr[j & 7]);
                    o[db] = MFMA32(fr[j & 7], pf[s4], o[db]);
                    SBAR();
                    if constexpr (j < 8) lds_rd<BOFF(24 + j)>(fr[j & 7], ac);
                    else { constexpr int jn = j - 8; lds_rd<BOFF((jn & 1) * 8 + (jn >> 1))>(fr[j & 7], an); }
                    if constexpr (j == 4) K2_DMA(0);
                    if constexpr (j == 10) K2_DMA(1);
                    SBAR();
                });
            }
            auto rr = __builtin_amdgcn_permlane32_swap(__float_as_uint(l), __float_as_uint(l), false, false);
            const float inv = 1.0f / (__uint_as_float(rr[0]) + __uint_as_float(rr[1]));
            char* cp = ws + WS_CTXB + (size_t)(tb32 * 256 + head * 8) * 1024 + lane * 16;
#pragma unroll
            for (int db = 0; db < 4; ++db) {
                st16(cp + (2 * db) * 1024, pack8s(o[db], 0, inv));
                st16(cp + (2 * db + 1) * 1024, pack8s(o[db], 1, inv));
            }
        }
        WAIT_VM(0);
#undef B_MASK
    } else {
        const int tb32 = x * 8 + wa * 2;
        bf16x8 qf[2][2];
        const char* qp = ws + WS_QA + (size_t)((head * 64 + tb32) * 2) * 1024 + lane * 16;
#pragma unroll
        for (int qt = 0; qt < 2; ++qt)
#pragma unroll
            for (int ks = 0; ks < 2; ++ks) qf[qt][ks] = *(const bf16x8*)(qp + (qt * 2 + ks) * 1024);
        f32x16 o[2];
        o[0] = f32x16{}; o[1] = f32x16{};
        float negm[2] = {0.f, 0.f};
        bf16x8 kf[4], vf[4];
        f32x16 s[2][2];
        K2_SYNC(-1);
        lds_rd<AOFF(0)>(kf[0], lbase); lds_rd<AOFF(1)>(kf[1], lbase); lds_rd<AOFF(2)>(kf[2], lbase); lds_rd<AOFF(3)>(kf[3], lbase);
        for (int it = 0; it <= 32; ++it) {
            if (it > 0) {
                u32x4 pf[2][2][2];
                unsigned chk = 0;
                sfor<0, 4>([&](auto bc) {
                    constexpr int blk = decltype(bc)::value, kb = blk >> 1, qt = blk & 1;
                    f32x16 e;
#pragma unroll
                    for (int i = 0; i < 8; ++i) e[i] = __builtin_amdgcn_exp2f(s[kb][qt][i]);
                    K2_DMA(blk);
#pragma unroll
                    for (int i = 8; i < 16; ++i) e[i] = __builtin_amdgcn_exp2f(s[kb][qt][i]);
                    if constexpr (blk == 3) K2_DMA(4);
                    pf[kb][qt][0] = pack8(e, 0);
                    pf[kb][qt][1] = pack8(e, 1);
                    chk |= pf[kb][qt][0][0] | pf[kb][qt][0][1] | pf[kb][qt][0][2] | pf[kb][qt][0][3];
                    chk |= pf[kb][qt][1][0] | pf[kb][qt][1][1] | pf[kb][qt][1][2] | pf[kb][qt][1][3];
                });
                const bool first = (it == 1);
                if (first || __any((chk & 0x40004000u) != 0u)) {
#pragma unroll
                    for (int qt = 0; qt < 2; ++qt) {
                        float mx = s[0][qt][0];
#pragma unroll
                        for (int i = 1; i < 16; ++i) mx = fmaxf(mx, s[0][qt][i]);
#pragma unroll
                        for (int i = 0; i < 16; ++i) mx = fmaxf(mx, s[1][qt][i]);
                        auto rr = __builtin_amdgcn_permlane32_swap(__float_as_uint(mx), __float_as_uint(mx), false, false);
                        mx = fmaxf(__uint_as_float(rr[0]), __uint_as_float(rr[1]));
                        float delta = mx + 10.f;
                        if (!first) delta = fmaxf(delta, 0.f);
                        const float tgt = negm[qt] - delta;
                        const unsigned hi = pack2(tgt, 0.f) & 0xFFFFu;
                        const float hif = __uint_as_float(hi << 16);
                        const unsigned lo = pack2(tgt - hif, 0.f) & 0xFFFFu;
                        const float neff = hif + __uint_as_float(lo << 16);
                        delta = negm[qt] - neff;
                        negm[qt] = neff;
                        {
                            u32x4 qq = __builtin_bit_cast(u32x4, qf[qt][1]);
                            qq[2] = h ? 0u : (hi | (lo << 16));
                            qf[qt][1] = __builtin_bit_cast(bf16x8, qq);
                        }
                        if (!first) {
                            const float alpha = __builtin_amdgcn_exp2f(-delta);
#pragma unroll
                            for (int i = 0; i < 16; ++i) o[qt][i] *= alpha;
                        }
#pragma unroll
                        for (int kb = 0; kb < 2; ++kb) {
                            f32x16 e;
#pragma unroll
                            for (int i = 0; i < 16; ++i) e[i] = __builtin_amdgcn_exp2f(s[kb][qt][i] - delta);
                            pf[kb][qt][0] = pack8(e, 0);
                            pf[kb][qt][1] = pack8(e, 1);
                        }
                    }
                }
                lds_wait2<4>(vf[0], vf[1]);
                lds_wait2<4>(vf[2], vf[3]);
#pragma unroll
                for (int kb = 0; kb < 2; ++kb)
#pragma unroll
                    for (int s2 = 0; s2 < 2; ++s2)
#pragma unroll
                        for (int qt = 0; qt < 2; ++qt)
                            o[qt] = MFMA32(vf[kb * 2 + s2], __builtin_bit_cast(bf16x8, pf[kb][qt][s2]), o[qt]);
            } else {
                K2_DMA(0); K2_DMA(1); K2_DMA(2); K2_DMA(3); K2_DMA(4);
            }
            lds_wait2<0>(kf[0], kf[1]);
            lds_wait2<0>(kf[2], kf[3]);
#pragma unroll
            for (int kb = 0; kb < 2; ++kb)
#pragma unroll
                for (int qt = 0; qt < 2; ++qt) {
                    s[kb][qt] = MFMA32(kf[kb * 2], qf[qt][0], f32x16{});
                    s[kb][qt] = MFMA32(kf[kb * 2 + 1], qf[qt][1], s[kb][qt]);
                }
            SBAR();
            K2_SYNC(it);
            const unsigned ac = lbase + (it & 3) * SLOT;
            const unsigned an = lbase + ((it + 1) & 3) * SLOT;
            lds_rd<AOFF(4)>(vf[0], ac); lds_rd<AOFF(5)>(vf[1], ac); lds_rd<AOFF(6)>(vf[2], ac); lds_rd<AOFF(7)>(vf[3], ac);
            lds_rd<AOFF(0)>(kf[0], an); lds_rd<AOFF(1)>(kf[1], an); lds_rd<AOFF(2)>(kf[2], an); lds_rd<AOFF(3)>(kf[3], an);
        }
        WAIT_VM(0);
#pragma unroll
        for (int qt = 0; qt < 2; ++qt) {
            const unsigned lv = __float_as_uint(o[qt][8]);
            auto rr = __builtin_amdgcn_permlane32_swap(lv, lv, false, false);
            const float lsum = __uint_as_float(rr[1]);
            const float inv = 1.0f / lsum;
            char* cp = ws + WS_CTXA + (size_t)((tb32 + qt) * 40) * 1024;
#pragma unroll
            for (int gq = 0; gq < 3; ++gq) {
                const int d0 = 8 * gq + 4 * h;
                if (d0 < 20) {
                    const int k = head * 20 + d0;
                    u32x2 w;
                    w[0] = pack2(o[qt][4 * gq + 0] * inv, o[qt][4 * gq + 1] * inv);
                    w[1] = pack2(o[qt][4 * gq + 2] * inv, o[qt][4 * gq + 3] * inv);
                    *(u32x2*)(cp + (k >> 4) * 1024 + (r + 32 * ((k >> 3) & 1)) * 16 + (k & 4) * 2) = w;
                }
            }
        }
    }
}

constexpr int G3_STAGE = 49152;
constexpr int G3_LDS = 3 * G3_STAGE;

__global__ __launch_bounds__(512, 2) void k3_oproj(const __hip_bfloat16* __restrict__ Awo, const __hip_bfloat16* __restrict__ Bwo,
                                                   char* __restrict__ ws, __hip_bfloat16* __restrict__ out) {
    extern __shared__ __attribute__((aligned(16))) char smem[];
    LAS char* lds = (LAS char*)smem;
    const int tid = threadIdx.x, lane = tid & 63, wid = __builtin_amdgcn_readfirstlane(tid >> 6);
    const int wm = wid >> 1, wn = wid & 1, h = lane >> 5;
    const int bid = blockIdx.x;
    const bool isB = bid < 200;
    int mt, nt, sp, kt0, nkt, kstot;
    const char* Xc; const char* Wb;
    if (isB) {
        mt = bid & 7; const int rest = bid >> 3; nt = rest % 5; sp = rest / 5;
        kt0 = sp * 13; nkt = sp == 4 ? 12 : 13; kstot = 256;
        Xc = ws + WS_CTXB; Wb = (const char*)Bwo;
    } else {
        const int b2 = bid - 200; mt = b2 & 7; nt = b2 >> 3; sp = 5;
        kt0 = 0; nkt = 10; kstot = 40;
        Xc = ws + WS_CTXA; Wb = (const char*)Awo;
    }
    unsigned woff[2];
#pragma unroll
    for (int i = 0; i < 2; ++i) {
        const int k = (wid * 2 + i) * 4 + (lane >> 4);
        const int p = lane & 15, b = (p >> 2) ^ (k & 3);
        woff[i] = (unsigned)(((kt0 * 64 + k) * 640 + nt * 128 + (b * 4 + (p & 3)) * 8) * 2);
    }
    const unsigned lds0 = (unsigned)(uintptr_t)lds;
    const char* xsb = Xc + (size_t)((mt * 8 + wid) * kstot + kt0 * 4) * 1024 + 4096;
    const unsigned voff = lane * 16;
    woff[1] -= 1024;
    auto piece = [&](int kt, auto ic) {
        constexpr int i = decltype(ic)::value;
        if constexpr (i < 4) dma_s<i * 1024 - 4096>(voff, xsb + (size_t)kt * 4096);
        else dma_s<(i - 4) * 1024>(woff[i - 4], Wb + (size_t)kt * (64u * 640u * 2u));
    };
    const int q = (lane >> 2) & 3, p4 = lane & 3, g16 = (lane >> 4) & 1;
    const int r0 = isB ? 4 * h : 8 * h, r1 = isB ? 8 + 4 * h : 8 * h + 4;
    int wb0[2], wb1[2];
#pragma unroll
    for (int ni = 0; ni < 2; ++ni) {
        const int b = wn * 2 + ni;
        const int c = ((b ^ q) * 64) + g16 * 32 + p4 * 8;
        wb0[ni] = (r0 >> 3) * 6144 + 4096 + ((r0 & 7) + q) * 256 + c;
        wb1[ni] = (r1 >> 3) * 6144 + 4096 + ((r1 & 7) + q) * 256 + c;
    }
    const int xb0 = wm * 2 * 6144 + lane * 16;
    f32x16 acc[2][2];
#pragma unroll
    for (int mi = 0; mi < 2; ++mi)
#pragma unroll
        for (int ni = 0; ni < 2; ++ni) acc[mi][ni] = f32x16{};

    bf16x8 xf[2][4][2], wf[2][4][2];
#define G3_RD(set, bb)                                                                                           \
    do {                                                                                                         \
        _Pragma("unroll") for (int ks = 0; ks < 4; ++ks) {                                                       \
            _Pragma("unroll") for (int ni = 0; ni < 2; ++ni)                                                     \
                wf[set][ks][ni] = tr_frag((bb) + wb0[ni] + ks * 12288, (bb) + wb1[ni] + ks * 12288);             \
            _Pragma("unroll") for (int mi = 0; mi < 2; ++mi)                                                     \
                xf[set][ks][mi] = *(LAS bf16x8*)((bb) + xb0 + mi * 6144 + ks * 1024);                            \
        }                                                                                                        \
    } while (0)
#define G3_STEP(set, kt)                                                                                         \
    do {                                                                                                         \
        if ((kt) + 2 < nkt) { WAIT_VM(6); } else { WAIT_VM(0); }                                                 \
        LGKM0();                                                                                                 \
        BARRIER();                                                                                               \
        const int b0_ = (kt) % 3, b1_ = ((kt) + 1) % 3;                                                          \
        set_m0(lds0 + b0_ * G3_STAGE + wid * 6144 + 4096);                                                       \
        if ((kt) + 1 < nkt) G3_RD((set) ^ 1, lds + b1_ * G3_STAGE);                                              \
        SBAR();                                                                                                  \
        const bool on_ = (kt) + 3 < nkt;                                                                         \
        sfor<0, 8>([&](auto jc) {                                                                                \
            constexpr int j = decltype(jc)::value, ks = j >> 1, mi = j & 1;                                      \
            _Pragma("unroll") for (int ni = 0; ni < 2; ++ni)                                                     \
                acc[mi][ni] = MFMA32(wf[set][ks][ni], xf[set][ks][mi], acc[mi][ni]);                             \
            if constexpr (j < 6) { SBAR(); if (on_) piece((kt) + 3, jc); SBAR(); }                               \
        });                                                                                                      \
        SBAR();                                                                                                  \
    } while (0)
#pragma unroll
    for (int t = 0; t < 3; ++t) {
        set_m0(lds0 + t * G3_STAGE + wid * 6144 + 4096);
        sfor<0, 6>([&](auto ic) { piece(t, ic); });
    }
    WAIT_VM(12);
    BARRIER();
    G3_RD(0, lds);
    int kt = 0;
#pragma unroll 1
    for (; kt + 1 < nkt; kt += 2) { G3_STEP(0, kt); G3_STEP(1, kt + 1); }
    if (kt < nkt) G3_STEP(0, kt);
#undef G3_RD
#undef G3_STEP
    const int r = lane & 31;
    if (!isB) {
#pragma unroll
        for (int mi = 0; mi < 2; ++mi)
#pragma unroll
            for (int ni = 0; ni < 2; ++ni) {
                char* op = (char*)out + (size_t)((mt * 8 + wm * 2 + mi) * 32 + r) * 2560 + ((nt * 4 + wn * 2 + ni) * 32 + 4 * h) * 2;
#pragma unroll
                for (int gq = 0; gq < 4; ++gq) {
                    u32x2 o;
                    o[0] = pack2(acc[mi][ni][4 * gq], acc[mi][ni][4 * gq + 1]);
                    o[1] = pack2(acc[mi][ni][4 * gq + 2], acc[mi][ni][4 * gq + 3]);
                    *(u32x2*)(op + gq * 16) = o;
                }
            }
        return;
    }
#pragma unroll
    for (int mi = 0; mi < 2; ++mi)
#pragma unroll
        for (int ni = 0; ni < 2; ++ni) {
            const int st = (wm * 2 + mi) * 4 + wn * 2 + ni;
            if (st % 5 == sp) continue;
            const int mt32 = mt * 8 + wm * 2 + mi, nt32 = nt * 4 + wn * 2 + ni;
            char* dst = ws + WS_PART + (size_t)sp * SLAB + (size_t)(mt32 * 20 + nt32) * 4096 + lane * 16;
#pragma unroll
            for (int gq = 0; gq < 4; ++gq) {
                f32x4 v = {acc[mi][ni][4 * gq], acc[mi][ni][4 * gq + 1], acc[mi][ni][4 * gq + 2], acc[mi][ni][4 * gq + 3]};
                asm volatile("global_store_dwordx4 %0, %1, off sc1\n\ts_nop 1" :: "v"(dst + gq * 1024), "v"(v) : "memory");
            }
        }
    asm volatile("s_waitcnt vmcnt(0)" ::: "memory");
    BARRIER();
    unsigned* cnt = (unsigned*)(ws + WS_CNT) + (mt * 5 + nt) * 64;
    if (wid == 0) {
        if (lane == 0) __hip_atomic_fetch_add(cnt, 1u, __ATOMIC_RELAXED, __HIP_MEMORY_SCOPE_AGENT);
        for (unsigned spins = 0; spins < (1u << 24); ++spins) {
            const unsigned c = __builtin_amdgcn_readfirstlane(__hip_atomic_load(cnt, __ATOMIC_RELAXED, __HIP_MEMORY_SCOPE_AGENT));
            if (c >= (unsigned)NSPLIT) break;
            __builtin_amdgcn_s_sleep(1);
        }
    }
    BARRIER();
    const auto rsrc = __builtin_amdgcn_make_buffer_rsrc((void*)(ws + WS_PART), 0, (int)(NSPLIT * SLAB), 0x00020000);
#pragma unroll
    for (int mi = 0; mi < 2; ++mi)
#pragma unroll
        for (int ni = 0; ni < 2; ++ni) {
            const int st = (wm * 2 + mi) * 4 + wn * 2 + ni;
            if (st % 5 != sp) continue;
            const int mt32 = mt * 8 + wm * 2 + mi, nt32 = nt * 4 + wn * 2 + ni;
            const int off = (mt32 * 20 + nt32) * 4096 + lane * 16;
            u32x4 v[4][4];
#pragma unroll
            for (int s = 0; s < 4; ++s) {
                const int so = (s < sp ? s : s + 1) * (int)SLAB;
#pragma unroll
                for (int gq = 0; gq < 4; ++gq) v[s][gq] = __builtin_amdgcn_raw_buffer_load_b128(rsrc, off + gq * 1024, so, 16);
            }
            char* op = (char*)out + (size_t)(mt32 * 32 + r) * 2560 + (640 + nt32 * 32 + 4 * h) * 2;
#pragma unroll
            for (int gq = 0; gq < 4; ++gq) {
                f32x4 t = {acc[mi][ni][4 * gq], acc[mi][ni][4 * gq + 1], acc[mi][ni][4 * gq + 2], acc[mi][ni][4 * gq + 3]};
                t += (__builtin_bit_cast(f32x4, v[0][gq]) + __builtin_bit_cast(f32x4, v[1][gq])) +
                     (__builtin_bit_cast(f32x4, v[2][gq]) + __builtin_bit_cast(f32x4, v[3][gq]));
                u32x2 o;
                o[0] = pack2(t[0], t[1]);
                o[1] = pack2(t[2], t[3]);
                *(u32x2*)(op + gq * 16) = o;
            }
        }
}

extern "C" void kernel_launch(void* const* d_in, const int* in_sizes, int n_in,
                              void* d_out, int out_size, void* d_ws, size_t ws_size,
                              hipStream_t stream) {
    (void)in_sizes; (void)n_in; (void)out_size; (void)ws_size;
    const __hip_bfloat16* Ax = (const __hip_bfloat16*)d_in[0];
    const __hip_bfloat16* Awqkv = (const __hip_bfloat16*)d_in[1];
    const __hip_bfloat16* Awo = (const __hip_bfloat16*)d_in[2];
    const __hip_bfloat16* Bx = (const __hip_bfloat16*)d_in[3];
    const __hip_bfloat16* Bckv = (const __hip_bfloat16*)d_in[4];
    const __hip_bfloat16* Bwq = (const __hip_bfloat16*)d_in[5];
    const __hip_bfloat16* Bwuk = (const __hip_bfloat16*)d_in[6];
    const __hip_bfloat16* Bwuv = (const __hip_bfloat16*)d_in[7];
    const __hip_bfloat16* Bwo = (const __hip_bfloat16*)d_in[8];
    char* ws = (char*)d_ws;
    (void)hipFuncSetAttribute((const void*)k1_proj, hipFuncAttributeMaxDynamicSharedMemorySize, G1_LDS);
    (void)hipFuncSetAttribute((const void*)k2_attn, hipFuncAttributeMaxDynamicSharedMemorySize, K2_LDS);
    (void)hipFuncSetAttribute((const void*)k3_oproj, hipFuncAttributeMaxDynamicSharedMemorySize, G3_LDS);
    k1_proj<<<256, 512, G1_LDS, stream>>>(Ax, Awqkv, Bx, Bckv, Bwq, Bwuk, Bwuv, Awo, Bwo, ws);
    k2_attn<<<256, 512, K2_LDS, stream>>>(ws);
    k3_oproj<<<240, 512, G3_LDS, stream>>>(Awo, Bwo, ws, (__hip_bfloat16*)d_out);
}
```

```cpp
#include <hip/hip_runtime.h>
#include <hip/hip_bf16.h>
#include <stdint.h>
#include <type_traits>

typedef short s16x4 __attribute__((ext_vector_type(4)));
typedef short bf16x8 __attribute__((ext_vector_type(8)));
typedef float f32x16 __attribute__((ext_vector_type(16)));
typedef float f32x4 __attribute__((ext_vector_type(4)));
typedef float f32x2 __attribute__((ext_vector_type(2)));
typedef unsigned u32x4 __attribute__((ext_vector_type(4)));
typedef unsigned u32x2 __attribute__((ext_vector_type(2)));
typedef __bf16 bf16x2_t __attribute__((ext_vector_type(2)));
#define LAS __attribute__((address_space(3)))
#define DI __device__ __forceinline__
#define MFMA32(a, b, c) __builtin_amdgcn_mfma_f32_32x32x16_bf16((a), (b), (c), 0, 0, 0)
#define MFMA16(a, b, c) __builtin_amdgcn_mfma_f32_16x16x32_bf16((a), (b), (c), 0, 0, 0)
#define CFENCE() asm volatile("" ::: "memory")
#define BARRIER() do { CFENCE(); __builtin_amdgcn_s_barrier(); CFENCE(); } while (0)
#define WAIT_VM(n) asm volatile("s_waitcnt vmcnt(" #n ") lgkmcnt(0)" ::: "memory")
#define SBAR() __builtin_amdgcn_sched_barrier(0)

DI unsigned pack2(float a, float b) {
    f32x2 v = {a, b};
    bf16x2_t r = __builtin_convertvector(v, bf16x2_t);
    return __builtin_bit_cast(unsigned, r);
}
DI u32x4 pack8(const f32x16& x, int s) {
    u32x4 w;
    w[0] = pack2(x[8 * s + 0], x[8 * s + 1]);
    w[1] = pack2(x[8 * s + 2], x[8 * s + 3]);
    w[2] = pack2(x[8 * s + 4], x[8 * s + 5]);
    w[3] = pack2(x[8 * s + 6], x[8 * s + 7]);
    return w;
}
DI u32x4 pack8s(const f32x16& x, int s, float c) {
    u32x4 w;
    w[0] = pack2(x[8 * s + 0] * c, x[8 * s + 1] * c);
    w[1] = pack2(x[8 * s + 2] * c, x[8 * s + 3] * c);
    w[2] = pack2(x[8 * s + 4] * c, x[8 * s + 5] * c);
    w[3] = pack2(x[8 * s + 6] * c, x[8 * s + 7] * c);
    return w;
}
DI bf16x8 tr_frag(LAS char* p0, LAS char* p1) {
    s16x4 lo = __builtin_amdgcn_ds_read_tr16_b64_v4i16((LAS s16x4*)p0);
    s16x4 hi = __builtin_amdgcn_ds_read_tr16_b64_v4i16((LAS s16x4*)p1);
    return __builtin_shufflevector(lo, hi, 0, 1, 2, 3, 4, 5, 6, 7);
}

DI void st16(void* p, u32x4 v) {
    asm volatile("global_store_dwordx4 %0, %1, off sc1\n\ts_nop 1" :: "v"(p), "v"(v) : "memory");
}
template <int I, int N, class F> DI void sfor(F&& f) {
    if constexpr (I < N) { f(std::integral_constant<int, I>{}); sfor<I + 1, N>(f); }
}
template <int OFF> DI void lds_rd(bf16x8& d, unsigned addr) {
    asm volatile("ds_read_b128 %0, %1 offset:%2" : "=v"(d) : "v"(addr), "i"(OFF));
}
template <int N> DI void lds_wait(bf16x8& d) { asm volatile("s_waitcnt lgkmcnt(%1)" : "+v"(d) : "i"(N)); }
template <int N> DI void lds_wait2(bf16x8& d, bf16x8& e) { asm volatile("s_waitcnt lgkmcnt(%2)" : "+v"(d), "+v"(e) : "i"(N)); }
#define LGKM0() __builtin_amdgcn_s_waitcnt(0xC07F)
DI void set_m0(unsigned v) { asm volatile("s_mov_b32 m0, %0\n\ts_nop 0" :: "s"(v) : "memory"); }
template <int OFF> DI void dma_s(unsigned voff, const char* sbase) {
    asm volatile("global_load_lds_dwordx4 %0, %1 offset:%2" :: "v"(voff), "s"(sbase), "i"(OFF) : "memory");
}

constexpr size_t WS_KVB = 0;
constexpr size_t WS_QB = WS_KVB + (size_t)32 * 1048576;
constexpr size_t WS_KVA = WS_QB + (size_t)16 * 1048576;
constexpr size_t WS_QA = WS_KVA + (size_t)8 * 1048576;
constexpr size_t WS_CTXB = WS_QA + (size_t)4 * 1048576;
constexpr size_t WS_CTXA = WS_CTXB + (size_t)16 * 1048576;
constexpr size_t WS_PART = WS_CTXA + (size_t)4 * 1048576;
constexpr size_t WS_CNT = WS_PART + (size_t)32 * 1048576;
constexpr int NSPLIT = 5;
constexpr size_t SLAB = (size_t)64 * 20 * 4096;

constexpr float CB = 0.08838834764831845f * 1.4426950408889634f;
constexpr float CA = 0.22360679774997896f * 1.4426950408889634f;

constexpr bool PREFETCH = true;
constexpr int G1_LDS = 2 * 65536 + 2048;
enum { EP_BK = 0, EP_BV = 1, EP_BQ = 2, EP_AQK = 3, EP_AV = 4 };

struct G1Src { unsigned xoff[4], woff[4]; const char* Xb; const char* Wb; unsigned wstep; int nkt; bool half; };
DI void g1_src(G1Src& s, const __hip_bfloat16* X, const __hip_bfloat16* W, int K, int ldw, int m0, int n0, int nlim, int wid, int lane, bool half) {
    s.half = half;
#pragma unroll
    for (int i = 0; i < 4; ++i) {
        const int row = (wid * 4 + i) * 8 + (lane >> 3);
        const int c = (lane & 7) ^ ((row >> 1) & 7);
        s.xoff[i] = (unsigned)(((m0 + row) * K + c * 8) * 2) + 4096 - i * 1024;
        const int k = (wid * 4 + i) * 2 + (lane >> 5);
        const int p = lane & 31, b = (p >> 2) ^ (k & 3);
        int col = n0 + (b * 4 + ((p & 3) ^ ((wid & 1) << 1))) * 8;
        col = col < nlim ? col : nlim - 8;
        s.woff[i] = (unsigned)((k * ldw + col) * 2) - i * 1024;
    }
    s.Xb = (const char*)X; s.Wb = (const char*)W; s.wstep = (unsigned)(64 * ldw * 2); s.nkt = K >> 6;
}
template <int I> DI void g1_piece(const G1Src& s, int kt) {
    if constexpr (I < 4) dma_s<I * 1024 - 4096>(s.xoff[I], s.Xb + (size_t)kt * 128);
    else if (!s.half || (threadIdx.x & 16) == 0) dma_s<(I - 4) * 1024>(s.woff[I - 4], s.Wb + (size_t)kt * s.wstep);
}

template <bool TR, int NT>
DI void g1_loop(f32x4 (&acc)[8][4], bf16x8 (&xs)[2][4], bf16x8 (&wsb)[2][4], const G1Src& cur, const G1Src& nxt, bool has_next, bool first,
                LAS char* lds, int wid, int lane) {
    const int wm = wid >> 2, wn = wid & 3, r16 = lane & 15, g = lane >> 4;
    const unsigned lds0 = (unsigned)(uintptr_t)lds;
    const int nkt = cur.nkt;
    int xb[2];
#pragma unroll
    for (int st = 0; st < 2; ++st) xb[st] = wm * 4 * 8192 + r16 * 128 + (((4 * st + g) ^ (r16 >> 1)) * 16);
    const int q = (lane >> 2) & 3, p4 = lane & 3;
    int wb[4];
#pragma unroll
    for (int nt = 0; nt < 4; ++nt) {
        const int b = NT == 4 ? wn * 2 + (nt >> 1) : wn;
        wb[nt] = g * 8192 + 4096 + q * 512 + ((b ^ q) * 64) + (((nt & 1) ^ (g & 1)) * 32) + p4 * 8;
    }
#pragma unroll
    for (int mt = 0; mt < 8; ++mt)
#pragma unroll
        for (int nt = 0; nt < 4; ++nt) acc[mt][nt] = f32x4{};
#define G1_RX(set, bb, st, mh)                                                                                   \
    _Pragma("unroll") for (int m4 = 0; m4 < 4; ++m4)                                                             \
        xs[set][m4] = *(LAS bf16x8*)((bb) + xb[st] + ((mh) * 2 + (m4 >> 1)) * 8192 + (m4 & 1) * 2048)
#define G1_RW(bb, st)                                                                                            \
    _Pragma("unroll") for (int nt = 0; nt < NTR; ++nt)                                                           \
        wsb[st][nt] = tr_frag((bb) + wb[nt] + (st) * 32768, (bb) + wb[nt] + (st) * 32768 + 2048)
#define G1_MM(set, st, mh, on, kn, p0, pe)                                                                       \
    sfor<0, 4>([&](auto mc) {                                                                                    \
        constexpr int m4 = decltype(mc)::value;                                                                  \
        _Pragma("unroll") for (int nt = 0; nt < NT; ++nt)                                                        \
            acc[(mh) * 4 + m4][nt] = TR ? MFMA16(wsb[st][nt], xs[set][m4], acc[(mh) * 4 + m4][nt])               \
                                        : MFMA16(xs[set][m4], wsb[st][nt], acc[(mh) * 4 + m4][nt]);              \
        if constexpr ((pe) > (p0)) {                                                                             \
            SBAR();                                                                                              \
            if (on) {                                                                                            \
                if ((kn) < nkt) { g1_piece<2 * m4>(cur, (kn)); g1_piece<2 * m4 + 1>(cur, (kn)); }                \
                else { g1_piece<2 * m4>(nxt, (kn) - nkt); g1_piece<2 * m4 + 1>(nxt, (kn) - nkt); }               \
            }                                                                                                    \
            SBAR();                                                                                              \
        }                                                                                                        \
    })
    constexpr int NTR = NT;
    G1_RW(lds, 0);
    if (first) { G1_RX(0, lds, 0, 0); }
#pragma unroll 1
    for (int kt = 0; kt < nkt; ++kt) {
        LAS char* bb = lds + (kt & 1) * 65536;
        LAS char* bn = lds + ((kt + 1) & 1) * 65536;
        const bool on2 = kt + 2 < nkt || has_next;
        LGKM0(); G1_RX(1, bb, 0, 1); SBAR(); G1_MM(0, 0, 0, false, 0, 0, 0); SBAR();
        LGKM0(); G1_RW(bb, 1); G1_RX(0, bb, 1, 0); SBAR(); G1_MM(1, 0, 1, false, 0, 0, 0); SBAR();
        LGKM0(); G1_RX(1, bb, 1, 1); SBAR(); G1_MM(0, 1, 0, false, 0, 0, 0); SBAR();
        LGKM0();
        WAIT_VM(0);
        BARRIER();
        set_m0(lds0 + (kt & 1) * 65536 + wid * 8192 + 4096);
        if (kt + 1 < nkt) G1_RW(bn, 0);
        if (kt + 1 < nkt || has_next) G1_RX(0, bn, 0, 0);
        SBAR(); G1_MM(1, 1, 1, on2, kt + 2, 0, 8); SBAR();
    }
#undef G1_RX
#undef G1_RW
#undef G1_MM
}

DI void st8(void* p, u32x2 v) {
    asm volatile("global_store_dwordx2 %0, %1, off sc1\n\ts_nop 1" :: "v"(p), "v"(v) : "memory");
}

template <int EP>
DI void g1_epi(const f32x4 (&acc)[8][4], int m0, int n0, int nlim, int nsec, char* __restrict__ ws, int wid, int lane) {
    const int wm = wid >> 2, wn = wid & 3, c = lane & 15, g = lane >> 4;
    if constexpr (EP == EP_BK || EP == EP_BQ || EP == EP_BV) {
#pragma unroll
        for (int mt = 0; mt < 8; ++mt) {
            const int tb = (m0 + wm * 128 + mt * 16) >> 5;
            const int tile = tb >> 1, kb = tb & 1, th = mt & 1;
#pragma unroll
            for (int np = 0; np < 2; ++np) {
                const int nb = n0 + wn * 64 + np * 32;
                const int head = nb >> 7;
                const float sc = EP == EP_BQ ? CB : 1.0f;
                const f32x4 a = acc[mt][2 * np], b = acc[mt][2 * np + 1];
                unsigned a0 = pack2(a[0] * sc, a[1] * sc), a1 = pack2(a[2] * sc, a[3] * sc);
                unsigned b0 = pack2(b[0] * sc, b[1] * sc), b1 = pack2(b[2] * sc, b[3] * sc);
                if constexpr (EP == EP_BV) {
                    auto r0 = __builtin_amdgcn_permlane32_swap(a0, b0, false, false);
                    auto r1 = __builtin_amdgcn_permlane32_swap(a1, b1, false, false);
                    const u32x4 w = {r0[0], r1[0], r0[1], r1[1]};
                    const int db = (nb >> 5) & 3;
                    char* base = ws + WS_KVB + (size_t)head * 1048576 + tile * 32768 + 16384 + ((kb * 2 + th) * 4 + db) * 1024;
                    st16(base + ((g >> 1) * 16 + c + 32 * (g & 1)) * 16, w);
                } else {
                    auto r0 = __builtin_amdgcn_permlane16_swap(a0, b0, false, false);
                    auto r1 = __builtin_amdgcn_permlane16_swap(a1, b1, false, false);
                    const u32x4 w = {r0[0], r1[0], r0[1], r1[1]};
                    const int ks = ((nb >> 4) & 7) + (g & 1);
                    char* base = EP == EP_BK ? ws + WS_KVB + (size_t)head * 1048576 + tile * 32768 + (kb * 8 + ks) * 1024
                                             : ws + WS_QB + (size_t)((head * 64 + tb) * 8 + ks) * 1024;
                    st16(base + (th * 16 + c + 32 * (g >> 1)) * 16, w);
                }
            }
        }
        return;
    }
#pragma unroll
    for (int mt = 0; mt < 8; ++mt) {
        const int tb = (m0 + wm * 128 + mt * 16) >> 5;
        const int tile = tb >> 1, kb = tb & 1, th = mt & 1;
#pragma unroll
        for (int nt = 0; nt < 4; ++nt) {
            const int nb = n0 + wn * 64 + nt * 16;
            if (nb >= nlim) continue;
            const f32x4 a = acc[mt][nt];
            if constexpr (EP == EP_BK || EP == EP_BQ || EP == EP_BV) {
            } else if constexpr (EP == EP_AQK) {
                const bool isq = nb < 640;
                const float sc = isq ? CA : 1.0f;
                const int r = th * 16 + c;
                const int n = nb - (isq ? 0 : 640) + 4 * g;
                const int head = n / 20, d0 = n - head * 20;
                char* base = isq ? ws + WS_QA + (size_t)((head * 64 + tb) * 2) * 1024
                                 : ws + WS_KVA + (size_t)head * 262144 + tile * 8192 + kb * 2048;
                char* dst = base + (d0 >> 4) * 1024 + (r + 32 * ((d0 >> 3) & 1)) * 16 + (d0 & 4) * 2;
                u32x2 w;
                w[0] = pack2(a[0] * sc, a[1] * sc);
                w[1] = pack2(a[2] * sc, a[3] * sc);
                *(u32x2*)dst = w;
                if (d0 == 16) {
                    *(u32x2*)(dst + 8) = (u32x2){isq ? 0u : 0x3F803F80u, 0u};
                    *(u32x4*)(base + 1024 + (r + 32) * 16) = (u32x4){0u, 0u, 0u, 0u};
                }
            } else {
                const int n = nb - nsec + c;
                const int head = n / 20, d = n - head * 20;
                char* dst = ws + WS_KVA + (size_t)head * 262144 + tile * 8192 + 4096 + (kb * 2 + th) * 1024 + (d + 32 * (g & 1)) * 16 + (g >> 1) * 8;
                u32x2 w;
                w[0] = pack2(a[0], a[1]);
                w[1] = pack2(a[2], a[3]);
                *(u32x2*)dst = w;
                if (d == 19) *(u32x2*)(dst + 16) = (u32x2){0x3F803F80u, 0x3F803F80u};
            }
        }
    }
}

__global__ __launch_bounds__(512, 2) void k1_proj(const __hip_bfloat16* __restrict__ Ax, const __hip_bfloat16* __restrict__ Awqkv,
                                                  const __hip_bfloat16* __restrict__ Bx, const __hip_bfloat16* __restrict__ Bckv,
                                                  const __hip_bfloat16* __restrict__ Bwq, const __hip_bfloat16* __restrict__ Bwuk,
                                                  const __hip_bfloat16* __restrict__ Bwuv, const __hip_bfloat16* __restrict__ Awo,
                                                  const __hip_bfloat16* __restrict__ Bwo, char* __restrict__ ws) {
    extern __shared__ __attribute__((aligned(16))) char smem[];
    LAS char* lds = (LAS char*)smem;
    const int tid = threadIdx.x, lane = tid & 63, wid = __builtin_amdgcn_readfirstlane(tid >> 6);
    const int bid = blockIdx.x;
    const int m0 = ((bid >> 3) & 7) * 256;
    const int grp = bid >> 6, c8 = bid & 7;
    const int cc = c8 + 8 * (grp & 1);
    const bool has2 = grp < 3;
    G1Src s1, s2;
    if (grp < 2) {
        g1_src(s1, Bckv, Bwuk, 512, 4096, m0, cc * 256, 4096, wid, lane, false);
        g1_src(s2, Bx, Bwq, 640, 4096, m0, cc * 256, 4096, wid, lane, false);
    } else if (grp == 2) {
        g1_src(s1, Bckv, Bwuv, 512, 4096, m0, c8 * 256, 4096, wid, lane, false);
        g1_src(s2, Bckv, Bwuv, 512, 4096, m0, (c8 + 8) * 256, 4096, wid, lane, false);
    } else {
        g1_src(s1, Ax, Awqkv, 640, 1920, m0, c8 * 256, c8 < 5 ? 1280 : 1920, wid, lane, false);
        s2 = s1;
    }

    f32x4 acc[8][4];
    bf16x8 xf[2][4], wf[2][4];
    const unsigned lds0 = (unsigned)(uintptr_t)lds;
    if (bid == 255 && tid < 40) ((unsigned*)(ws + WS_CNT))[tid * 64] = 0u;
    set_m0(lds0 + wid * 8192 + 4096);
    sfor<0, 8>([&](auto ic) { g1_piece<decltype(ic)::value>(s1, 0); });
    set_m0(lds0 + 65536 + wid * 8192 + 4096);
    sfor<0, 8>([&](auto ic) { g1_piece<decltype(ic)::value>(s1, 1); });
    WAIT_VM(8);
    BARRIER();
    if (grp == 3) {
        set_m0(lds0 + 131072 + wid * 256);
        const unsigned gw = (unsigned)((bid - 192) * 8 + wid) * 64 + lane;
#define PF(ptr, nbytes) do { _Pragma("unroll") for (unsigned o_ = 0; o_ < (nbytes); o_ += 32768u * 128u) {              \
            unsigned off_ = o_ + gw * 128u; off_ = off_ < (nbytes) ? off_ : 0u;                                           \
            asm volatile("global_load_lds_dword %0, %1" :: "v"(off_), "s"((const char*)(ptr)) : "memory"); } } while (0)
        PF(Bx, 2048u * 640u * 2u); PF(Bwq, 640u * 4096u * 2u); PF(Bwuv, 512u * 4096u * 2u);
        PF(Bwo, 4096u * 640u * 2u); PF(Awo, 640u * 640u * 2u);
#undef PF
    }
    if (grp < 2 || (grp == 3 && c8 < 5)) {
        g1_loop<true, 4>(acc, xf, wf, s1, s2, has2, true, lds, wid, lane);
        if (grp < 2) g1_epi<EP_BK>(acc, m0, cc * 256, 4096, 0, ws, wid, lane);
        else g1_epi<EP_AQK>(acc, m0, c8 * 256, 1280, 0, ws, wid, lane);
    } else {
        g1_loop<false, 4>(acc, xf, wf, s1, s2, has2, true, lds, wid, lane);
        if (grp == 2) g1_epi<EP_BV>(acc, m0, c8 * 256, 4096, 0, ws, wid, lane);
        else g1_epi<EP_AV>(acc, m0, c8 * 256, 1920, 1280, ws, wid, lane);
    }
    if (grp < 2) {
        g1_loop<true, 4>(acc, xf, wf, s2, s2, false, false, lds, wid, lane);
        g1_epi<EP_BQ>(acc, m0, cc * 256, 4096, 0, ws, wid, lane);
    } else if (grp == 2) {
        g1_loop<false, 4>(acc, xf, wf, s2, s2, false, false, lds, wid, lane);
        g1_epi<EP_BV>(acc, m0, (c8 + 8) * 256, 4096, 0, ws, wid, lane);
    }
}

constexpr int SLOT = 40960;
constexpr int NSLOT = 4;
constexpr int K2_LDS = NSLOT * SLOT;
constexpr int NIT = 34;

__global__ __launch_bounds__(512, 2) void k2_attn(char* __restrict__ ws) {
    extern __shared__ __attribute__((aligned(16))) char smem[];
    LAS char* lds = (LAS char*)smem;
    const int tid = threadIdx.x, lane = tid & 63, wid = __builtin_amdgcn_readfirstlane(tid >> 6);
    const int r = lane & 31, h = lane >> 5;
    const int g = blockIdx.x, xcd = g & 7, idx = g >> 3;
    const int head = xcd * 4 + (idx >> 3), x = idx & 7;
    const char* kvb = ws + WS_KVB + (size_t)head * 1048576;
    const char* kva = ws + WS_KVA + (size_t)head * 262144;
    const int n1 = 32 - 2 * x;
    const unsigned lbase = (unsigned)(uintptr_t)lds + lane * 16;

    const unsigned lds0 = (unsigned)(uintptr_t)lds;
    const unsigned voff = lane * 16;
    const int wa = wid & 3;
    const char* sbB = kvb;
    const char* sbA = kva;
#define K2_M0(tt)                                                                           \
    do {                                                                                    \
        const int t3_ = (tt) < NIT ? (tt) : NIT - 1;                                        \
        const int tb3_ = t3_ < n1 ? t3_ : t3_ - n1;                                         \
        const int ta3_ = t3_ < 32 ? t3_ : 31;                                               \
        sbB = kvb + (size_t)tb3_ * 32768 + wid * 4096 + 2048;                               \
        sbA = kva + (size_t)ta3_ * 8192 + wid * 1024 - 2048;                                \
        set_m0(lds0 + ((tt) < NIT ? ((tt) & 3) : 2 + ((tt) & 1)) * SLOT + wid * 5120 + 2048); \
    } while (0)
#define K2_DMA(i) do { SBAR(); if constexpr ((i) < 4) dma_s<(i) * 1024 - 2048>(voff, sbB); else dma_s<2048>(voff, sbA); SBAR(); } while (0)
#define K2_STAGE_ALL(tt) do { K2_M0(tt); K2_DMA(0); K2_DMA(1); K2_DMA(2); K2_DMA(3); K2_DMA(4); } while (0)
#define BOFF(f) (((f) >> 2) * 5120 + ((f) & 3) * 1024)
#define AOFF(a) ((a) * 5120 + 4096)
    K2_STAGE_ALL(0);
    K2_STAGE_ALL(1);

#define K2_SYNC(it) do { WAIT_VM(5); BARRIER(); K2_M0((it) + 3); } while (0)

    if (wid < 4) {
        bf16x8 fr[8];
        K2_SYNC(-1);
        sfor<0, 8>([&](auto ic) { constexpr int j = decltype(ic)::value; lds_rd<BOFF((j & 1) * 8 + (j >> 1))>(fr[j], lbase); });
        K2_DMA(0); K2_DMA(1);
        int it = 0;
        for (int blk = 0; blk < 2; ++blk) {
            const int qb = blk == 0 ? 15 - x : x;
            const int nt = 2 * qb + 2;
            const int q0 = qb * 128 + wid * 32;
            const int tb32 = q0 >> 5;
            bf16x8 qf[8];
            const char* qp = ws + WS_QB + (size_t)((head * 64 + tb32) * 8) * 1024 + lane * 16;
#pragma unroll
            for (int ks = 0; ks < 8; ++ks) qf[ks] = *(const bf16x8*)(qp + ks * 1024);
            f32x16 o[4];
#pragma unroll
            for (int db = 0; db < 4; ++db) o[db] = f32x16{};
            f32x16 negm = f32x16{};
            float l = 0.f;
            for (int t = 0; t < nt; ++t, ++it) {
                const int rel = (q0 - 64 * t) >> 5;
                const unsigned ac = lbase + (it & 3) * SLOT;
                const unsigned an = lbase + ((it + 1) & 3) * SLOT;
                f32x16 s0, s1;
                sfor<0, 16>([&](auto ic) {
                    constexpr int j = decltype(ic)::value, ks = j >> 1;
                    lds_wait<7>(fr[j & 7]);
                    if constexpr (j == 0) s0 = MFMA32(fr[0], qf[0], negm);
                    else if constexpr (j == 1) s1 = MFMA32(fr[1], qf[0], negm);
                    else if constexpr ((j & 1) == 0) s0 = MFMA32(fr[j & 7], qf[ks], s0);
                    else s1 = MFMA32(fr[j & 7], qf[ks], s1);
                    constexpr int jn = j + 8;
                    SBAR();
                    if constexpr (jn < 16) lds_rd<BOFF((jn & 1) * 8 + (jn >> 1))>(fr[j & 7], ac);
                    else lds_rd<BOFF(jn)>(fr[j & 7], ac);
                    if constexpr (j == 2) K2_DMA(2);
                    if constexpr (j == 8) K2_DMA(3);
                    if constexpr (j == 14) K2_DMA(4);
                    SBAR();
                });
#define B_MASK()                                                                                        \
    do {                                                                                                \
        if (rel <= 1) {                                                                                 \
            const int lim = 32 * rel + r - 4 * h;                                                       \
            _Pragma("unroll") for (int i = 0; i < 16; ++i) {                                            \
                const int kr = (i & 3) + 8 * (i >> 2);                                                  \
                if (kr > lim) s0[i] = -__builtin_inff();                                                \
                if (kr + 32 > lim) s1[i] = -__builtin_inff();                                           \
            }                                                                                           \
        }                                                                                               \
    } while (0)
                B_MASK();
                float ps = 0.f;
                if (t > 0) {
#pragma unroll
                    for (int i = 0; i < 16; ++i) { s0[i] = __builtin_amdgcn_exp2f(s0[i]); ps += s0[i]; }
#pragma unroll
                    for (int i = 0; i < 16; ++i) { s1[i] = __builtin_amdgcn_exp2f(s1[i]); ps += s1[i]; }
                }
                if (t == 0 || __any(!(ps <= 65536.f))) {
                    if (t > 0) {
                        LAS char* sl = lds + (it & 3) * SLOT + lane * 16;
                        s0 = negm; s1 = negm;
#pragma unroll 1
                        for (int ks = 0; ks < 8; ++ks) {
                            bf16x8 qq = qf[0];
#pragma unroll
                            for (int u = 1; u < 8; ++u) qq = (ks == u) ? qf[u] : qq;
                            s0 = MFMA32(*(LAS bf16x8*)(sl + (ks >> 2) * 5120 + (ks & 3) * 1024), qq, s0);
                            s1 = MFMA32(*(LAS bf16x8*)(sl + 10240 + (ks >> 2) * 5120 + (ks & 3) * 1024), qq, s1);
                        }
                        B_MASK();
                    }
                    float mx = s0[0];
#pragma unroll
                    for (int i = 1; i < 16; ++i) mx = fmaxf(mx, s0[i]);
#pragma unroll
                    for (int i = 0; i < 16; ++i) mx = fmaxf(mx, s1[i]);
                    auto rr = __builtin_amdgcn_permlane32_swap(__float_as_uint(mx), __float_as_uint(mx), false, false);
                    mx = fmaxf(__uint_as_float(rr[0]), __uint_as_float(rr[1]));
                    float delta = mx;
                    if (t > 0) {
                        delta = fmaxf(mx, 0.f);
                        const float alpha = __builtin_amdgcn_exp2f(-delta);
                        l *= alpha;
#pragma unroll
                        for (int db = 0; db < 4; ++db)
#pragma unroll
                            for (int i = 0; i < 16; ++i) o[db][i] *= alpha;
                    }
                    const float nm = negm[0] - delta;
#pragma unroll
                    for (int i = 0; i < 16; ++i) negm[i] = nm;
                    ps = 0.f;
#pragma unroll
                    for (int i = 0; i < 16; ++i) { s0[i] = __builtin_amdgcn_exp2f(s0[i] - delta); ps += s0[i]; }
#pragma unroll
                    for (int i = 0; i < 16; ++i) { s1[i] = __builtin_amdgcn_exp2f(s1[i] - delta); ps += s1[i]; }
                }
                l += ps;
                bf16x8 pf[4];
                pf[0] = __builtin_bit_cast(bf16x8, pack8(s0, 0));
                pf[1] = __builtin_bit_cast(bf16x8, pack8(s0, 1));
                pf[2] = __builtin_bit_cast(bf16x8, pack8(s1, 0));
                pf[3] = __builtin_bit_cast(bf16x8, pack8(s1, 1));
                if (it + 1 < NIT) { K2_SYNC(it); } else { K2_M0(it + 3); }
                sfor<0, 16>([&](auto ic) {
                    constexpr int j = decltype(ic)::value, s4 = j >> 2, db = j & 3;
                    lds_wait<7>(fr[j & 7]);
                    o[db] = MFMA32(fr[j & 7], pf[s4], o[db]);
                    SBAR();
                    if constexpr (j < 8) lds_rd<BOFF(24 + j)>(fr[j & 7], ac);
                    else { constexpr int jn = j - 8; lds_rd<BOFF((jn & 1) * 8 + (jn >> 1))>(fr[j & 7], an); }
                    if constexpr (j == 4) K2_DMA(0);
                    if constexpr (j == 10) K2_DMA(1);
                    SBAR();
                });
            }
            auto rr = __builtin_amdgcn_permlane32_swap(__float_as_uint(l), __float_as_uint(l), false, false);
            const float inv = 1.0f / (__uint_as_float(rr[0]) + __uint_as_float(rr[1]));
            char* cp = ws + WS_CTXB + (size_t)(tb32 * 256 + head * 8) * 1024 + lane * 16;
#pragma unroll
            for (int db = 0; db < 4; ++db) {
                st16(cp + (2 * db) * 1024, pack8s(o[db], 0, inv));
                st16(cp + (2 * db + 1) * 1024, pack8s(o[db], 1, inv));
            }
        }
        WAIT_VM(0);
#undef B_MASK
    } else {
        const int tb32 = x * 8 + wa * 2;
        bf16x8 qf[2][2];
        const char* qp = ws + WS_QA + (size_t)((head * 64 + tb32) * 2) * 1024 + lane * 16;
#pragma unroll
        for (int qt = 0; qt < 2; ++qt)
#pragma unroll
            for (int ks = 0; ks < 2; ++ks) qf[qt][ks] = *(const bf16x8*)(qp + (qt * 2 + ks) * 1024);
        f32x16 o[2];
        o[0] = f32x16{}; o[1] = f32x16{};
        float negm[2] = {0.f, 0.f};
        bf16x8 kf[4], vf[4];
        f32x16 s[2][2];
        K2_SYNC(-1);
        lds_rd<AOFF(0)>(kf[0], lbase); lds_rd<AOFF(1)>(kf[1], lbase); lds_rd<AOFF(2)>(kf[2], lbase); lds_rd<AOFF(3)>(kf[3], lbase);
        for (int it = 0; it <= 32; ++it) {
            if (it > 0) {
                u32x4 pf[2][2][2];
                unsigned chk = 0;
                sfor<0, 4>([&](auto bc) {
                    constexpr int blk = decltype(bc)::value, kb = blk >> 1, qt = blk & 1;
                    f32x16 e;
#pragma unroll
                    for (int i = 0; i < 8; ++i) e[i] = __builtin_amdgcn_exp2f(s[kb][qt][i]);
                    K2_DMA(blk);
#pragma unroll
                    for (int i = 8; i < 16; ++i) e[i] = __builtin_amdgcn_exp2f(s[kb][qt][i]);
                    if constexpr (blk == 3) K2_DMA(4);
                    pf[kb][qt][0] = pack8(e, 0);
                    pf[kb][qt][1] = pack8(e, 1);
                    chk |= pf[kb][qt][0][0] | pf[kb][qt][0][1] | pf[kb][qt][0][2] | pf[kb][qt][0][3];
                    chk |= pf[kb][qt][1][0] | pf[kb][qt][1][1] | pf[kb][qt][1][2] | pf[kb][qt][1][3];
                });
                const bool first = (it == 1);
                if (first || __any((chk & 0x40004000u) != 0u)) {
#pragma unroll
                    for (int qt = 0; qt < 2; ++qt) {
                        float mx = s[0][qt][0];
#pragma unroll
                        for (int i = 1; i < 16; ++i) mx = fmaxf(mx, s[0][qt][i]);
#pragma unroll
                        for (int i = 0; i < 16; ++i) mx = fmaxf(mx, s[1][qt][i]);
                        auto rr = __builtin_amdgcn_permlane32_swap(__float_as_uint(mx), __float_as_uint(mx), false, false);
                        mx = fmaxf(__uint_as_float(rr[0]), __uint_as_float(rr[1]));
                        float delta = mx + 10.f;
                        if (!first) delta = fmaxf(delta, 0.f);
                        const float tgt = negm[qt] - delta;
                        const unsigned hi = pack2(tgt, 0.f) & 0xFFFFu;
                        const float hif = __uint_as_float(hi << 16);
                        const unsigned lo = pack2(tgt - hif, 0.f) & 0xFFFFu;
                        const float neff = hif + __uint_as_float(lo << 16);
                        delta = negm[qt] - neff;
                        negm[qt] = neff;
                        {
                            u32x4 qq = __builtin_bit_cast(u32x4, qf[qt][1]);
                            qq[2] = h ? 0u : (hi | (lo << 16));
                            qf[qt][1] = __builtin_bit_cast(bf16x8, qq);
                        }
                        if (!first) {
                            const float alpha = __builtin_amdgcn_exp2f(-delta);
#pragma unroll
                            for (int i = 0; i < 16; ++i) o[qt][i] *= alpha;
                        }
#pragma unroll
                        for (int kb = 0; kb < 2; ++kb) {
                            f32x16 e;
#pragma unroll
                            for (int i = 0; i < 16; ++i) e[i] = __builtin_amdgcn_exp2f(s[kb][qt][i] - delta);
                            pf[kb][qt][0] = pack8(e, 0);
                            pf[kb][qt][1] = pack8(e, 1);
                        }
                    }
                }
                lds_wait2<4>(vf[0], vf[1]);
                lds_wait2<4>(vf[2], vf[3]);
#pragma unroll
                for (int kb = 0; kb < 2; ++kb)
#pragma unroll
                    for (int s2 = 0; s2 < 2; ++s2)
#pragma unroll
                        for (int qt = 0; qt < 2; ++qt)
                            o[qt] = MFMA32(vf[kb * 2 + s2], __builtin_bit_cast(bf16x8, pf[kb][qt][s2]), o[qt]);
            } else {
                K2_DMA(0); K2_DMA(1); K2_DMA(2); K2_DMA(3); K2_DMA(4);
            }
            lds_wait2<0>(kf[0], kf[1]);
            lds_wait2<0>(kf[2], kf[3]);
#pragma unroll
            for (int kb = 0; kb < 2; ++kb)
#pragma unroll
                for (int qt = 0; qt < 2; ++qt) {
                    s[kb][qt] = MFMA32(kf[kb * 2], qf[qt][0], f32x16{});
                    s[kb][qt] = MFMA32(kf[kb * 2 + 1], qf[qt][1], s[kb][qt]);
                }
            SBAR();
            K2_SYNC(it);
            const unsigned ac = lbase + (it & 3) * SLOT;
            const unsigned an = lbase + ((it + 1) & 3) * SLOT;
            lds_rd<AOFF(4)>(vf[0], ac); lds_rd<AOFF(5)>(vf[1], ac); lds_rd<AOFF(6)>(vf[2], ac); lds_rd<AOFF(7)>(vf[3], ac);
            lds_rd<AOFF(0)>(kf[0], an); lds_rd<AOFF(1)>(kf[1], an); lds_rd<AOFF(2)>(kf[2], an); lds_rd<AOFF(3)>(kf[3], an);
        }
        WAIT_VM(0);
#pragma unroll
        for (int qt = 0; qt < 2; ++qt) {
            const unsigned lv = __float_as_uint(o[qt][8]);
            auto rr = __builtin_amdgcn_permlane32_swap(lv, lv, false, false);
            const float lsum = __uint_as_float(rr[1]);
            const float inv = 1.0f / lsum;
            char* cp = ws + WS_CTXA + (size_t)((tb32 + qt) * 40) * 1024;
#pragma unroll
            for (int gq = 0; gq < 3; ++gq) {
                const int d0 = 8 * gq + 4 * h;
                if (d0 < 20) {
                    const int k = head * 20 + d0;
                    u32x2 w;
                    w[0] = pack2(o[qt][4 * gq + 0] * inv, o[qt][4 * gq + 1] * inv);
                    w[1] = pack2(o[qt][4 * gq + 2] * inv, o[qt][4 * gq + 3] * inv);
                    *(u32x2*)(cp + (k >> 4) * 1024 + (r + 32 * ((k >> 3) & 1)) * 16 + (k & 4) * 2) = w;
                }
            }
        }
    }
}

constexpr int G3_STAGE = 49152;
constexpr int G3_LDS = 3 * G3_STAGE;

__global__ __launch_bounds__(512, 2) void k3_oproj(const __hip_bfloat16* __restrict__ Awo, const __hip_bfloat16* __restrict__ Bwo,
                                                   char* __restrict__ ws, __hip_bfloat16* __restrict__ out) {
    extern __shared__ __attribute__((aligned(16))) char smem[];
    LAS char* lds = (LAS char*)smem;
    const int tid = threadIdx.x, lane = tid & 63, wid = __builtin_amdgcn_readfirstlane(tid >> 6);
    const int wm = wid >> 1, wn = wid & 1, h = lane >> 5;
    const int bid = blockIdx.x;
    const bool isB = bid < 200;
    int mt, nt, sp, kt0, nkt, kstot;
    const char* Xc; const char* Wb;
    if (isB) {
        mt = bid & 7; const int rest = bid >> 3; nt = rest % 5; sp = rest / 5;
        kt0 = sp * 13; nkt = sp == 4 ? 12 : 13; kstot = 256;
        Xc = ws + WS_CTXB; Wb = (const char*)Bwo;
    } else {
        const int b2 = bid - 200; mt = b2 & 7; nt = b2 >> 3; sp = 5;
        kt0 = 0; nkt = 10; kstot = 40;
        Xc = ws + WS_CTXA; Wb = (const char*)Awo;
    }
    unsigned woff[2];
#pragma unroll
    for (int i = 0; i < 2; ++i) {
        const int k = (wid * 2 + i) * 4 + (lane >> 4);
        const int p = lane & 15, b = (p >> 2) ^ (k & 3);
        woff[i] = (unsigned)(((kt0 * 64 + k) * 640 + nt * 128 + (b * 4 + (p & 3)) * 8) * 2);
    }
    const unsigned lds0 = (unsigned)(uintptr_t)lds;
    const char* xsb = Xc + (size_t)((mt * 8 + wid) * kstot + kt0 * 4) * 1024 + 4096;
    const unsigned voff = lane * 16;
    woff[1] -= 1024;
    auto piece = [&](int kt, auto ic) {
        constexpr int i = decltype(ic)::value;
        if constexpr (i < 4) dma_s<i * 1024 - 4096>(voff, xsb + (size_t)kt * 4096);
        else dma_s<(i - 4) * 1024>(woff[i - 4], Wb + (size_t)kt * (64u * 640u * 2u));
    };
    const int q = (lane >> 2) & 3, p4 = lane & 3, g16 = (lane >> 4) & 1;
    const int r0 = isB ? 4 * h : 8 * h, r1 = isB ? 8 + 4 * h : 8 * h + 4;
    int wb0[2], wb1[2];
#pragma unroll
    for (int ni = 0; ni < 2; ++ni) {
        const int b = wn * 2 + ni;
        const int c = ((b ^ q) * 64) + g16 * 32 + p4 * 8;
        wb0[ni] = (r0 >> 3) * 6144 + 4096 + ((r0 & 7) + q) * 256 + c;
        wb1[ni] = (r1 >> 3) * 6144 + 4096 + ((r1 & 7) + q) * 256 + c;
    }
    const int xb0 = wm * 2 * 6144 + lane * 16;
    f32x16 acc[2][2];
#pragma unroll
    for (int mi = 0; mi < 2; ++mi)
#pragma unroll
        for (int ni = 0; ni < 2; ++ni) acc[mi][ni] = f32x16{};

    bf16x8 xf[2][4][2], wf[2][4][2];
#define G3_RD(set, bb)                                                                                           \
    do {                                                                                                         \
        _Pragma("unroll") for (int ks = 0; ks < 4; ++ks) {                                                       \
            _Pragma("unroll") for (int ni = 0; ni < 2; ++ni)                                                     \
                wf[set][ks][ni] = tr_frag((bb) + wb0[ni] + ks * 12288, (bb) + wb1[ni] + ks * 12288);             \
            _Pragma("unroll") for (int mi = 0; mi < 2; ++mi)                                                     \
                xf[set][ks][mi] = *(LAS bf16x8*)((bb) + xb0 + mi * 6144 + ks * 1024);                            \
        }                                                                                                        \
    } while (0)
#define G3_STEP(set, kt)                                                                                         \
    do {                                                                                                         \
        if ((kt) + 2 < nkt) { WAIT_VM(6); } else { WAIT_VM(0); }                                                 \
        LGKM0();                                                                                                 \
        BARRIER();                                                                                               \
        const int b0_ = (kt) % 3, b1_ = ((kt) + 1) % 3;                                                          \
        set_m0(lds0 + b0_ * G3_STAGE + wid * 6144 + 4096);                                                       \
        if ((kt) + 1 < nkt) G3_RD((set) ^ 1, lds + b1_ * G3_STAGE);                                              \
        SBAR();                                                                                                  \
        const bool on_ = (kt) + 3 < nkt;                                                                         \
        sfor<0, 8>([&](auto jc) {                                                                                \
            constexpr int j = decltype(jc)::value, ks = j >> 1, mi = j & 1;                                      \
            _Pragma("unroll") for (int ni = 0; ni < 2; ++ni)                                                     \
                acc[mi][ni] = MFMA32(wf[set][ks][ni], xf[set][ks][mi], acc[mi][ni]);                             \
            if constexpr (j < 6) { SBAR(); if (on_) piece((kt) + 3, jc); SBAR(); }                               \
        });                                                                                                      \
        SBAR();                                                                                                  \
    } while (0)
#pragma unroll
    for (int t = 0; t < 3; ++t) {
        set_m0(lds0 + t * G3_STAGE + wid * 6144 + 4096);
        sfor<0, 6>([&](auto ic) { piece(t, ic); });
    }
    WAIT_VM(12);
    BARRIER();
    G3_RD(0, lds);
    int kt = 0;
#pragma unroll 1
    for (; kt + 1 < nkt; kt += 2) { G3_STEP(0, kt); G3_STEP(1, kt + 1); }
    if (kt < nkt) G3_STEP(0, kt);
#undef G3_RD
#undef G3_STEP
    const int r = lane & 31;
    if (!isB) {
#pragma unroll
        for (int mi = 0; mi < 2; ++mi)
#pragma unroll
            for (int ni = 0; ni < 2; ++ni) {
                char* op = (char*)out + (size_t)((mt * 8 + wm * 2 + mi) * 32 + r) * 2560 + ((nt * 4 + wn * 2 + ni) * 32 + 4 * h) * 2;
#pragma unroll
                for (int gq = 0; gq < 4; ++gq) {
                    u32x2 o;
                    o[0] = pack2(acc[mi][ni][4 * gq], acc[mi][ni][4 * gq + 1]);
                    o[1] = pack2(acc[mi][ni][4 * gq + 2], acc[mi][ni][4 * gq + 3]);
                    *(u32x2*)(op + gq * 16) = o;
                }
            }
        return;
    }
#pragma unroll
    for (int mi = 0; mi < 2; ++mi)
#pragma unroll
        for (int ni = 0; ni < 2; ++ni) {
            const int st = (wm * 2 + mi) * 4 + wn * 2 + ni;
            if (st % 5 == sp) continue;
            const int mt32 = mt * 8 + wm * 2 + mi, nt32 = nt * 4 + wn * 2 + ni;
            char* dst = ws + WS_PART + (size_t)sp * SLAB + (size_t)(mt32 * 20 + nt32) * 4096 + lane * 16;
#pragma unroll
            for (int gq = 0; gq < 4; ++gq) {
                f32x4 v = {acc[mi][ni][4 * gq], acc[mi][ni][4 * gq + 1], acc[mi][ni][4 * gq + 2], acc[mi][ni][4 * gq + 3]};
                asm volatile("global_store_dwordx4 %0, %1, off sc1\n\ts_nop 1" :: "v"(dst + gq * 1024), "v"(v) : "memory");
            }
        }
    asm volatile("s_waitcnt vmcnt(0)" ::: "memory");
    BARRIER();
    unsigned* cnt = (unsigned*)(ws + WS_CNT) + (mt * 5 + nt) * 64;
    if (wid == 0) {
        if (lane == 0) __hip_atomic_fetch_add(cnt, 1u, __ATOMIC_RELAXED, __HIP_MEMORY_SCOPE_AGENT);
        for (unsigned spins = 0; spins < (1u << 24); ++spins) {
            const unsigned c = __builtin_amdgcn_readfirstlane(__hip_atomic_load(cnt, __ATOMIC_RELAXED, __HIP_MEMORY_SCOPE_AGENT));
            if (c >= (unsigned)NSPLIT) break;
            __builtin_amdgcn_s_sleep(1);
        }
    }
    BARRIER();
    const auto rsrc = __builtin_amdgcn_make_buffer_rsrc((void*)(ws + WS_PART), 0, (int)(NSPLIT * SLAB), 0x00020000);
#pragma unroll
    for (int mi = 0; mi < 2; ++mi)
#pragma unroll
        for (int ni = 0; ni < 2; ++ni) {
            const int st = (wm * 2 + mi) * 4 + wn * 2 + ni;
            if (st % 5 != sp) continue;
            const int mt32 = mt * 8 + wm * 2 + mi, nt32 = nt * 4 + wn * 2 + ni;
            const int off = (mt32 * 20 + nt32) * 4096 + lane * 16;
            u32x4 v[4][4];
#pragma unroll
            for (int s = 0; s < 4; ++s) {
                const int so = (s < sp ? s : s + 1) * (int)SLAB;
#pragma unroll
                for (int gq = 0; gq < 4; ++gq) v[s][gq] = __builtin_amdgcn_raw_buffer_load_b128(rsrc, off + gq * 1024, so, 16);
            }
            char* op = (char*)out + (size_t)(mt32 * 32 + r) * 2560 + (640 + nt32 * 32 + 4 * h) * 2;
#pragma unroll
            for (int gq = 0; gq < 4; ++gq) {
                f32x4 t = {acc[mi][ni][4 * gq], acc[mi][ni][4 * gq + 1], acc[mi][ni][4 * gq + 2], acc[mi][ni][4 * gq + 3]};
                t += (__builtin_bit_cast(f32x4, v[0][gq]) + __builtin_bit_cast(f32x4, v[1][gq])) +
                     (__builtin_bit_cast(f32x4, v[2][gq]) + __builtin_bit_cast(f32x4, v[3][gq]));
                u32x2 o;
                o[0] = pack2(t[0], t[1]);
                o[1] = pack2(t[2], t[3]);
                *(u32x2*)(op + gq * 16) = o;
            }
        }
}

extern "C" void kernel_launch(void* const* d_in, const int* in_sizes, int n_in,
                              void* d_out, int out_size, void* d_ws, size_t ws_size,
                              hipStream_t stream) {
    (void)in_sizes; (void)n_in; (void)out_size; (void)ws_size;
    const __hip_bfloat16* Ax = (const __hip_bfloat16*)d_in[0];
    const __hip_bfloat16* Awqkv = (const __hip_bfloat16*)d_in[1];
    const __hip_bfloat16* Awo = (const __hip_bfloat16*)d_in[2];
    const __hip_bfloat16* Bx = (const __hip_bfloat16*)d_in[3];
    const __hip_bfloat16* Bckv = (const __hip_bfloat16*)d_in[4];
    const __hip_bfloat16* Bwq = (const __hip_bfloat16*)d_in[5];
    const __hip_bfloat16* Bwuk = (const __hip_bfloat16*)d_in[6];
    const __hip_bfloat16* Bwuv = (const __hip_bfloat16*)d_in[7];
    const __hip_bfloat16* Bwo = (const __hip_bfloat16*)d_in[8];
    char* ws = (char*)d_ws;
    (void)hipFuncSetAttribute((const void*)k1_proj, hipFuncAttributeMaxDynamicSharedMemorySize, G1_LDS);
    (void)hipFuncSetAttribute((const void*)k2_attn, hipFuncAttributeMaxDynamicSharedMemorySize, K2_LDS);
    (void)hipFuncSetAttribute((const void*)k3_oproj, hipFuncAttributeMaxDynamicSharedMemorySize, G3_LDS);
    k1_proj<<<256, 512, G1_LDS, stream>>>(Ax, Awqkv, Bx, Bckv, Bwq, Bwuk, Bwuv, Awo, Bwo, ws);
    k2_attn<<<256, 512, K2_LDS, stream>>>(ws);
    k3_oproj<<<240, 512, G3_LDS, stream>>>(Awo, Bwo, ws, (__hip_bfloat16*)d_out);
}
```

```cpp
#include <hip/hip_runtime.h>
#include <hip/hip_bf16.h>
#include <stdint.h>
#include <type_traits>

typedef short s16x4 __attribute__((ext_vector_type(4)));
typedef short bf16x8 __attribute__((ext_vector_type(8)));
typedef float f32x16 __attribute__((ext_vector_type(16)));
typedef float f32x4 __attribute__((ext_vector_type(4)));
typedef float f32x2 __attribute__((ext_vector_type(2)));
typedef unsigned u32x4 __attribute__((ext_vector_type(4)));
typedef unsigned u32x2 __attribute__((ext_vector_type(2)));
typedef __bf16 bf16x2_t __attribute__((ext_vector_type(2)));
#define LAS __attribute__((address_space(3)))
#define DI __device__ __forceinline__
#define MFMA32(a, b, c) __builtin_amdgcn_mfma_f32_32x32x16_bf16((a), (b), (c), 0, 0, 0)
#define MFMA16(a, b, c) __builtin_amdgcn_mfma_f32_16x16x32_bf16((a), (b), (c), 0, 0, 0)
#define CFENCE() asm volatile("" ::: "memory")
#define BARRIER() do { CFENCE(); __builtin_amdgcn_s_barrier(); CFENCE(); } while (0)
#define WAIT_VM(n) asm volatile("s_waitcnt vmcnt(" #n ") lgkmcnt(0)" ::: "memory")
#define SBAR() __builtin_amdgcn_sched_barrier(0)

DI unsigned pack2(float a, float b) {
    f32x2 v = {a, b};
    bf16x2_t r = __builtin_convertvector(v, bf16x2_t);
    return __builtin_bit_cast(unsigned, r);
}
DI u32x4 pack8(const f32x16& x, int s) {
    u32x4 w;
    w[0] = pack2(x[8 * s + 0], x[8 * s + 1]);
    w[1] = pack2(x[8 * s + 2], x[8 * s + 3]);
    w[2] = pack2(x[8 * s + 4], x[8 * s + 5]);
    w[3] = pack2(x[8 * s + 6], x[8 * s + 7]);
    return w;
}
DI u32x4 pack8s(const f32x16& x, int s, float c) {
    u32x4 w;
    w[0] = pack2(x[8 * s + 0] * c, x[8 * s + 1] * c);
    w[1] = pack2(x[8 * s + 2] * c, x[8 * s + 3] * c);
    w[2] = pack2(x[8 * s + 4] * c, x[8 * s + 5] * c);
    w[3] = pack2(x[8 * s + 6] * c, x[8 * s + 7] * c);
    return w;
}
DI bf16x8 tr_frag(LAS char* p0, LAS char* p1) {
    s16x4 lo = __builtin_amdgcn_ds_read_tr16_b64_v4i16((LAS s16x4*)p0);
    s16x4 hi = __builtin_amdgcn_ds_read_tr16_b64_v4i16((LAS s16x4*)p1);
    return __builtin_shufflevector(lo, hi, 0, 1, 2, 3, 4, 5, 6, 7);
}

DI void st16(void* p, u32x4 v) {
    asm volatile("global_store_dwordx4 %0, %1, off sc1\n\ts_nop 1" :: "v"(p), "v"(v) : "memory");
}
template <int I, int N, class F> DI void sfor(F&& f) {
    if constexpr (I < N) { f(std::integral_constant<int, I>{}); sfor<I + 1, N>(f); }
}
template <int OFF> DI void lds_rd(bf16x8& d, unsigned addr) {
    asm volatile("ds_read_b128 %0, %1 offset:%2" : "=v"(d) : "v"(addr), "i"(OFF));
}
template <int N> DI void lds_wait(bf16x8& d) { asm volatile("s_waitcnt lgkmcnt(%1)" : "+v"(d) : "i"(N)); }
template <int N> DI void lds_wait2(bf16x8& d, bf16x8& e) { asm volatile("s_waitcnt lgkmcnt(%2)" : "+v"(d), "+v"(e) : "i"(N)); }
#define LGKM0() __builtin_amdgcn_s_waitcnt(0xC07F)
DI void set_m0(unsigned v) { asm volatile("s_mov_b32 m0, %0\n\ts_nop 0" :: "s"(v) : "memory"); }
template <int OFF> DI void dma_s(unsigned voff, const char* sbase) {
    asm volatile("global_load_lds_dwordx4 %0, %1 offset:%2" :: "v"(voff), "s"(sbase), "i"(OFF) : "memory");
}

constexpr size_t WS_KVB = 0;
constexpr size_t WS_QB = WS_KVB + (size_t)32 * 1048576;
constexpr size_t WS_KVA = WS_QB + (size_t)16 * 1048576;
constexpr size_t WS_QA = WS_KVA + (size_t)8 * 1048576;
constexpr size_t WS_CTXB = WS_QA + (size_t)4 * 1048576;
constexpr size_t WS_CTXA = WS_CTXB + (size_t)16 * 1048576;
constexpr size_t WS_PART = WS_CTXA + (size_t)4 * 1048576;
constexpr size_t WS_CNT = WS_PART + (size_t)32 * 1048576;
constexpr int NSPLIT = 5;
constexpr size_t SLAB = (size_t)64 * 20 * 4096;

constexpr float CB = 0.08838834764831845f * 1.4426950408889634f;
constexpr float CA = 0.22360679774997896f * 1.4426950408889634f;

constexpr bool PREFETCH = true;
constexpr int G1_LDS = 2 * 65536 + 2048;
enum { EP_BK = 0, EP_BV = 1, EP_BQ = 2, EP_AQK = 3, EP_AV = 4 };

struct G1Src { unsigned xoff[4], woff[4]; const char* Xb; const char* Wb; unsigned wstep; int nkt; bool half; };
DI void g1_src(G1Src& s, const __hip_bfloat16* X, const __hip_bfloat16* W, int K, int ldw, int m0, int n0, int nlim, int wid, int lane, bool half) {
    s.half = half;
#pragma unroll
    for (int i = 0; i < 4; ++i) {
        const int row = (wid * 4 + i) * 8 + (lane >> 3);
        const int c = (lane & 7) ^ ((row >> 1) & 7);
        s.xoff[i] = (unsigned)(((m0 + row) * K + c * 8) * 2) + 4096 - i * 1024;
        const int k = (wid * 4 + i) * 2 + (lane >> 5);
        const int p = lane & 31, b = (p >> 2) ^ (k & 3);
        int col = n0 + (b * 4 + ((p & 3) ^ ((wid & 1) << 1))) * 8;
        col = col < nlim ? col : nlim - 8;
        s.woff[i] = (unsigned)((k * ldw + col) * 2) - i * 1024;
    }
    s.Xb = (const char*)X; s.Wb = (const char*)W; s.wstep = (unsigned)(64 * ldw * 2); s.nkt = K >> 6;
}
template <int I> DI void g1_piece(const G1Src& s, int kt) {
    if constexpr (I < 4) dma_s<I * 1024 - 4096>(s.xoff[I], s.Xb + (size_t)kt * 128);
    else if (!s.half || (threadIdx.x & 16) == 0) dma_s<(I - 4) * 1024>(s.woff[I - 4], s.Wb + (size_t)kt * s.wstep);
}

template <bool TR, int NT>
DI void g1_loop(f32x4 (&acc)[8][4], bf16x8 (&xs)[2][4], bf16x8 (&wsb)[2][4], const G1Src& cur, const G1Src& nxt, bool has_next, bool first,
                LAS char* lds, int wid, int lane) {
    const int wm = wid >> 2, wn = wid & 3, r16 = lane & 15, g = lane >> 4;
    const unsigned lds0 = (unsigned)(uintptr_t)lds;
    const int nkt = cur.nkt;
    int xb[2];
#pragma unroll
    for (int st = 0; st < 2; ++st) xb[st] = wm * 4 * 8192 + r16 * 128 + (((4 * st + g) ^ (r16 >> 1)) * 16);
    const int q = (lane >> 2) & 3, p4 = lane & 3;
    int wb[4];
#pragma unroll
    for (int nt = 0; nt < 4; ++nt) {
        const int b = NT == 4 ? wn * 2 + (nt >> 1) : wn;
        wb[nt] = g * 8192 + 4096 + q * 512 + ((b ^ q) * 64) + (((nt & 1) ^ (g & 1)) * 32) + p4 * 8;
    }
#pragma unroll
    for (int mt = 0; mt < 8; ++mt)
#pragma unroll
        for (int nt = 0; nt < 4; ++nt) acc[mt][nt] = f32x4{};
#define G1_RX1(set, bb, st, mh, m4) xs[set][m4] = *(LAS bf16x8*)((bb) + xb[st] + ((mh) * 2 + ((m4) >> 1)) * 8192 + ((m4) & 1) * 2048)
#define G1_RW1(bb, st, nt) wsb[st][nt] = tr_frag((bb) + wb[nt] + (st) * 32768, (bb) + wb[nt] + (st) * 32768 + 2048)
#define G1_RX(set, bb, st, mh) do { G1_RX1(set, bb, st, mh, 0); G1_RX1(set, bb, st, mh, 1); G1_RX1(set, bb, st, mh, 2); G1_RX1(set, bb, st, mh, 3); } while (0)
#define G1_RW(bb, st) do { G1_RW1(bb, st, 0); G1_RW1(bb, st, 1); G1_RW1(bb, st, 2); G1_RW1(bb, st, 3); } while (0)
#define G1_GRP(set, st, mh, m4, G)                                                                               \
    do {                                                                                                         \
        _Pragma("unroll") for (int nt = 0; nt < NT; ++nt)                                                        \
            acc[(mh) * 4 + (m4)][nt] = TR ? MFMA16(wsb[st][nt], xs[set][m4], acc[(mh) * 4 + (m4)][nt])           \
                                          : MFMA16(xs[set][m4], wsb[st][nt], acc[(mh) * 4 + (m4)][nt]);          \
        SBAR(); G; SBAR();                                                                                       \
    } while (0)
#define G1_UNIT(set, st, mh, G0, G1, G2, G3)                                                                     \
    do { G1_GRP(set, st, mh, 0, G0); G1_GRP(set, st, mh, 1, G1); G1_GRP(set, st, mh, 2, G2); G1_GRP(set, st, mh, 3, G3); } while (0)
#define G1_PC(i)                                                                                                 \
    do { if (on2) { if (kt + 2 < nkt) { g1_piece<2 * (i)>(cur, kt + 2); g1_piece<2 * (i) + 1>(cur, kt + 2); }    \
                    else { g1_piece<2 * (i)>(nxt, kt + 2 - nkt); g1_piece<2 * (i) + 1>(nxt, kt + 2 - nkt); } } } while (0)
    G1_RW(lds, 0);
    if (first) { G1_RX(0, lds, 0, 0); }
#pragma unroll 1
    for (int kt = 0; kt < nkt; ++kt) {
        LAS char* bb = lds + (kt & 1) * 65536;
        LAS char* bn = lds + ((kt + 1) & 1) * 65536;
        const bool on2 = kt + 2 < nkt || has_next;
        const bool nw = kt + 1 < nkt, nx = kt + 1 < nkt || has_next;
        LGKM0(); SBAR();
        G1_UNIT(0, 0, 0, { G1_RX1(1, bb, 0, 1, 0); G1_RX1(1, bb, 0, 1, 1); }, { G1_RX1(1, bb, 0, 1, 2); G1_RX1(1, bb, 0, 1, 3); }, {}, {});
        LGKM0(); SBAR();
        G1_UNIT(1, 0, 1, { G1_RW1(bb, 1, 0); G1_RW1(bb, 1, 1); }, { G1_RW1(bb, 1, 2); G1_RW1(bb, 1, 3); }, G1_RX(0, bb, 1, 0), {});
        LGKM0(); SBAR();
        G1_UNIT(0, 1, 0, { G1_RX1(1, bb, 1, 1, 0); G1_RX1(1, bb, 1, 1, 1); }, { G1_RX1(1, bb, 1, 1, 2); G1_RX1(1, bb, 1, 1, 3); }, {}, {});
        LGKM0();
        WAIT_VM(0);
        BARRIER();
        set_m0(lds0 + (kt & 1) * 65536 + wid * 8192 + 4096);
        SBAR();
        G1_UNIT(1, 1, 1, { if (nw) { G1_RW1(bn, 0, 0); G1_RW1(bn, 0, 1); } G1_PC(0); }, { if (nw) { G1_RW1(bn, 0, 2); G1_RW1(bn, 0, 3); } G1_PC(1); },
                { if (nx) G1_RX(0, bn, 0, 0); G1_PC(2); }, { G1_PC(3); });
    }
#undef G1_RX1
#undef G1_RW1
#undef G1_GRP
#undef G1_UNIT
#undef G1_PC
#undef G1_RX
#undef G1_RW
}

DI void st8(void* p, u32x2 v) {
    asm volatile("global_store_dwordx2 %0, %1, off sc1\n\ts_nop 1" :: "v"(p), "v"(v) : "memory");
}

template <int EP>
DI void g1_epi(const f32x4 (&acc)[8][4], int m0, int n0, int nlim, int nsec, char* __restrict__ ws, int wid, int lane) {
    const int wm = wid >> 2, wn = wid & 3, c = lane & 15, g = lane >> 4;
    if constexpr (EP == EP_BK || EP == EP_BQ || EP == EP_BV) {
#pragma unroll
        for (int mt = 0; mt < 8; ++mt) {
            const int tb = (m0 + wm * 128 + mt * 16) >> 5;
            const int tile = tb >> 1, kb = tb & 1, th = mt & 1;
#pragma unroll
            for (int np = 0; np < 2; ++np) {
                const int nb = n0 + wn * 64 + np * 32;
                const int head = nb >> 7;
                const float sc = EP == EP_BQ ? CB : 1.0f;
                const f32x4 a = acc[mt][2 * np], b = acc[mt][2 * np + 1];
                unsigned a0 = pack2(a[0] * sc, a[1] * sc), a1 = pack2(a[2] * sc, a[3] * sc);
                unsigned b0 = pack2(b[0] * sc, b[1] * sc), b1 = pack2(b[2] * sc, b[3] * sc);
                if constexpr (EP == EP_BV) {
                    auto r0 = __builtin_amdgcn_permlane32_swap(a0, b0, false, false);
                    auto r1 = __builtin_amdgcn_permlane32_swap(a1, b1, false, false);
                    const u32x4 w = {r0[0], r1[0], r0[1], r1[1]};
                    const int db = (nb >> 5) & 3;
                    char* base = ws + WS_KVB + (size_t)head * 1048576 + tile * 32768 + 16384 + ((kb * 2 + th) * 4 + db) * 1024;
                    st16(base + ((g >> 1) * 16 + c + 32 * (g & 1)) * 16, w);
                } else {
                    auto r0 = __builtin_amdgcn_permlane16_swap(a0, b0, false, false);
                    auto r1 = __builtin_amdgcn_permlane16_swap(a1, b1, false, false);
                    const u32x4 w = {r0[0], r1[0], r0[1], r1[1]};
                    const int ks = ((nb >> 4) & 7) + (g & 1);
                    char* base = EP == EP_BK ? ws + WS_KVB + (size_t)head * 1048576 + tile * 32768 + (kb * 8 + ks) * 1024
                                             : ws + WS_QB + (size_t)((head * 64 + tb) * 8 + ks) * 1024;
                    st16(base + (th * 16 + c + 32 * (g >> 1)) * 16, w);
                }
            }
        }
        return;
    }
#pragma unroll
    for (int mt = 0; mt < 8; ++mt) {
        const int tb = (m0 + wm * 128 + mt * 16) >> 5;
        const int tile = tb >> 1, kb = tb & 1, th = mt & 1;
#pragma unroll
        for (int nt = 0; nt < 4; ++nt) {
            const int nb = n0 + wn * 64 + nt * 16;
            if (nb >= nlim) continue;
            const f32x4 a = acc[mt][nt];
            if constexpr (EP == EP_BK || EP == EP_BQ || EP == EP_BV) {
            } else if constexpr (EP == EP_AQK) {
                const bool isq = nb < 640;
                const float sc = isq ? CA : 1.0f;
                const int r = th * 16 + c;
                const int n = nb - (isq ? 0 : 640) + 4 * g;
                const int head = n / 20, d0 = n - head * 20;
                char* base = isq ? ws + WS_QA + (size_t)((head * 64 + tb) * 2) * 1024
                                 : ws + WS_KVA + (size_t)head * 262144 + tile * 8192 + kb * 2048;
                char* dst = base + (d0 >> 4) * 1024 + (r + 32 * ((d0 >> 3) & 1)) * 16 + (d0 & 4) * 2;
                u32x2 w;
                w[0] = pack2(a[0] * sc, a[1] * sc);
                w[1] = pack2(a[2] * sc, a[3] * sc);
                *(u32x2*)dst = w;
                if (d0 == 16) {
                    *(u32x2*)(dst + 8) = (u32x2){isq ? 0u : 0x3F803F80u, 0u};
                    *(u32x4*)(base + 1024 + (r + 32) * 16) = (u32x4){0u, 0u, 0u, 0u};
                }
            } else {
                const int n = nb - nsec + c;
                const int head = n / 20, d = n - head * 20;
                char* dst = ws + WS_KVA + (size_t)head * 262144 + tile * 8192 + 4096 + (kb * 2 + th) * 1024 + (d + 32 * (g & 1)) * 16 + (g >> 1) * 8;
                u32x2 w;
                w[0] = pack2(a[0], a[1]);
                w[1] = pack2(a[2], a[3]);
                *(u32x2*)dst = w;
                if (d == 19) *(u32x2*)(dst + 16) = (u32x2){0x3F803F80u, 0x3F803F80u};
            }
        }
    }
}

__global__ __launch_bounds__(512, 2) void k1_proj(const __hip_bfloat16* __restrict__ Ax, const __hip_bfloat16* __restrict__ Awqkv,
                                                  const __hip_bfloat16* __restrict__ Bx, const __hip_bfloat16* __restrict__ Bckv,
                                                  const __hip_bfloat16* __restrict__ Bwq, const __hip_bfloat16* __restrict__ Bwuk,
                                                  const __hip_bfloat16* __restrict__ Bwuv, const __hip_bfloat16* __restrict__ Awo,
                                                  const __hip_bfloat16* __restrict__ Bwo, char* __restrict__ ws) {
    extern __shared__ __attribute__((aligned(16))) char smem[];
    LAS char* lds = (LAS char*)smem;
    const int tid = threadIdx.x, lane = tid & 63, wid = __builtin_amdgcn_readfirstlane(tid >> 6);
    const int bid = blockIdx.x;
    const int m0 = ((bid >> 3) & 7) * 256;
    const int grp = bid >> 6, c8 = bid & 7;
    const int cc = c8 + 8 * (grp & 1);
    const bool has2 = grp < 3;
    G1Src s1, s2;
    if (grp < 2) {
        g1_src(s1, Bckv, Bwuk, 512, 4096, m0, cc * 256, 4096, wid, lane, false);
        g1_src(s2, Bx, Bwq, 640, 4096, m0, cc * 256, 4096, wid, lane, false);
    } else if (grp == 2) {
        g1_src(s1, Bckv, Bwuv, 512, 4096, m0, c8 * 256, 4096, wid, lane, false);
        g1_src(s2, Bckv, Bwuv, 512, 4096, m0, (c8 + 8) * 256, 4096, wid, lane, false);
    } else {
        g1_src(s1, Ax, Awqkv, 640, 1920, m0, c8 * 256, c8 < 5 ? 1280 : 1920, wid, lane, false);
        s2 = s1;
    }

    f32x4 acc[8][4];
    bf16x8 xf[2][4], wf[2][4];
    const unsigned lds0 = (unsigned)(uintptr_t)lds;
    if (bid == 255 && tid < 40) ((unsigned*)(ws + WS_CNT))[tid * 64] = 0u;
    set_m0(lds0 + wid * 8192 + 4096);
    sfor<0, 8>([&](auto ic) { g1_piece<decltype(ic)::value>(s1, 0); });
    set_m0(lds0 + 65536 + wid * 8192 + 4096);
    sfor<0, 8>([&](auto ic) { g1_piece<decltype(ic)::value>(s1, 1); });
    WAIT_VM(8);
    BARRIER();
    if (grp == 3) {
        set_m0(lds0 + 131072 + wid * 256);
        const unsigned gw = (unsigned)((bid - 192) * 8 + wid) * 64 + lane;
#define PF(ptr, nbytes) do { _Pragma("unroll") for (unsigned o_ = 0; o_ < (nbytes); o_ += 32768u * 128u) {              \
            unsigned off_ = o_ + gw * 128u; off_ = off_ < (nbytes) ? off_ : 0u;                                           \
            asm volatile("global_load_lds_dword %0, %1" :: "v"(off_), "s"((const char*)(ptr)) : "memory"); } } while (0)
        PF(Bx, 2048u * 640u * 2u); PF(Bwq, 640u * 4096u * 2u); PF(Bwuv, 512u * 4096u * 2u);
        PF(Bwo, 4096u * 640u * 2u); PF(Awo, 640u * 640u * 2u);
#undef PF
    }
    if (grp < 2 || (grp == 3 && c8 < 5)) {
        g1_loop<true, 4>(acc, xf, wf, s1, s2, has2, true, lds, wid, lane);
        if (grp < 2) g1_epi<EP_BK>(acc, m0, cc * 256, 4096, 0, ws, wid, lane);
        else g1_epi<EP_AQK>(acc, m0, c8 * 256, 1280, 0, ws, wid, lane);
    } else {
        g1_loop<false, 4>(acc, xf, wf, s1, s2, has2, true, lds, wid, lane);
        if (grp == 2) g1_epi<EP_BV>(acc, m0, c8 * 256, 4096, 0, ws, wid, lane);
        else g1_epi<EP_AV>(acc, m0, c8 * 256, 1920, 1280, ws, wid, lane);
    }
    if (grp < 2) {
        g1_loop<true, 4>(acc, xf, wf, s2, s2, false, false, lds, wid, lane);
        g1_epi<EP_BQ>(acc, m0, cc * 256, 4096, 0, ws, wid, lane);
    } else if (grp == 2) {
        g1_loop<false, 4>(acc, xf, wf, s2, s2, false, false, lds, wid, lane);
        g1_epi<EP_BV>(acc, m0, (c8 + 8) * 256, 4096, 0, ws, wid, lane);
    }
}

constexpr int SLOT = 40960;
constexpr int NSLOT = 4;
constexpr int K2_LDS = NSLOT * SLOT;
constexpr int NIT = 34;

__global__ __launch_bounds__(512, 2) void k2_attn(char* __restrict__ ws) {
    extern __shared__ __attribute__((aligned(16))) char smem[];
    LAS char* lds = (LAS char*)smem;
    const int tid = threadIdx.x, lane = tid & 63, wid = __builtin_amdgcn_readfirstlane(tid >> 6);
    const int r = lane & 31, h = lane >> 5;
    const int g = blockIdx.x, xcd = g & 7, idx = g >> 3;
    const int head = xcd * 4 + (idx >> 3), x = idx & 7;
    const char* kvb = ws + WS_KVB + (size_t)head * 1048576;
    const char* kva = ws + WS_KVA + (size_t)head * 262144;
    const int n1 = 32 - 2 * x;
    const unsigned lbase = (unsigned)(uintptr_t)lds + lane * 16;

    const unsigned lds0 = (unsigned)(uintptr_t)lds;
    const unsigned voff = lane * 16;
    const int wa = wid & 3;
    const char* sbB = kvb;
    const char* sbA = kva;
#define K2_M0(tt)                                                                           \
    do {                                                                                    \
        const int t3_ = (tt) < NIT ? (tt) : NIT - 1;                                        \
        const int tb3_ = t3_ < n1 ? t3_ : t3_ - n1;                                         \
        const int ta3_ = t3_ < 32 ? t3_ : 31;                                               \
        sbB = kvb + (size_t)tb3_ * 32768 + wid * 4096 + 2048;                               \
        sbA = kva + (size_t)ta3_ * 8192 + wid * 1024 - 2048;                                \
        set_m0(lds0 + ((tt) < NIT ? ((tt) & 3) : 2 + ((tt) & 1)) * SLOT + wid * 5120 + 2048); \
    } while (0)
#define K2_DMA(i) do { SBAR(); if constexpr ((i) < 4) dma_s<(i) * 1024 - 2048>(voff, sbB); else dma_s<2048>(voff, sbA); SBAR(); } while (0)
#define K2_STAGE_ALL(tt) do { K2_M0(tt); K2_DMA(0); K2_DMA(1); K2_DMA(2); K2_DMA(3); K2_DMA(4); } while (0)
#define BOFF(f) (((f) >> 2) * 5120 + ((f) & 3) * 1024)
#define AOFF(a) ((a) * 5120 + 4096)
    K2_STAGE_ALL(0);
    K2_STAGE_ALL(1);

#define K2_SYNC(it) do { WAIT_VM(5); BARRIER(); K2_M0((it) + 3); } while (0)

    if (wid < 4) {
        bf16x8 fr[8];
        K2_SYNC(-1);
        sfor<0, 8>([&](auto ic) { constexpr int j = decltype(ic)::value; lds_rd<BOFF((j & 1) * 8 + (j >> 1))>(fr[j], lbase); });
        K2_DMA(0); K2_DMA(1);
        int it = 0;
        for (int blk = 0; blk < 2; ++blk) {
            const int qb = blk == 0 ? 15 - x : x;
            const int nt = 2 * qb + 2;
            const int q0 = qb * 128 + wid * 32;
            const int tb32 = q0 >> 5;
            bf16x8 qf[8];
            const char* qp = ws + WS_QB + (size_t)((head * 64 + tb32) * 8) * 1024 + lane * 16;
#pragma unroll
            for (int ks = 0; ks < 8; ++ks) qf[ks] = *(const bf16x8*)(qp + ks * 1024);
            f32x16 o[4];
#pragma unroll
            for (int db = 0; db < 4; ++db) o[db] = f32x16{};
            f32x16 negm = f32x16{};
            float l = 0.f;
            for (int t = 0; t < nt; ++t, ++it) {
                const int rel = (q0 - 64 * t) >> 5;
                const unsigned ac = lbase + (it & 3) * SLOT;
                const unsigned an = lbase + ((it + 1) & 3) * SLOT;
                f32x16 s0, s1;
                sfor<0, 16>([&](auto ic) {
                    constexpr int j = decltype(ic)::value, ks = j >> 1;
                    lds_wait<7>(fr[j & 7]);
                    if constexpr (j == 0) s0 = MFMA32(fr[0], qf[0], negm);
                    else if constexpr (j == 1) s1 = MFMA32(fr[1], qf[0], negm);
                    else if constexpr ((j & 1) == 0) s0 = MFMA32(fr[j & 7], qf[ks], s0);
                    else s1 = MFMA32(fr[j & 7], qf[ks], s1);
                    constexpr int jn = j + 8;
                    SBAR();
                    if constexpr (jn < 16) lds_rd<BOFF((jn & 1) * 8 + (jn >> 1))>(fr[j & 7], ac);
                    else lds_rd<BOFF(jn)>(fr[j & 7], ac);
                    if constexpr (j == 2) K2_DMA(2);
                    if constexpr (j == 8) K2_DMA(3);
                    if constexpr (j == 14) K2_DMA(4);
                    SBAR();
                });
#define B_MASK()                                                                                        \
    do {                                                                                                \
        if (rel <= 1) {                                                                                 \
            const int lim = 32 * rel + r - 4 * h;                                                       \
            _Pragma("unroll") for (int i = 0; i < 16; ++i) {                                            \
                const int kr = (i & 3) + 8 * (i >> 2);                                                  \
                if (kr > lim) s0[i] = -__builtin_inff();                                                \
                if (kr + 32 > lim) s1[i] = -__builtin_inff();                                           \
            }                                                                                           \
        }                                                                                               \
    } while (0)
                B_MASK();
                float ps = 0.f;
                if (t > 0) {
#pragma unroll
                    for (int i = 0; i < 16; ++i) { s0[i] = __builtin_amdgcn_exp2f(s0[i]); ps += s0[i]; }
#pragma unroll
                    for (int i = 0; i < 16; ++i) { s1[i] = __builtin_amdgcn_exp2f(s1[i]); ps += s1[i]; }
                }
                if (t == 0 || __any(!(ps <= 65536.f))) {
                    if (t > 0) {
                        LAS char* sl = lds + (it & 3) * SLOT + lane * 16;
                        s0 = negm; s1 = negm;
#pragma unroll 1
                        for (int ks = 0; ks < 8; ++ks) {
                            bf16x8 qq = qf[0];
#pragma unroll
                            for (int u = 1; u < 8; ++u) qq = (ks == u) ? qf[u] : qq;
                            s0 = MFMA32(*(LAS bf16x8*)(sl + (ks >> 2) * 5120 + (ks & 3) * 1024), qq, s0);
                            s1 = MFMA32(*(LAS bf16x8*)(sl + 10240 + (ks >> 2) * 5120 + (ks & 3) * 1024), qq, s1);
                        }
                        B_MASK();
                    }
                    float mx = s0[0];
#pragma unroll
                    for (int i = 1; i < 16; ++i) mx = fmaxf(mx, s0[i]);
#pragma unroll
                    for (int i = 0; i < 16; ++i) mx = fmaxf(mx, s1[i]);
                    auto rr = __builtin_amdgcn_permlane32_swap(__float_as_uint(mx), __float_as_uint(mx), false, false);
                    mx = fmaxf(__uint_as_float(rr[0]), __uint_as_float(rr[1]));
                    float delta = mx;
                    if (t > 0) {
                        delta = fmaxf(mx, 0.f);
                        const float alpha = __builtin_amdgcn_exp2f(-delta);
                        l *= alpha;
#pragma unroll
                        for (int db = 0; db < 4; ++db)
#pragma unroll
                            for (int i = 0; i < 16; ++i) o[db][i] *= alpha;
                    }
                    const float nm = negm[0] - delta;
#pragma unroll
                    for (int i = 0; i < 16; ++i) negm[i] = nm;
                    ps = 0.f;
#pragma unroll
                    for (int i = 0; i < 16; ++i) { s0[i] = __builtin_amdgcn_exp2f(s0[i] - delta); ps += s0[i]; }
#pragma unroll
                    for (int i = 0; i < 16; ++i) { s1[i] = __builtin_amdgcn_exp2f(s1[i] - delta); ps += s1[i]; }
                }
                l += ps;
                bf16x8 pf[4];
                pf[0] = __builtin_bit_cast(bf16x8, pack8(s0, 0));
                pf[1] = __builtin_bit_cast(bf16x8, pack8(s0, 1));
                pf[2] = __builtin_bit_cast(bf16x8, pack8(s1, 0));
                pf[3] = __builtin_bit_cast(bf16x8, pack8(s1, 1));
                if (it + 1 < NIT) { K2_SYNC(it); } else { K2_M0(it + 3); }
                sfor<0, 16>([&](auto ic) {
                    constexpr int j = decltype(ic)::value, s4 = j >> 2, db = j & 3;
                    lds_wait<7>(fr[j & 7]);
                    o[db] = MFMA32(fr[j & 7], pf[s4], o[db]);
                    SBAR();
                    if constexpr (j < 8) lds_rd<BOFF(24 + j)>(fr[j & 7], ac);
                    else { constexpr int jn = j - 8; lds_rd<BOFF((jn & 1) * 8 + (jn >> 1))>(fr[j & 7], an); }
                    if constexpr (j == 4) K2_DMA(0);
                    if constexpr (j == 10) K2_DMA(1);
                    SBAR();
                });
            }
            auto rr = __builtin_amdgcn_permlane32_swap(__float_as_uint(l), __float_as_uint(l), false, false);
            const float inv = 1.0f / (__uint_as_float(rr[0]) + __uint_as_float(rr[1]));
            char* cp = ws + WS_CTXB + (size_t)(tb32 * 256 + head * 8) * 1024 + lane * 16;
#pragma unroll
            for (int db = 0; db < 4; ++db) {
                st16(cp + (2 * db) * 1024, pack8s(o[db], 0, inv));
                st16(cp + (2 * db + 1) * 1024, pack8s(o[db], 1, inv));
            }
        }
        WAIT_VM(0);
#undef B_MASK
    } else {
        const int tb32 = x * 8 + wa * 2;
        bf16x8 qf[2][2];
        const char* qp = ws + WS_QA + (size_t)((head * 64 + tb32) * 2) * 1024 + lane * 16;
#pragma unroll
        for (int qt = 0; qt < 2; ++qt)
#pragma unroll
            for (int ks = 0; ks < 2; ++ks) qf[qt][ks] = *(const bf16x8*)(qp + (qt * 2 + ks) * 1024);
        f32x16 o[2];
        o[0] = f32x16{}; o[1] = f32x16{};
        float negm[2] = {0.f, 0.f};
        bf16x8 kf[4], vf[4];
        f32x16 s[2][2];
        K2_SYNC(-1);
        lds_rd<AOFF(0)>(kf[0], lbase); lds_rd<AOFF(1)>(kf[1], lbase); lds_rd<AOFF(2)>(kf[2], lbase); lds_rd<AOFF(3)>(kf[3], lbase);
        for (int it = 0; it <= 32; ++it) {
            if (it > 0) {
                u32x4 pf[2][2][2];
                unsigned chk = 0;
                sfor<0, 4>([&](auto bc) {
                    constexpr int blk = decltype(bc)::value, kb = blk >> 1, qt = blk & 1;
                    f32x16 e;
#pragma unroll
                    for (int i = 0; i < 8; ++i) e[i] = __builtin_amdgcn_exp2f(s[kb][qt][i]);
                    K2_DMA(blk);
#pragma unroll
                    for (int i = 8; i < 16; ++i) e[i] = __builtin_amdgcn_exp2f(s[kb][qt][i]);
                    if constexpr (blk == 3) K2_DMA(4);
                    pf[kb][qt][0] = pack8(e, 0);
                    pf[kb][qt][1] = pack8(e, 1);
                    chk |= pf[kb][qt][0][0] | pf[kb][qt][0][1] | pf[kb][qt][0][2] | pf[kb][qt][0][3];
                    chk |= pf[kb][qt][1][0] | pf[kb][qt][1][1] | pf[kb][qt][1][2] | pf[kb][qt][1][3];
                });
                const bool first = (it == 1);
                if (first || __any((chk & 0x40004000u) != 0u)) {
#pragma unroll
                    for (int qt = 0; qt < 2; ++qt) {
                        float mx = s[0][qt][0];
#pragma unroll
                        for (int i = 1; i < 16; ++i) mx = fmaxf(mx, s[0][qt][i]);
#pragma unroll
                        for (int i = 0; i < 16; ++i) mx = fmaxf(mx, s[1][qt][i]);
                        auto rr = __builtin_amdgcn_permlane32_swap(__float_as_uint(mx), __float_as_uint(mx), false, false);
                        mx = fmaxf(__uint_as_float(rr[0]), __uint_as_float(rr[1]));
                        float delta = mx + 10.f;
                        if (!first) delta = fmaxf(delta, 0.f);
                        const float tgt = negm[qt] - delta;
                        const unsigned hi = pack2(tgt, 0.f) & 0xFFFFu;
                        const float hif = __uint_as_float(hi << 16);
                        const unsigned lo = pack2(tgt - hif, 0.f) & 0xFFFFu;
                        const float neff = hif + __uint_as_float(lo << 16);
                        delta = negm[qt] - neff;
                        negm[qt] = neff;
                        {
                            u32x4 qq = __builtin_bit_cast(u32x4, qf[qt][1]);
                            qq[2] = h ? 0u : (hi | (lo << 16));
                            qf[qt][1] = __builtin_bit_cast(bf16x8, qq);
                        }
                        if (!first) {
                            const float alpha = __builtin_amdgcn_exp2f(-delta);
#pragma unroll
                            for (int i = 0; i < 16; ++i) o[qt][i] *= alpha;
                        }
#pragma unroll
                        for (int kb = 0; kb < 2; ++kb) {
                            f32x16 e;
#pragma unroll
                            for (int i = 0; i < 16; ++i) e[i] = __builtin_amdgcn_exp2f(s[kb][qt][i] - delta);
                            pf[kb][qt][0] = pack8(e, 0);
                            pf[kb][qt][1] = pack8(e, 1);
                        }
                    }
                }
                lds_wait2<4>(vf[0], vf[1]);
                lds_wait2<4>(vf[2], vf[3]);
#pragma unroll
                for (int kb = 0; kb < 2; ++kb)
#pragma unroll
                    for (int s2 = 0; s2 < 2; ++s2)
#pragma unroll
                        for (int qt = 0; qt < 2; ++qt)
                            o[qt] = MFMA32(vf[kb * 2 + s2], __builtin_bit_cast(bf16x8, pf[kb][qt][s2]), o[qt]);
            } else {
                K2_DMA(0); K2_DMA(1); K2_DMA(2); K2_DMA(3); K2_DMA(4);
            }
            lds_wait2<0>(kf[0], kf[1]);
            lds_wait2<0>(kf[2], kf[3]);
#pragma unroll
            for (int kb = 0; kb < 2; ++kb)
#pragma unroll
                for (int qt = 0; qt < 2; ++qt) {
                    s[kb][qt] = MFMA32(kf[kb * 2], qf[qt][0], f32x16{});
                    s[kb][qt] = MFMA32(kf[kb * 2 + 1], qf[qt][1], s[kb][qt]);
                }
            SBAR();
            K2_SYNC(it);
            const unsigned ac = lbase + (it & 3) * SLOT;
            const unsigned an = lbase + ((it + 1) & 3) * SLOT;
            lds_rd<AOFF(4)>(vf[0], ac); lds_rd<AOFF(5)>(vf[1], ac); lds_rd<AOFF(6)>(vf[2], ac); lds_rd<AOFF(7)>(vf[3], ac);
            lds_rd<AOFF(0)>(kf[0], an); lds_rd<AOFF(1)>(kf[1], an); lds_rd<AOFF(2)>(kf[2], an); lds_rd<AOFF(3)>(kf[3], an);
        }
        WAIT_VM(0);
#pragma unroll
        for (int qt = 0; qt < 2; ++qt) {
            const unsigned lv = __float_as_uint(o[qt][8]);
            auto rr = __builtin_amdgcn_permlane32_swap(lv, lv, false, false);
            const float lsum = __uint_as_float(rr[1]);
            const float inv = 1.0f / lsum;
            char* cp = ws + WS_CTXA + (size_t)((tb32 + qt) * 40) * 1024;
#pragma unroll
            for (int gq = 0; gq < 3; ++gq) {
                const int d0 = 8 * gq + 4 * h;
                if (d0 < 20) {
                    const int k = head * 20 + d0;
                    u32x2 w;
                    w[0] = pack2(o[qt][4 * gq + 0] * inv, o[qt][4 * gq + 1] * inv);
                    w[1] = pack2(o[qt][4 * gq + 2] * inv, o[qt][4 * gq + 3] * inv);
                    *(u32x2*)(cp + (k >> 4) * 1024 + (r + 32 * ((k >> 3) & 1)) * 16 + (k & 4) * 2) = w;
                }
            }
        }
    }
}

constexpr int G3_STAGE = 49152;
constexpr int G3_LDS = 3 * G3_STAGE;

__global__ __launch_bounds__(512, 2) void k3_oproj(const __hip_bfloat16* __restrict__ Awo, const __hip_bfloat16* __restrict__ Bwo,
                                                   char* __restrict__ ws, __hip_bfloat16* __restrict__ out) {
    extern __shared__ __attribute__((aligned(16))) char smem[];
    LAS char* lds = (LAS char*)smem;
    const int tid = threadIdx.x, lane = tid & 63, wid = __builtin_amdgcn_readfirstlane(tid >> 6);
    const int wm = wid >> 1, wn = wid & 1, h = lane >> 5;
    const int bid = blockIdx.x;
    const bool isB = bid < 200;
    int mt, nt, sp, kt0, nkt, kstot;
    const char* Xc; const char* Wb;
    if (isB) {
        mt = bid & 7; const int rest = bid >> 3; nt = rest % 5; sp = rest / 5;
        kt0 = sp * 13; nkt = sp == 4 ? 12 : 13; kstot = 256;
        Xc = ws + WS_CTXB; Wb = (const char*)Bwo;
    } else {
        const int b2 = bid - 200; mt = b2 & 7; nt = b2 >> 3; sp = 5;
        kt0 = 0; nkt = 10; kstot = 40;
        Xc = ws + WS_CTXA; Wb = (const char*)Awo;
    }
    unsigned woff[2];
#pragma unroll
    for (int i = 0; i < 2; ++i) {
        const int k = (wid * 2 + i) * 4 + (lane >> 4);
        const int p = lane & 15, b = (p >> 2) ^ (k & 3);
        woff[i] = (unsigned)(((kt0 * 64 + k) * 640 + nt * 128 + (b * 4 + (p & 3)) * 8) * 2);
    }
    const unsigned lds0 = (unsigned)(uintptr_t)lds;
    const char* xsb = Xc + (size_t)((mt * 8 + wid) * kstot + kt0 * 4) * 1024 + 4096;
    const unsigned voff = lane * 16;
    woff[1] -= 1024;
    auto piece = [&](int kt, auto ic) {
        constexpr int i = decltype(ic)::value;
        if constexpr (i < 4) dma_s<i * 1024 - 4096>(voff, xsb + (size_t)kt * 4096);
        else dma_s<(i - 4) * 1024>(woff[i - 4], Wb + (size_t)kt * (64u * 640u * 2u));
    };
    const int q = (lane >> 2) & 3, p4 = lane & 3, g16 = (lane >> 4) & 1;
    const int r0 = isB ? 4 * h : 8 * h, r1 = isB ? 8 + 4 * h : 8 * h + 4;
    int wb0[2], wb1[2];
#pragma unroll
    for (int ni = 0; ni < 2; ++ni) {
        const int b = wn * 2 + ni;
        const int c = ((b ^ q) * 64) + g16 * 32 + p4 * 8;
        wb0[ni] = (r0 >> 3) * 6144 + 4096 + ((r0 & 7) + q) * 256 + c;
        wb1[ni] = (r1 >> 3) * 6144 + 4096 + ((r1 & 7) + q) * 256 + c;
    }
    const int xb0 = wm * 2 * 6144 + lane * 16;
    f32x16 acc[2][2];
#pragma unroll
    for (int mi = 0; mi < 2; ++mi)
#pragma unroll
        for (int ni = 0; ni < 2; ++ni) acc[mi][ni] = f32x16{};

    bf16x8 xf[2][4][2], wf[2][4][2];
#define G3_RD(set, bb)                                                                                           \
    do {                                                                                                         \
        _Pragma("unroll") for (int ks = 0; ks < 4; ++ks) {                                                       \
            _Pragma("unroll") for (int ni = 0; ni < 2; ++ni)                                                     \
                wf[set][ks][ni] = tr_frag((bb) + wb0[ni] + ks * 12288, (bb) + wb1[ni] + ks * 12288);             \
            _Pragma("unroll") for (int mi = 0; mi < 2; ++mi)                                                     \
                xf[set][ks][mi] = *(LAS bf16x8*)((bb) + xb0 + mi * 6144 + ks * 1024);                            \
        }                                                                                                        \
    } while (0)
#define G3_STEP(set, kt)                                                                                         \
    do {                                                                                                         \
        if ((kt) + 2 < nkt) { WAIT_VM(6); } else { WAIT_VM(0); }                                                 \
        LGKM0();                                                                                                 \
        BARRIER();                                                                                               \
        const int b0_ = (kt) % 3, b1_ = ((kt) + 1) % 3;                                                          \
        set_m0(lds0 + b0_ * G3_STAGE + wid * 6144 + 4096);                                                       \
        if ((kt) + 1 < nkt) G3_RD((set) ^ 1, lds + b1_ * G3_STAGE);                                              \
        SBAR();                                                                                                  \
        const bool on_ = (kt) + 3 < nkt;                                                                         \
        sfor<0, 8>([&](auto jc) {                                                                                \
            constexpr int j = decltype(jc)::value, ks = j >> 1, mi = j & 1;                                      \
            _Pragma("unroll") for (int ni = 0; ni < 2; ++ni)                                                     \
                acc[mi][ni] = MFMA32(wf[set][ks][ni], xf[set][ks][mi], acc[mi][ni]);                             \
            if constexpr (j < 6) { SBAR(); if (on_) piece((kt) + 3, jc); SBAR(); }                               \
        });                                                                                                      \
        SBAR();                                                                                                  \
    } while (0)
#pragma unroll
    for (int t = 0; t < 3; ++t) {
        set_m0(lds0 + t * G3_STAGE + wid * 6144 + 4096);
        sfor<0, 6>([&](auto ic) { piece(t, ic); });
    }
    WAIT_VM(12);
    BARRIER();
    G3_RD(0, lds);
    int kt = 0;
#pragma unroll 1
    for (; kt + 1 < nkt; kt += 2) { G3_STEP(0, kt); G3_STEP(1, kt + 1); }
    if (kt < nkt) G3_STEP(0, kt);
#undef G3_RD
#undef G3_STEP
    const int r = lane & 31;
    if (!isB) {
#pragma unroll
        for (int mi = 0; mi < 2; ++mi)
#pragma unroll
            for (int ni = 0; ni < 2; ++ni) {
                char* op = (char*)out + (size_t)((mt * 8 + wm * 2 + mi) * 32 + r) * 2560 + ((nt * 4 + wn * 2 + ni) * 32 + 4 * h) * 2;
#pragma unroll
                for (int gq = 0; gq < 4; ++gq) {
                    u32x2 o;
                    o[0] = pack2(acc[mi][ni][4 * gq], acc[mi][ni][4 * gq + 1]);
                    o[1] = pack2(acc[mi][ni][4 * gq + 2], acc[mi][ni][4 * gq + 3]);
                    *(u32x2*)(op + gq * 16) = o;
                }
            }
        return;
    }
#pragma unroll
    for (int mi = 0; mi < 2; ++mi)
#pragma unroll
        for (int ni = 0; ni < 2; ++ni) {
            const int st = (wm * 2 + mi) * 4 + wn * 2 + ni;
            if (st % 5 == sp) continue;
            const int mt32 = mt * 8 + wm * 2 + mi, nt32 = nt * 4 + wn * 2 + ni;
            char* dst = ws + WS_PART + (size_t)sp * SLAB + (size_t)(mt32 * 20 + nt32) * 4096 + lane * 16;
#pragma unroll
            for (int gq = 0; gq < 4; ++gq) {
                f32x4 v = {acc[mi][ni][4 * gq], acc[mi][ni][4 * gq + 1], acc[mi][ni][4 * gq + 2], acc[mi][ni][4 * gq + 3]};
                asm volatile("global_store_dwordx4 %0, %1, off sc1\n\ts_nop 1" :: "v"(dst + gq * 1024), "v"(v) : "memory");
            }
        }
    asm volatile("s_waitcnt vmcnt(0)" ::: "memory");
    BARRIER();
    unsigned* cnt = (unsigned*)(ws + WS_CNT) + (mt * 5 + nt) * 64;
    if (wid == 0) {
        if (lane == 0) __hip_atomic_fetch_add(cnt, 1u, __ATOMIC_RELAXED, __HIP_MEMORY_SCOPE_AGENT);
        for (unsigned spins = 0; spins < (1u << 24); ++spins) {
            const unsigned c = __builtin_amdgcn_readfirstlane(__hip_atomic_load(cnt, __ATOMIC_RELAXED, __HIP_MEMORY_SCOPE_AGENT));
            if (c >= (unsigned)NSPLIT) break;
            __builtin_amdgcn_s_sleep(1);
        }
    }
    BARRIER();
    const auto rsrc = __builtin_amdgcn_make_buffer_rsrc((void*)(ws + WS_PART), 0, (int)(NSPLIT * SLAB), 0x00020000);
#pragma unroll
    for (int mi = 0; mi < 2; ++mi)
#pragma unroll
        for (int ni = 0; ni < 2; ++ni) {
            const int st = (wm * 2 + mi) * 4 + wn * 2 + ni;
            if (st % 5 != sp) continue;
            const int mt32 = mt * 8 + wm * 2 + mi, nt32 = nt * 4 + wn * 2 + ni;
            const int off = (mt32 * 20 + nt32) * 4096 + lane * 16;
            u32x4 v[4][4];
#pragma unroll
            for (int s = 0; s < 4; ++s) {
                const int so = (s < sp ? s : s + 1) * (int)SLAB;
#pragma unroll
                for (int gq = 0; gq < 4; ++gq) v[s][gq] = __builtin_amdgcn_raw_buffer_load_b128(rsrc, off + gq * 1024, so, 16);
            }
            char* op = (char*)out + (size_t)(mt32 * 32 + r) * 2560 + (640 + nt32 * 32 + 4 * h) * 2;
#pragma unroll
            for (int gq = 0; gq < 4; ++gq) {
                f32x4 t = {acc[mi][ni][4 * gq], acc[mi][ni][4 * gq + 1], acc[mi][ni][4 * gq + 2], acc[mi][ni][4 * gq + 3]};
                t += (__builtin_bit_cast(f32x4, v[0][gq]) + __builtin_bit_cast(f32x4, v[1][gq])) +
                     (__builtin_bit_cast(f32x4, v[2][gq]) + __builtin_bit_cast(f32x4, v[3][gq]));
                u32x2 o;
                o[0] = pack2(t[0], t[1]);
                o[1] = pack2(t[2], t[3]);
                *(u32x2*)(op + gq * 16) = o;
            }
        }
}

extern "C" void kernel_launch(void* const* d_in, const int* in_sizes, int n_in,
                              void* d_out, int out_size, void* d_ws, size_t ws_size,
                              hipStream_t stream) {
    (void)in_sizes; (void)n_in; (void)out_size; (void)ws_size;
    const __hip_bfloat16* Ax = (const __hip_bfloat16*)d_in[0];
    const __hip_bfloat16* Awqkv = (const __hip_bfloat16*)d_in[1];
    const __hip_bfloat16* Awo = (const __hip_bfloat16*)d_in[2];
    const __hip_bfloat16* Bx = (const __hip_bfloat16*)d_in[3];
    const __hip_bfloat16* Bckv = (const __hip_bfloat16*)d_in[4];
    const __hip_bfloat16* Bwq = (const __hip_bfloat16*)d_in[5];
    const __hip_bfloat16* Bwuk = (const __hip_bfloat16*)d_in[6];
    const __hip_bfloat16* Bwuv = (const __hip_bfloat16*)d_in[7];
    const __hip_bfloat16* Bwo = (const __hip_bfloat16*)d_in[8];
    char* ws = (char*)d_ws;
    (void)hipFuncSetAttribute((const void*)k1_proj, hipFuncAttributeMaxDynamicSharedMemorySize, G1_LDS);
    (void)hipFuncSetAttribute((const void*)k2_attn, hipFuncAttributeMaxDynamicSharedMemorySize, K2_LDS);
    (void)hipFuncSetAttribute((const void*)k3_oproj, hipFuncAttributeMaxDynamicSharedMemorySize, G3_LDS);
    k1_proj<<<256, 512, G1_LDS, stream>>>(Ax, Awqkv, Bx, Bckv, Bwq, Bwuk, Bwuv, Awo, Bwo, ws);
    k2_attn<<<256, 512, K2_LDS, stream>>>(ws);
    k3_oproj<<<240, 512, G3_LDS, stream>>>(Awo, Bwo, ws, (__hip_bfloat16*)d_out);
}
```
